```python
import torch

def _softmax(x, axis=-1):
    return torch.softmax(x, dim=axis)
def _gelu(x):
    return torch.nn.functional.gelu(x, approximate="tanh")
def _silu(x):
    return torch.nn.functional.silu(x)
def _sigmoid(x):
    return torch.sigmoid(x)
def _layernorm(x, axis=-1, eps=1e-5):
    m = x.mean(dim=axis, keepdim=True)
    v = x.var(dim=axis, keepdim=True, unbiased=False)
    return (x - m) / torch.sqrt(v + eps)
def _scatter_add(tgt, idx, src, axis=0):
    return tgt.clone().index_add_(axis, idx, src)
def _segment_sum(src, seg, n):
    out = torch.zeros((n,) + src.shape[1:], dtype=src.dtype)
    return out.index_add_(0, seg, src)
def _pad(x, pw):
    flat = tuple(v for pair in reversed(pw) for v in pair)
    return torch.nn.functional.pad(x, flat)

kernel_name = 'standalone__mha_softmax__near_ridge'

def setup_inputs():

    _w = (lambda a: (lambda t: t.to(torch.bfloat16) if t.is_floating_point() else t)(torch.as_tensor(a)))
    B, H, Sq, Sk, D = 8, 16, 512, 1024, 256
    return {
        "q": _w((lambda *s: torch.randn(*s, dtype=torch.bfloat16) * 0.8)(B, H, Sq, D)),
        "k": _w((lambda *s: torch.randn(*s, dtype=torch.bfloat16) * 0.8)(B, H, Sk, D)),
        "v": _w((lambda *s: torch.randn(*s, dtype=torch.bfloat16) * 0.05)(B, H, Sk, D)),
    }

def reference(q, k, v):
    scale = q.shape[-1] ** -0.5
    scores = torch.einsum("bhqd,bhkd->bhqk", q, k) * scale
    p = _softmax(scores, axis=-1)
    return torch.einsum("bhqk,bhkd->bhqd", p, v)
```

```cpp
#include <hip/hip_runtime.h>
#include <hip/hip_bf16.h>

#define MHA_STAMP 0

__global__ __launch_bounds__(256, 1)
void mha_fwd_d256(const unsigned short* __restrict__ Q, const unsigned short* __restrict__ K, const unsigned short* __restrict__ V,
                  unsigned short* __restrict__ O, unsigned* __restrict__ dbg) {
  __shared__ __attribute__((aligned(1024))) char lds[163840];
  const unsigned b = blockIdx.x;
  const unsigned w = __builtin_amdgcn_readfirstlane(threadIdx.x >> 6);

  asm volatile(
    "s_load_dwordx8 s[92:99], %[ka], 0x0\n"
    "v_mbcnt_lo_u32_b32 v251, -1, 0\n"
    "v_mbcnt_hi_u32_b32 v251, -1, v251\n"
    "s_getpc_b64 s[62:63]\n"
    "L_pc_%=:\n"
    "s_lshr_b32 s64, %[blk], 3\n"
    "v_and_b32 v254, 3, v251\n"
    "s_lshl2_add_u32 s64, s64, %[w]\n"
    "v_lshlrev_b32 v254, 6, v254\n"
    "s_lshl_b32 s64, s64, 8\n"
    "s_min_u32 s64, s64, L_hotend_%=-L_pc_%=-256\n"
    "v_add_u32 v254, s64, v254\n"
    "global_load_dword v0, v254, s[62:63]\n"
    "s_lshr_b32 s61, %[blk], 4\n"
    "v_lshrrev_b32 v254, 4, v251\n"
    "s_lshl_b32 s61, s61, 3\n"
    "v_lshrrev_b32 v252, 3, v251\n"
    "s_and_b32 s60, %[blk], 7\n"
    "v_xor_b32 v255, v251, v254\n"
    "s_or_b32 s61, s61, s60\n"
    "v_and_b32 v255, 7, v255\n"
    "s_bfe_u32 s60, %[blk], 0x10003\n"
    "v_lshlrev_b32 v255, 4, v255\n"
    "s_lshl1_add_u32 s60, s61, s60\n"
    "v_lshl_add_u32 v255, v252, 9, v255\n"
    "s_lshl_b32 s60, s60, 17\n"
    "v_xor_b32 v250, 64, v255\n"
    "s_lshl_b32 s61, s61, 19\n"
    "s_xor_b32 s36, %[w], 2\n"
    "s_lshl_b32 s36, s36, 14\n"
    "s_add_u32 s36, s36, 0x8000\n"
    "s_lshl_b32 s38, %[w], 15\n"
    "s_waitcnt lgkmcnt(0)\n"
    "s_add_u32 s48, s92, s60\n"
    "s_addc_u32 s49, s93, 0\n"
    "s_and_b32 s49, s49, 0xffff\n"
    "s_mov_b32 s50, 0x20000\n"
    "s_mov_b32 s51, 0x20000\n"
    "s_add_u32 m0, s36, 0x0\n"
    "s_add_u32 s64, s38, 0x0\n"
    "buffer_load_dwordx4 v255, s[48:51], s64 offen offset:0 nt lds\n"
    "s_add_u32 m0, s36, 0x400\n"
    "s_add_u32 s64, s38, 0x1000\n"
    "buffer_load_dwordx4 v250, s[48:51], s64 offen offset:0 nt lds\n"
    "s_add_u32 m0, s36, 0x800\n"
    "s_add_u32 s64, s38, 0x2000\n"
    "buffer_load_dwordx4 v255, s[48:51], s64 offen offset:0 nt lds\n"
    "s_add_u32 m0, s36, 0xc00\n"
    "s_add_u32 s64, s38, 0x3000\n"
    "buffer_load_dwordx4 v250, s[48:51], s64 offen offset:0 nt lds\n"
    "s_lshl_b32 s37, %[w], 14\n"
    "s_add_u32 s37, s37, 0x18000\n"
    "s_add_u32 s40, s94, s61\n"
    "s_addc_u32 s41, s95, 0\n"
    "s_and_b32 s41, s41, 0xffff\n"
    "s_mov_b32 s42, 0x80000\n"
    "s_mov_b32 s43, 0x20000\n"
    "s_lshl_b32 s58, %[w], 10\n"
    "s_lshl_b32 s59, %[w], 12\n"
    "s_lshl_b32 s65, %[w], 2\n"
    "v_add_u32 v244, s65, v254\n"
    "v_xor_b32 v244, v251, v244\n"
    "v_and_b32 v244, 7, v244\n"
    "v_lshlrev_b32 v244, 4, v244\n"
    "v_lshl_add_u32 v244, v252, 9, v244\n"
    "v_add_u32 v244, s59, v244\n"
    "s_mov_b32 s56, 0x0\n"
    "s_add_u32 m0, s58, 0x0\n"
    "s_nop 0\n"
    "buffer_load_dwordx4 v244, s[40:43], s56 offen offset:0 nt lds\n"
    "s_add_u32 m0, s58, 0xf80\n"
    "s_nop 0\n"
    "buffer_load_dwordx4 v244, s[40:43], s56 offen offset:128 nt lds\n"
    "s_add_u32 m0, s58, 0x1f00\n"
    "s_nop 0\n"
    "buffer_load_dwordx4 v244, s[40:43], s56 offen offset:256 nt lds\n"
    "s_add_u32 m0, s58, 0x2e80\n"
    "s_nop 0\n"
    "buffer_load_dwordx4 v244, s[40:43], s56 offen offset:384 nt lds\n"
    "s_add_u32 m0, s36, 0xf80\n"
    "s_add_u32 s64, s38, 0x0\n"
    "buffer_load_dwordx4 v255, s[48:51], s64 offen offset:128 nt lds\n"
    "s_add_u32 m0, s36, 0x1380\n"
    "s_add_u32 s64, s38, 0x1000\n"
    "buffer_load_dwordx4 v250, s[48:51], s64 offen offset:128 nt lds\n"
    "s_add_u32 m0, s36, 0x1780\n"
    "s_add_u32 s64, s38, 0x2000\n"
    "buffer_load_dwordx4 v255, s[48:51], s64 offen offset:128 nt lds\n"
    "s_add_u32 m0, s36, 0x1b80\n"
    "s_add_u32 s64, s38, 0x3000\n"
    "buffer_load_dwordx4 v250, s[48:51], s64 offen offset:128 nt lds\n"
    "s_add_u32 m0, s36, 0x1f00\n"
    "s_add_u32 s64, s38, 0x0\n"
    "buffer_load_dwordx4 v255, s[48:51], s64 offen offset:256 nt lds\n"
    "s_add_u32 m0, s36, 0x2300\n"
    "s_add_u32 s64, s38, 0x1000\n"
    "buffer_load_dwordx4 v250, s[48:51], s64 offen offset:256 nt lds\n"
    "s_add_u32 m0, s36, 0x2700\n"
    "s_add_u32 s64, s38, 0x2000\n"
    "buffer_load_dwordx4 v255, s[48:51], s64 offen offset:256 nt lds\n"
    "s_add_u32 m0, s36, 0x2b00\n"
    "s_add_u32 s64, s38, 0x3000\n"
    "buffer_load_dwordx4 v250, s[48:51], s64 offen offset:256 nt lds\n"
    "s_add_u32 m0, s36, 0x2e80\n"
    "s_add_u32 s64, s38, 0x0\n"
    "buffer_load_dwordx4 v255, s[48:51], s64 offen offset:384 nt lds\n"
    "s_add_u32 m0, s36, 0x3280\n"
    "s_add_u32 s64, s38, 0x1000\n"
    "buffer_load_dwordx4 v250, s[48:51], s64 offen offset:384 nt lds\n"
    "s_add_u32 m0, s36, 0x3680\n"
    "s_add_u32 s64, s38, 0x2000\n"
    "buffer_load_dwordx4 v255, s[48:51], s64 offen offset:384 nt lds\n"
    "s_add_u32 m0, s36, 0x3a80\n"
    "s_add_u32 s64, s38, 0x3000\n"
    "buffer_load_dwordx4 v250, s[48:51], s64 offen offset:384 nt lds\n"
    "s_add_u32 s44, s96, s61\n"
    "s_addc_u32 s45, s97, 0\n"
    "s_and_b32 s45, s45, 0xffff\n"
    "s_mov_b32 s46, 0x80000\n"
    "s_mov_b32 s47, 0x20000\n"
    "s_add_u32 s52, s98, s60\n"
    "s_addc_u32 s53, s99, 0\n"
    "s_and_b32 s53, s53, 0xffff\n"
    "s_mov_b32 s54, 0x20000\n"
    "s_mov_b32 s55, 0x20000\n"
    "v_lshrrev_b32 v254, 2, v251\n"
    "v_and_b32 v254, 7, v254\n"
    "v_lshlrev_b32 v245, 9, v254\n"
    "v_lshrrev_b32 v254, 1, v254\n"
    "v_and_b32 v254, 2, v254\n"
    "v_xor_b32 v254, v251, v254\n"
    "v_and_b32 v254, 3, v254\n"
    "v_lshl_add_u32 v245, v254, 4, v245\n"
    "v_lshrrev_b32 v254, 5, v251\n"
    "v_lshl_add_u32 v245, v254, 6, v245\n"
    "v_add_u32 v245, s59, v245\n"
    "s_add_u32 s59, s59, 0x10000\n"
    "s_mov_b32 s66, 0x3db8aa3b\n"
    "v_and_b32 v252, 15, v251\n"
    "v_lshrrev_b32 v253, 4, v251\n"
    "v_lshrrev_b32 v254, 1, v253\n"
    "v_lshlrev_b32 v242, 12, v254\n"
    "v_and_b32 v254, 1, v253\n"
    "v_mul_u32_u24 v254, 0x120, v254\n"
    "v_add_u32 v242, v254, v242\n"
    "v_lshrrev_b32 v254, 2, v252\n"
    "v_lshl_add_u32 v242, v254, 6, v242\n"
    "v_and_b32 v254, 3, v252\n"
    "v_lshl_add_u32 v242, v254, 3, v242\n"
    "v_add_u32 v242, 0x10000, v242\n"
    "v_xor_b32 v243, 32, v242\n"
    "v_lshrrev_b32 v254, 1, v252\n"
    "v_xor_b32 v254, v253, v254\n"
    "v_lshlrev_b32 v254, 4, v254\n"
    "v_lshl_add_u32 v196, v252, 7, v254\n"
    "v_xor_b32 v197, 64, v196\n"
    "v_mov_b32 v246, 0\n"
    "v_mov_b32 v247, 0\n"
    "v_mov_b32 v248, 0\n"
    "v_mov_b32 v249, 0\n"
    "v_mov_b32 v236, 0\n"
    "v_mov_b32 v237, 0\n"
    "v_mov_b32 v238, 0\n"
    "v_mov_b32 v239, 0\n"
    "s_nop 1\n"
    "v_mfma_f32_32x32x16_bf16 a[0:15], v[236:239], v[236:239], 0\n"
    "v_mfma_f32_32x32x16_bf16 a[16:31], v[236:239], v[236:239], 0\n"
    "v_mfma_f32_32x32x16_bf16 a[32:47], v[236:239], v[236:239], 0\n"
    "v_mfma_f32_32x32x16_bf16 a[48:63], v[236:239], v[236:239], 0\n"
    "v_mfma_f32_32x32x16_bf16 a[64:79], v[236:239], v[236:239], 0\n"
    "v_mfma_f32_32x32x16_bf16 a[80:95], v[236:239], v[236:239], 0\n"
    "v_mfma_f32_32x32x16_bf16 a[96:111], v[236:239], v[236:239], 0\n"
    "v_mfma_f32_32x32x16_bf16 a[112:127], v[236:239], v[236:239], 0\n"
    "v_mfma_f32_32x32x16_bf16 a[128:143], v[236:239], v[236:239], 0\n"
    "v_mfma_f32_32x32x16_bf16 a[144:159], v[236:239], v[236:239], 0\n"
    "v_mfma_f32_32x32x16_bf16 a[160:175], v[236:239], v[236:239], 0\n"
    "v_mfma_f32_32x32x16_bf16 a[176:191], v[236:239], v[236:239], 0\n"
    "v_mfma_f32_32x32x16_bf16 a[192:207], v[236:239], v[236:239], 0\n"
    "v_mfma_f32_32x32x16_bf16 a[208:223], v[236:239], v[236:239], 0\n"
    "v_mfma_f32_32x32x16_bf16 a[224:239], v[236:239], v[236:239], 0\n"
    "v_mfma_f32_32x32x16_bf16 a[240:255], v[236:239], v[236:239], 0\n"
    "s_mov_b32 s67, 0x3db8aa3b\n"
    "v_add_u32 v192, s36, v196\n"
    "v_add_u32 v193, s36, v197\n"
    "s_add_u32 m0, s37, 0x0\n"
    "s_add_u32 s64, s38, 0x4000\n"
    "buffer_load_dwordx4 v255, s[48:51], s64 offen offset:0 nt lds\n"
    "s_add_u32 m0, s37, 0x400\n"
    "s_add_u32 s64, s38, 0x5000\n"
    "buffer_load_dwordx4 v250, s[48:51], s64 offen offset:0 nt lds\n"
    "s_add_u32 m0, s37, 0x800\n"
    "s_add_u32 s64, s38, 0x6000\n"
    "buffer_load_dwordx4 v255, s[48:51], s64 offen offset:0 nt lds\n"
    "s_add_u32 m0, s37, 0xc00\n"
    "s_add_u32 s64, s38, 0x7000\n"
    "buffer_load_dwordx4 v250, s[48:51], s64 offen offset:0 nt lds\n"
    "s_waitcnt vmcnt(20)\n"
    "ds_read_b128 v[0:3], v192 offset:0\n"
    "ds_read_b128 v[32:35], v192 offset:2048\n"
    "ds_read_b128 v[4:7], v193 offset:0\n"
    "ds_read_b128 v[36:39], v193 offset:2048\n"
    "s_waitcnt lgkmcnt(3)\n"
    "v_lshlrev_b32 v160, 16, v0\n"
    "v_and_b32 v161, 0xffff0000, v0\n"
    "v_lshlrev_b32 v162, 16, v1\n"
    "v_and_b32 v163, 0xffff0000, v1\n"
    "v_lshlrev_b32 v164, 16, v2\n"
    "v_and_b32 v165, 0xffff0000, v2\n"
    "v_lshlrev_b32 v166, 16, v3\n"
    "v_and_b32 v167, 0xffff0000, v3\n"
    "v_pk_mul_f32 v[160:161], v[160:161], s[66:67]\n"
    "v_pk_mul_f32 v[162:163], v[162:163], s[66:67]\n"
    "v_pk_mul_f32 v[164:165], v[164:165], s[66:67]\n"
    "v_pk_mul_f32 v[166:167], v[166:167], s[66:67]\n"
    "v_cvt_pk_bf16_f32 v0, v160, v161\n"
    "v_cvt_pk_bf16_f32 v1, v162, v163\n"
    "v_cvt_pk_bf16_f32 v2, v164, v165\n"
    "v_cvt_pk_bf16_f32 v3, v166, v167\n"
    "s_waitcnt lgkmcnt(2)\n"
    "v_lshlrev_b32 v168, 16, v32\n"
    "v_and_b32 v169, 0xffff0000, v32\n"
    "v_lshlrev_b32 v170, 16, v33\n"
    "v_and_b32 v171, 0xffff0000, v33\n"
    "v_lshlrev_b32 v172, 16, v34\n"
    "v_and_b32 v173, 0xffff0000, v34\n"
    "v_lshlrev_b32 v174, 16, v35\n"
    "v_and_b32 v175, 0xffff0000, v35\n"
    "v_pk_mul_f32 v[168:169], v[168:169], s[66:67]\n"
    "v_pk_mul_f32 v[170:171], v[170:171], s[66:67]\n"
    "v_pk_mul_f32 v[172:173], v[172:173], s[66:67]\n"
    "v_pk_mul_f32 v[174:175], v[174:175], s[66:67]\n"
    "v_cvt_pk_bf16_f32 v32, v168, v169\n"
    "v_cvt_pk_bf16_f32 v33, v170, v171\n"
    "v_cvt_pk_bf16_f32 v34, v172, v173\n"
    "v_cvt_pk_bf16_f32 v35, v174, v175\n"
    "s_waitcnt lgkmcnt(1)\n"
    "v_lshlrev_b32 v160, 16, v4\n"
    "v_and_b32 v161, 0xffff0000, v4\n"
    "v_lshlrev_b32 v162, 16, v5\n"
    "v_and_b32 v163, 0xffff0000, v5\n"
    "v_lshlrev_b32 v164, 16, v6\n"
    "v_and_b32 v165, 0xffff0000, v6\n"
    "v_lshlrev_b32 v166, 16, v7\n"
    "v_and_b32 v167, 0xffff0000, v7\n"
    "v_pk_mul_f32 v[160:161], v[160:161], s[66:67]\n"
    "v_pk_mul_f32 v[162:163], v[162:163], s[66:67]\n"
    "v_pk_mul_f32 v[164:165], v[164:165], s[66:67]\n"
    "v_pk_mul_f32 v[166:167], v[166:167], s[66:67]\n"
    "v_cvt_pk_bf16_f32 v4, v160, v161\n"
    "v_cvt_pk_bf16_f32 v5, v162, v163\n"
    "v_cvt_pk_bf16_f32 v6, v164, v165\n"
    "v_cvt_pk_bf16_f32 v7, v166, v167\n"
    "s_waitcnt lgkmcnt(0)\n"
    "v_lshlrev_b32 v168, 16, v36\n"
    "v_and_b32 v169, 0xffff0000, v36\n"
    "v_lshlrev_b32 v170, 16, v37\n"
    "v_and_b32 v171, 0xffff0000, v37\n"
    "v_lshlrev_b32 v172, 16, v38\n"
    "v_and_b32 v173, 0xffff0000, v38\n"
    "v_lshlrev_b32 v174, 16, v39\n"
    "v_and_b32 v175, 0xffff0000, v39\n"
    "v_pk_mul_f32 v[168:169], v[168:169], s[66:67]\n"
    "v_pk_mul_f32 v[170:171], v[170:171], s[66:67]\n"
    "v_pk_mul_f32 v[172:173], v[172:173], s[66:67]\n"
    "v_pk_mul_f32 v[174:175], v[174:175], s[66:67]\n"
    "v_cvt_pk_bf16_f32 v36, v168, v169\n"
    "v_cvt_pk_bf16_f32 v37, v170, v171\n"
    "v_cvt_pk_bf16_f32 v38, v172, v173\n"
    "v_cvt_pk_bf16_f32 v39, v174, v175\n"
    "s_add_u32 m0, s37, 0xf80\n"
    "s_add_u32 s64, s38, 0x4000\n"
    "buffer_load_dwordx4 v255, s[48:51], s64 offen offset:128 nt lds\n"
    "s_add_u32 m0, s37, 0x1380\n"
    "s_add_u32 s64, s38, 0x5000\n"
    "buffer_load_dwordx4 v250, s[48:51], s64 offen offset:128 nt lds\n"
    "s_add_u32 m0, s37, 0x1780\n"
    "s_add_u32 s64, s38, 0x6000\n"
    "buffer_load_dwordx4 v255, s[48:51], s64 offen offset:128 nt lds\n"
    "s_add_u32 m0, s37, 0x1b80\n"
    "s_add_u32 s64, s38, 0x7000\n"
    "buffer_load_dwordx4 v250, s[48:51], s64 offen offset:128 nt lds\n"
    "s_waitcnt vmcnt(16)\n"
    "ds_read_b128 v[8:11], v192 offset:4096\n"
    "ds_read_b128 v[40:43], v192 offset:6144\n"
    "ds_read_b128 v[12:15], v193 offset:4096\n"
    "ds_read_b128 v[44:47], v193 offset:6144\n"
    "s_waitcnt lgkmcnt(3)\n"
    "v_lshlrev_b32 v160, 16, v8\n"
    "v_and_b32 v161, 0xffff0000, v8\n"
    "v_lshlrev_b32 v162, 16, v9\n"
    "v_and_b32 v163, 0xffff0000, v9\n"
    "v_lshlrev_b32 v164, 16, v10\n"
    "v_and_b32 v165, 0xffff0000, v10\n"
    "v_lshlrev_b32 v166, 16, v11\n"
    "v_and_b32 v167, 0xffff0000, v11\n"
    "v_pk_mul_f32 v[160:161], v[160:161], s[66:67]\n"
    "v_pk_mul_f32 v[162:163], v[162:163], s[66:67]\n"
    "v_pk_mul_f32 v[164:165], v[164:165], s[66:67]\n"
    "v_pk_mul_f32 v[166:167], v[166:167], s[66:67]\n"
    "v_cvt_pk_bf16_f32 v8, v160, v161\n"
    "v_cvt_pk_bf16_f32 v9, v162, v163\n"
    "v_cvt_pk_bf16_f32 v10, v164, v165\n"
    "v_cvt_pk_bf16_f32 v11, v166, v167\n"
    "s_waitcnt lgkmcnt(2)\n"
    "v_lshlrev_b32 v168, 16, v40\n"
    "v_and_b32 v169, 0xffff0000, v40\n"
    "v_lshlrev_b32 v170, 16, v41\n"
    "v_and_b32 v171, 0xffff0000, v41\n"
    "v_lshlrev_b32 v172, 16, v42\n"
    "v_and_b32 v173, 0xffff0000, v42\n"
    "v_lshlrev_b32 v174, 16, v43\n"
    "v_and_b32 v175, 0xffff0000, v43\n"
    "v_pk_mul_f32 v[168:169], v[168:169], s[66:67]\n"
    "v_pk_mul_f32 v[170:171], v[170:171], s[66:67]\n"
    "v_pk_mul_f32 v[172:173], v[172:173], s[66:67]\n"
    "v_pk_mul_f32 v[174:175], v[174:175], s[66:67]\n"
    "v_cvt_pk_bf16_f32 v40, v168, v169\n"
    "v_cvt_pk_bf16_f32 v41, v170, v171\n"
    "v_cvt_pk_bf16_f32 v42, v172, v173\n"
    "v_cvt_pk_bf16_f32 v43, v174, v175\n"
    "s_waitcnt lgkmcnt(1)\n"
    "v_lshlrev_b32 v160, 16, v12\n"
    "v_and_b32 v161, 0xffff0000, v12\n"
    "v_lshlrev_b32 v162, 16, v13\n"
    "v_and_b32 v163, 0xffff0000, v13\n"
    "v_lshlrev_b32 v164, 16, v14\n"
    "v_and_b32 v165, 0xffff0000, v14\n"
    "v_lshlrev_b32 v166, 16, v15\n"
    "v_and_b32 v167, 0xffff0000, v15\n"
    "v_pk_mul_f32 v[160:161], v[160:161], s[66:67]\n"
    "v_pk_mul_f32 v[162:163], v[162:163], s[66:67]\n"
    "v_pk_mul_f32 v[164:165], v[164:165], s[66:67]\n"
    "v_pk_mul_f32 v[166:167], v[166:167], s[66:67]\n"
    "v_cvt_pk_bf16_f32 v12, v160, v161\n"
    "v_cvt_pk_bf16_f32 v13, v162, v163\n"
    "v_cvt_pk_bf16_f32 v14, v164, v165\n"
    "v_cvt_pk_bf16_f32 v15, v166, v167\n"
    "s_waitcnt lgkmcnt(0)\n"
    "v_lshlrev_b32 v168, 16, v44\n"
    "v_and_b32 v169, 0xffff0000, v44\n"
    "v_lshlrev_b32 v170, 16, v45\n"
    "v_and_b32 v171, 0xffff0000, v45\n"
    "v_lshlrev_b32 v172, 16, v46\n"
    "v_and_b32 v173, 0xffff0000, v46\n"
    "v_lshlrev_b32 v174, 16, v47\n"
    "v_and_b32 v175, 0xffff0000, v47\n"
    "v_pk_mul_f32 v[168:169], v[168:169], s[66:67]\n"
    "v_pk_mul_f32 v[170:171], v[170:171], s[66:67]\n"
    "v_pk_mul_f32 v[172:173], v[172:173], s[66:67]\n"
    "v_pk_mul_f32 v[174:175], v[174:175], s[66:67]\n"
    "v_cvt_pk_bf16_f32 v44, v168, v169\n"
    "v_cvt_pk_bf16_f32 v45, v170, v171\n"
    "v_cvt_pk_bf16_f32 v46, v172, v173\n"
    "v_cvt_pk_bf16_f32 v47, v174, v175\n"
    "s_add_u32 m0, s37, 0x1f00\n"
    "s_add_u32 s64, s38, 0x4000\n"
    "buffer_load_dwordx4 v255, s[48:51], s64 offen offset:256 nt lds\n"
    "s_add_u32 m0, s37, 0x2300\n"
    "s_add_u32 s64, s38, 0x5000\n"
    "buffer_load_dwordx4 v250, s[48:51], s64 offen offset:256 nt lds\n"
    "s_add_u32 m0, s37, 0x2700\n"
    "s_add_u32 s64, s38, 0x6000\n"
    "buffer_load_dwordx4 v255, s[48:51], s64 offen offset:256 nt lds\n"
    "s_add_u32 m0, s37, 0x2b00\n"
    "s_add_u32 s64, s38, 0x7000\n"
    "buffer_load_dwordx4 v250, s[48:51], s64 offen offset:256 nt lds\n"
    "s_waitcnt vmcnt(16)\n"
    "ds_read_b128 v[16:19], v192 offset:8192\n"
    "ds_read_b128 v[48:51], v192 offset:10240\n"
    "ds_read_b128 v[20:23], v193 offset:8192\n"
    "ds_read_b128 v[52:55], v193 offset:10240\n"
    "s_waitcnt lgkmcnt(3)\n"
    "v_lshlrev_b32 v160, 16, v16\n"
    "v_and_b32 v161, 0xffff0000, v16\n"
    "v_lshlrev_b32 v162, 16, v17\n"
    "v_and_b32 v163, 0xffff0000, v17\n"
    "v_lshlrev_b32 v164, 16, v18\n"
    "v_and_b32 v165, 0xffff0000, v18\n"
    "v_lshlrev_b32 v166, 16, v19\n"
    "v_and_b32 v167, 0xffff0000, v19\n"
    "v_pk_mul_f32 v[160:161], v[160:161], s[66:67]\n"
    "v_pk_mul_f32 v[162:163], v[162:163], s[66:67]\n"
    "v_pk_mul_f32 v[164:165], v[164:165], s[66:67]\n"
    "v_pk_mul_f32 v[166:167], v[166:167], s[66:67]\n"
    "v_cvt_pk_bf16_f32 v16, v160, v161\n"
    "v_cvt_pk_bf16_f32 v17, v162, v163\n"
    "v_cvt_pk_bf16_f32 v18, v164, v165\n"
    "v_cvt_pk_bf16_f32 v19, v166, v167\n"
    "s_waitcnt lgkmcnt(2)\n"
    "v_lshlrev_b32 v168, 16, v48\n"
    "v_and_b32 v169, 0xffff0000, v48\n"
    "v_lshlrev_b32 v170, 16, v49\n"
    "v_and_b32 v171, 0xffff0000, v49\n"
    "v_lshlrev_b32 v172, 16, v50\n"
    "v_and_b32 v173, 0xffff0000, v50\n"
    "v_lshlrev_b32 v174, 16, v51\n"
    "v_and_b32 v175, 0xffff0000, v51\n"
    "v_pk_mul_f32 v[168:169], v[168:169], s[66:67]\n"
    "v_pk_mul_f32 v[170:171], v[170:171], s[66:67]\n"
    "v_pk_mul_f32 v[172:173], v[172:173], s[66:67]\n"
    "v_pk_mul_f32 v[174:175], v[174:175], s[66:67]\n"
    "v_cvt_pk_bf16_f32 v48, v168, v169\n"
    "v_cvt_pk_bf16_f32 v49, v170, v171\n"
    "v_cvt_pk_bf16_f32 v50, v172, v173\n"
    "v_cvt_pk_bf16_f32 v51, v174, v175\n"
    "s_waitcnt lgkmcnt(1)\n"
    "v_lshlrev_b32 v160, 16, v20\n"
    "v_and_b32 v161, 0xffff0000, v20\n"
    "v_lshlrev_b32 v162, 16, v21\n"
    "v_and_b32 v163, 0xffff0000, v21\n"
    "v_lshlrev_b32 v164, 16, v22\n"
    "v_and_b32 v165, 0xffff0000, v22\n"
    "v_lshlrev_b32 v166, 16, v23\n"
    "v_and_b32 v167, 0xffff0000, v23\n"
    "v_pk_mul_f32 v[160:161], v[160:161], s[66:67]\n"
    "v_pk_mul_f32 v[162:163], v[162:163], s[66:67]\n"
    "v_pk_mul_f32 v[164:165], v[164:165], s[66:67]\n"
    "v_pk_mul_f32 v[166:167], v[166:167], s[66:67]\n"
    "v_cvt_pk_bf16_f32 v20, v160, v161\n"
    "v_cvt_pk_bf16_f32 v21, v162, v163\n"
    "v_cvt_pk_bf16_f32 v22, v164, v165\n"
    "v_cvt_pk_bf16_f32 v23, v166, v167\n"
    "s_waitcnt lgkmcnt(0)\n"
    "v_lshlrev_b32 v168, 16, v52\n"
    "v_and_b32 v169, 0xffff0000, v52\n"
    "v_lshlrev_b32 v170, 16, v53\n"
    "v_and_b32 v171, 0xffff0000, v53\n"
    "v_lshlrev_b32 v172, 16, v54\n"
    "v_and_b32 v173, 0xffff0000, v54\n"
    "v_lshlrev_b32 v174, 16, v55\n"
    "v_and_b32 v175, 0xffff0000, v55\n"
    "v_pk_mul_f32 v[168:169], v[168:169], s[66:67]\n"
    "v_pk_mul_f32 v[170:171], v[170:171], s[66:67]\n"
    "v_pk_mul_f32 v[172:173], v[172:173], s[66:67]\n"
    "v_pk_mul_f32 v[174:175], v[174:175], s[66:67]\n"
    "v_cvt_pk_bf16_f32 v52, v168, v169\n"
    "v_cvt_pk_bf16_f32 v53, v170, v171\n"
    "v_cvt_pk_bf16_f32 v54, v172, v173\n"
    "v_cvt_pk_bf16_f32 v55, v174, v175\n"
    "s_add_u32 m0, s37, 0x2e80\n"
    "s_add_u32 s64, s38, 0x4000\n"
    "buffer_load_dwordx4 v255, s[48:51], s64 offen offset:384 nt lds\n"
    "s_add_u32 m0, s37, 0x3280\n"
    "s_add_u32 s64, s38, 0x5000\n"
    "buffer_load_dwordx4 v250, s[48:51], s64 offen offset:384 nt lds\n"
    "s_add_u32 m0, s37, 0x3680\n"
    "s_add_u32 s64, s38, 0x6000\n"
    "buffer_load_dwordx4 v255, s[48:51], s64 offen offset:384 nt lds\n"
    "s_add_u32 m0, s37, 0x3a80\n"
    "s_add_u32 s64, s38, 0x7000\n"
    "buffer_load_dwordx4 v250, s[48:51], s64 offen offset:384 nt lds\n"
    "s_waitcnt vmcnt(16)\n"
    "ds_read_b128 v[24:27], v192 offset:12288\n"
    "ds_read_b128 v[56:59], v192 offset:14336\n"
    "ds_read_b128 v[28:31], v193 offset:12288\n"
    "ds_read_b128 v[60:63], v193 offset:14336\n"
    "s_waitcnt lgkmcnt(3)\n"
    "v_lshlrev_b32 v160, 16, v24\n"
    "v_and_b32 v161, 0xffff0000, v24\n"
    "v_lshlrev_b32 v162, 16, v25\n"
    "v_and_b32 v163, 0xffff0000, v25\n"
    "v_lshlrev_b32 v164, 16, v26\n"
    "v_and_b32 v165, 0xffff0000, v26\n"
    "v_lshlrev_b32 v166, 16, v27\n"
    "v_and_b32 v167, 0xffff0000, v27\n"
    "v_pk_mul_f32 v[160:161], v[160:161], s[66:67]\n"
    "v_pk_mul_f32 v[162:163], v[162:163], s[66:67]\n"
    "v_pk_mul_f32 v[164:165], v[164:165], s[66:67]\n"
    "v_pk_mul_f32 v[166:167], v[166:167], s[66:67]\n"
    "v_cvt_pk_bf16_f32 v24, v160, v161\n"
    "v_cvt_pk_bf16_f32 v25, v162, v163\n"
    "v_cvt_pk_bf16_f32 v26, v164, v165\n"
    "v_cvt_pk_bf16_f32 v27, v166, v167\n"
    "s_waitcnt lgkmcnt(2)\n"
    "v_lshlrev_b32 v168, 16, v56\n"
    "v_and_b32 v169, 0xffff0000, v56\n"
    "v_lshlrev_b32 v170, 16, v57\n"
    "v_and_b32 v171, 0xffff0000, v57\n"
    "v_lshlrev_b32 v172, 16, v58\n"
    "v_and_b32 v173, 0xffff0000, v58\n"
    "v_lshlrev_b32 v174, 16, v59\n"
    "v_and_b32 v175, 0xffff0000, v59\n"
    "v_pk_mul_f32 v[168:169], v[168:169], s[66:67]\n"
    "v_pk_mul_f32 v[170:171], v[170:171], s[66:67]\n"
    "v_pk_mul_f32 v[172:173], v[172:173], s[66:67]\n"
    "v_pk_mul_f32 v[174:175], v[174:175], s[66:67]\n"
    "v_cvt_pk_bf16_f32 v56, v168, v169\n"
    "v_cvt_pk_bf16_f32 v57, v170, v171\n"
    "v_cvt_pk_bf16_f32 v58, v172, v173\n"
    "v_cvt_pk_bf16_f32 v59, v174, v175\n"
    "s_waitcnt lgkmcnt(1)\n"
    "v_lshlrev_b32 v160, 16, v28\n"
    "v_and_b32 v161, 0xffff0000, v28\n"
    "v_lshlrev_b32 v162, 16, v29\n"
    "v_and_b32 v163, 0xffff0000, v29\n"
    "v_lshlrev_b32 v164, 16, v30\n"
    "v_and_b32 v165, 0xffff0000, v30\n"
    "v_lshlrev_b32 v166, 16, v31\n"
    "v_and_b32 v167, 0xffff0000, v31\n"
    "v_pk_mul_f32 v[160:161], v[160:161], s[66:67]\n"
    "v_pk_mul_f32 v[162:163], v[162:163], s[66:67]\n"
    "v_pk_mul_f32 v[164:165], v[164:165], s[66:67]\n"
    "v_pk_mul_f32 v[166:167], v[166:167], s[66:67]\n"
    "v_cvt_pk_bf16_f32 v28, v160, v161\n"
    "v_cvt_pk_bf16_f32 v29, v162, v163\n"
    "v_cvt_pk_bf16_f32 v30, v164, v165\n"
    "v_cvt_pk_bf16_f32 v31, v166, v167\n"
    "s_waitcnt lgkmcnt(0)\n"
    "v_lshlrev_b32 v168, 16, v60\n"
    "v_and_b32 v169, 0xffff0000, v60\n"
    "v_lshlrev_b32 v170, 16, v61\n"
    "v_and_b32 v171, 0xffff0000, v61\n"
    "v_lshlrev_b32 v172, 16, v62\n"
    "v_and_b32 v173, 0xffff0000, v62\n"
    "v_lshlrev_b32 v174, 16, v63\n"
    "v_and_b32 v175, 0xffff0000, v63\n"
    "v_pk_mul_f32 v[168:169], v[168:169], s[66:67]\n"
    "v_pk_mul_f32 v[170:171], v[170:171], s[66:67]\n"
    "v_pk_mul_f32 v[172:173], v[172:173], s[66:67]\n"
    "v_pk_mul_f32 v[174:175], v[174:175], s[66:67]\n"
    "v_cvt_pk_bf16_f32 v60, v168, v169\n"
    "v_cvt_pk_bf16_f32 v61, v170, v171\n"
    "v_cvt_pk_bf16_f32 v62, v172, v173\n"
    "v_cvt_pk_bf16_f32 v63, v174, v175\n"
    "v_mov_b32 v240, v196\n"
    "v_mov_b32 v241, v197\n"
    "v_add_u32 v192, s37, v196\n"
    "v_add_u32 v193, s37, v197\n"
    "s_waitcnt vmcnt(16)\n"
    "v_mbcnt_lo_u32_b32 v254, -1, 0\n"
    "v_mbcnt_hi_u32_b32 v254, -1, v254\n"
    "v_and_b32 v255, 15, v254\n"
    "v_lshrrev_b32 v250, 4, v254\n"
    "s_lshl_b32 s65, %[w], 13\n"
    "s_add_u32 s65, s65, 0x20000\n"
    "v_lshrrev_b32 v251, 1, v250\n"
    "v_xor_b32 v251, v255, v251\n"
    "v_and_b32 v251, 7, v251\n"
    "v_lshlrev_b32 v251, 4, v251\n"
    "v_lshl_add_u32 v251, v255, 7, v251\n"
    "v_and_b32 v250, 1, v250\n"
    "v_lshl_add_u32 v251, v250, 3, v251\n"
    "v_add_u32 v251, s65, v251\n"
    "v_lshrrev_b32 v250, 3, v254\n"
    "v_xor_b32 v255, v254, v250\n"
    "v_and_b32 v255, 7, v255\n"
    "v_lshlrev_b32 v255, 4, v255\n"
    "v_lshl_add_u32 v255, v250, 7, v255\n"
    "v_add_u32 v252, s65, v255\n"
    "v_and_b32 v255, 7, v254\n"
    "v_lshlrev_b32 v255, 4, v255\n"
    "v_lshl_add_u32 v253, v250, 9, v255\n"
    "s_lshl_b32 s67, %[w], 15\n"
    "s_add_u32 s48, s67, 0x1000\n"
    "s_add_u32 s49, s67, 0x2000\n"
    "s_add_u32 s50, s67, 0x3000\n"
    "s_add_u32 s51, s67, 0x4000\n"
    "s_add_u32 s36, s67, 0x5000\n"
    "s_add_u32 s37, s67, 0x6000\n"
    "s_add_u32 s38, s67, 0x7000\n"
    "s_barrier\n"
    "ds_read_b128 v[208:211], v240 offset:0\n"
    "ds_read_b128 v[212:215], v240 offset:2048\n"
    "ds_read_b128 v[216:219], v241 offset:0\n"
    "ds_read_b128 v[220:223], v241 offset:2048\n"
    "ds_read_b128 v[224:227], v240 offset:4096\n"
    "ds_read_b128 v[228:231], v240 offset:6144\n"
    "ds_read_b128 v[232:235], v241 offset:4096\n"
    "ds_read_b128 v[236:239], v241 offset:6144\n"
    "s_waitcnt lgkmcnt(4)\n"
    "v_mfma_f32_16x16x32_bf16 v[128:131], v[208:211], v[0:3], 0\n"
    "v_mfma_f32_16x16x32_bf16 v[136:139], v[208:211], v[32:35], 0\n"
    "v_mfma_f32_16x16x32_bf16 v[132:135], v[212:215], v[0:3], 0\n"
    "v_mfma_f32_16x16x32_bf16 v[140:143], v[212:215], v[32:35], 0\n"
    "v_mfma_f32_16x16x32_bf16 v[128:131], v[216:219], v[4:7], v[128:131]\n"
    "v_mfma_f32_16x16x32_bf16 v[136:139], v[216:219], v[36:39], v[136:139]\n"
    "v_mfma_f32_16x16x32_bf16 v[132:135], v[220:223], v[4:7], v[132:135]\n"
    "v_mfma_f32_16x16x32_bf16 v[140:143], v[220:223], v[36:39], v[140:143]\n"
    "ds_read_b128 v[208:211], v240 offset:8192\n"
    "ds_read_b128 v[212:215], v240 offset:10240\n"
    "ds_read_b128 v[216:219], v241 offset:8192\n"
    "ds_read_b128 v[220:223], v241 offset:10240\n"
    "s_waitcnt lgkmcnt(4)\n"
    "v_mfma_f32_16x16x32_bf16 v[128:131], v[224:227], v[8:11], v[128:131]\n"
    "v_mfma_f32_16x16x32_bf16 v[136:139], v[224:227], v[40:43], v[136:139]\n"
    "v_mfma_f32_16x16x32_bf16 v[132:135], v[228:231], v[8:11], v[132:135]\n"
    "v_mfma_f32_16x16x32_bf16 v[140:143], v[228:231], v[40:43], v[140:143]\n"
    "v_mfma_f32_16x16x32_bf16 v[128:131], v[232:235], v[12:15], v[128:131]\n"
    "v_mfma_f32_16x16x32_bf16 v[136:139], v[232:235], v[44:47], v[136:139]\n"
    "v_mfma_f32_16x16x32_bf16 v[132:135], v[236:239], v[12:15], v[132:135]\n"
    "v_mfma_f32_16x16x32_bf16 v[140:143], v[236:239], v[44:47], v[140:143]\n"
    "ds_read_b128 v[224:227], v240 offset:12288\n"
    "ds_read_b128 v[228:231], v240 offset:14336\n"
    "ds_read_b128 v[232:235], v241 offset:12288\n"
    "ds_read_b128 v[236:239], v241 offset:14336\n"
    "s_waitcnt lgkmcnt(4)\n"
    "v_mfma_f32_16x16x32_bf16 v[128:131], v[208:211], v[16:19], v[128:131]\n"
    "v_mfma_f32_16x16x32_bf16 v[136:139], v[208:211], v[48:51], v[136:139]\n"
    "v_mfma_f32_16x16x32_bf16 v[132:135], v[212:215], v[16:19], v[132:135]\n"
    "v_mfma_f32_16x16x32_bf16 v[140:143], v[212:215], v[48:51], v[140:143]\n"
    "v_mfma_f32_16x16x32_bf16 v[128:131], v[216:219], v[20:23], v[128:131]\n"
    "v_mfma_f32_16x16x32_bf16 v[136:139], v[216:219], v[52:55], v[136:139]\n"
    "v_mfma_f32_16x16x32_bf16 v[132:135], v[220:223], v[20:23], v[132:135]\n"
    "v_mfma_f32_16x16x32_bf16 v[140:143], v[220:223], v[52:55], v[140:143]\n"
    "s_waitcnt lgkmcnt(0)\n"
    "v_mfma_f32_16x16x32_bf16 v[128:131], v[224:227], v[24:27], v[128:131]\n"
    "v_mfma_f32_16x16x32_bf16 v[136:139], v[224:227], v[56:59], v[136:139]\n"
    "v_mfma_f32_16x16x32_bf16 v[132:135], v[228:231], v[24:27], v[132:135]\n"
    "v_mfma_f32_16x16x32_bf16 v[140:143], v[228:231], v[56:59], v[140:143]\n"
    "v_mfma_f32_16x16x32_bf16 v[128:131], v[232:235], v[28:31], v[128:131]\n"
    "v_mfma_f32_16x16x32_bf16 v[136:139], v[232:235], v[60:63], v[136:139]\n"
    "v_mfma_f32_16x16x32_bf16 v[132:135], v[236:239], v[28:31], v[132:135]\n"
    "v_mfma_f32_16x16x32_bf16 v[140:143], v[236:239], v[60:63], v[140:143]\n"
    "s_nop 4\n"
    "v_max3_f32 v176, v128, v129, v130\n"
    "v_max3_f32 v184, v136, v137, v138\n"
    "v_max3_f32 v176, v176, v131, v132\n"
    "v_max3_f32 v184, v184, v139, v140\n"
    "v_max3_f32 v176, v176, v133, v134\n"
    "v_max3_f32 v184, v184, v141, v142\n"
    "v_max_f32 v176, v176, v135\n"
    "v_max_f32 v184, v184, v143\n"
    "v_mov_b32 v177, v176\n"
    "v_mov_b32 v185, v184\n"
    "s_nop 0\n"
    "v_permlane32_swap_b32 v176, v177\n"
    "v_permlane32_swap_b32 v184, v185\n"
    "v_max_f32 v176, v176, v177\n"
    "v_max_f32 v184, v184, v185\n"
    "v_mov_b32 v177, v176\n"
    "v_mov_b32 v185, v184\n"
    "s_nop 0\n"
    "v_permlane16_swap_b32 v176, v177\n"
    "v_permlane16_swap_b32 v184, v185\n"
    "v_max_f32 v176, v176, v177\n"
    "v_max_f32 v184, v184, v185\n"
    "v_sub_f32 v128, v128, v176\n"
    "v_sub_f32 v136, v136, v184\n"
    "v_sub_f32 v129, v129, v176\n"
    "v_sub_f32 v137, v137, v184\n"
    "v_sub_f32 v130, v130, v176\n"
    "v_sub_f32 v138, v138, v184\n"
    "v_sub_f32 v131, v131, v176\n"
    "v_sub_f32 v139, v139, v184\n"
    "v_sub_f32 v132, v132, v176\n"
    "v_sub_f32 v140, v140, v184\n"
    "v_sub_f32 v133, v133, v176\n"
    "v_sub_f32 v141, v141, v184\n"
    "v_sub_f32 v134, v134, v176\n"
    "v_sub_f32 v142, v142, v184\n"
    "v_sub_f32 v135, v135, v176\n"
    "v_sub_f32 v143, v143, v184\n"
    "v_exp_f32 v128, v128\n"
    "s_nop 0\n"
    "v_add_f32 v246, v128, v246\n"
    "v_exp_f32 v129, v129\n"
    "s_nop 0\n"
    "v_add_f32 v246, v129, v246\n"
    "v_cvt_pk_bf16_f32 v128, v128, v129\n"
    "v_exp_f32 v130, v130\n"
    "v_exp_f32 v131, v131\n"
    "v_add_f32 v246, v130, v246\n"
    "v_add_f32 v246, v131, v246\n"
    "v_cvt_pk_bf16_f32 v129, v130, v131\n"
    "v_exp_f32 v132, v132\n"
    "v_exp_f32 v133, v133\n"
    "v_add_f32 v246, v132, v246\n"
    "v_add_f32 v246, v133, v246\n"
    "v_cvt_pk_bf16_f32 v130, v132, v133\n"
    "v_exp_f32 v134, v134\n"
    "s_nop 0\n"
    "v_add_f32 v246, v134, v246\n"
    "v_exp_f32 v135, v135\n"
    "v_exp_f32 v136, v136\n"
    "v_add_f32 v246, v135, v246\n"
    "v_cvt_pk_bf16_f32 v131, v134, v135\n"
    "v_add_f32 v247, v136, v247\n"
    "v_exp_f32 v137, v137\n"
    "v_exp_f32 v138, v138\n"
    "v_add_f32 v247, v137, v247\n"
    "v_cvt_pk_bf16_f32 v136, v136, v137\n"
    "v_add_f32 v247, v138, v247\n"
    "v_exp_f32 v139, v139\n"
    "s_nop 0\n"
    "v_add_f32 v247, v139, v247\n"
    "v_cvt_pk_bf16_f32 v137, v138, v139\n"
    "v_exp_f32 v140, v140\n"
    "s_nop 0\n"
    "v_add_f32 v247, v140, v247\n"
    "ds_read_b128 v[208:211], v240 offset:0\n"
    "ds_read_b128 v[212:215], v240 offset:2048\n"
    "ds_read_b128 v[216:219], v241 offset:0\n"
    "ds_read_b128 v[220:223], v241 offset:2048\n"
    "s_waitcnt vmcnt(12)\n"
    "ds_read_b128 v[64:67], v192 offset:0\n"
    "ds_read_b128 v[96:99], v192 offset:2048\n"
    "ds_read_b128 v[68:71], v193 offset:0\n"
    "ds_read_b128 v[100:103], v193 offset:2048\n"
    "s_mov_b32 s56, 0x4000\n"
    "s_add_u32 m0, s58, 0x4000\n"
    "s_nop 0\n"
    "buffer_load_dwordx4 v244, s[40:43], s56 offen offset:0 nt lds\n"
    "s_add_u32 m0, s58, 0x4f80\n"
    "s_nop 0\n"
    "buffer_load_dwordx4 v244, s[40:43], s56 offen offset:128 nt lds\n"
    "s_add_u32 m0, s58, 0x5f00\n"
    "s_nop 0\n"
    "buffer_load_dwordx4 v244, s[40:43], s56 offen offset:256 nt lds\n"
    "s_add_u32 m0, s58, 0x6e80\n"
    "s_nop 0\n"
    "buffer_load_dwordx4 v244, s[40:43], s56 offen offset:384 nt lds\n"
    "s_waitcnt lgkmcnt(3)\n"
    "v_lshlrev_b32 v160, 16, v64\n"
    "v_and_b32 v161, 0xffff0000, v64\n"
    "v_lshlrev_b32 v162, 16, v65\n"
    "v_and_b32 v163, 0xffff0000, v65\n"
    "v_lshlrev_b32 v164, 16, v66\n"
    "v_and_b32 v165, 0xffff0000, v66\n"
    "v_lshlrev_b32 v166, 16, v67\n"
    "v_and_b32 v167, 0xffff0000, v67\n"
    "v_mul_f32 v160, s66, v160\n"
    "v_mul_f32 v161, s66, v161\n"
    "v_mul_f32 v162, s66, v162\n"
    "v_mul_f32 v163, s66, v163\n"
    "v_mul_f32 v164, s66, v164\n"
    "v_mul_f32 v165, s66, v165\n"
    "v_mul_f32 v166, s66, v166\n"
    "v_mul_f32 v167, s66, v167\n"
    "v_cvt_pk_bf16_f32 v64, v160, v161\n"
    "v_cvt_pk_bf16_f32 v65, v162, v163\n"
    "v_cvt_pk_bf16_f32 v66, v164, v165\n"
    "v_cvt_pk_bf16_f32 v67, v166, v167\n"
    "s_waitcnt lgkmcnt(2)\n"
    "v_lshlrev_b32 v168, 16, v96\n"
    "v_and_b32 v169, 0xffff0000, v96\n"
    "v_lshlrev_b32 v170, 16, v97\n"
    "v_and_b32 v171, 0xffff0000, v97\n"
    "v_lshlrev_b32 v172, 16, v98\n"
    "v_and_b32 v173, 0xffff0000, v98\n"
    "v_lshlrev_b32 v174, 16, v99\n"
    "v_and_b32 v175, 0xffff0000, v99\n"
    "v_mul_f32 v168, s66, v168\n"
    "v_mul_f32 v169, s66, v169\n"
    "v_mul_f32 v170, s66, v170\n"
    "v_mul_f32 v171, s66, v171\n"
    "v_mul_f32 v172, s66, v172\n"
    "v_mul_f32 v173, s66, v173\n"
    "v_mul_f32 v174, s66, v174\n"
    "v_mul_f32 v175, s66, v175\n"
    "v_cvt_pk_bf16_f32 v96, v168, v169\n"
    "v_cvt_pk_bf16_f32 v97, v170, v171\n"
    "v_cvt_pk_bf16_f32 v98, v172, v173\n"
    "v_cvt_pk_bf16_f32 v99, v174, v175\n"
    "s_waitcnt lgkmcnt(1)\n"
    "v_lshlrev_b32 v160, 16, v68\n"
    "v_and_b32 v161, 0xffff0000, v68\n"
    "v_lshlrev_b32 v162, 16, v69\n"
    "v_and_b32 v163, 0xffff0000, v69\n"
    "v_lshlrev_b32 v164, 16, v70\n"
    "v_and_b32 v165, 0xffff0000, v70\n"
    "v_lshlrev_b32 v166, 16, v71\n"
    "v_and_b32 v167, 0xffff0000, v71\n"
    "v_mul_f32 v160, s66, v160\n"
    "v_mul_f32 v161, s66, v161\n"
    "v_mul_f32 v162, s66, v162\n"
    "v_mul_f32 v163, s66, v163\n"
    "v_mul_f32 v164, s66, v164\n"
    "v_mul_f32 v165, s66, v165\n"
    "v_mul_f32 v166, s66, v166\n"
    "v_mul_f32 v167, s66, v167\n"
    "v_cvt_pk_bf16_f32 v68, v160, v161\n"
    "v_cvt_pk_bf16_f32 v69, v162, v163\n"
    "v_cvt_pk_bf16_f32 v70, v164, v165\n"
    "v_cvt_pk_bf16_f32 v71, v166, v167\n"
    "s_waitcnt lgkmcnt(0)\n"
    "v_lshlrev_b32 v168, 16, v100\n"
    "v_and_b32 v169, 0xffff0000, v100\n"
    "v_lshlrev_b32 v170, 16, v101\n"
    "v_and_b32 v171, 0xffff0000, v101\n"
    "v_lshlrev_b32 v172, 16, v102\n"
    "v_and_b32 v173, 0xffff0000, v102\n"
    "v_lshlrev_b32 v174, 16, v103\n"
    "v_and_b32 v175, 0xffff0000, v103\n"
    "v_mul_f32 v168, s66, v168\n"
    "v_mul_f32 v169, s66, v169\n"
    "v_mul_f32 v170, s66, v170\n"
    "v_mul_f32 v171, s66, v171\n"
    "v_mul_f32 v172, s66, v172\n"
    "v_mul_f32 v173, s66, v173\n"
    "v_mul_f32 v174, s66, v174\n"
    "v_mul_f32 v175, s66, v175\n"
    "v_cvt_pk_bf16_f32 v100, v168, v169\n"
    "v_cvt_pk_bf16_f32 v101, v170, v171\n"
    "v_cvt_pk_bf16_f32 v102, v172, v173\n"
    "v_cvt_pk_bf16_f32 v103, v174, v175\n"
    "ds_read_b128 v[224:227], v240 offset:4096\n"
    "ds_read_b128 v[228:231], v240 offset:6144\n"
    "ds_read_b128 v[232:235], v241 offset:4096\n"
    "ds_read_b128 v[236:239], v241 offset:6144\n"
    "s_waitcnt vmcnt(12)\n"
    "ds_read_b128 v[72:75], v192 offset:4096\n"
    "ds_read_b128 v[104:107], v192 offset:6144\n"
    "ds_read_b128 v[76:79], v193 offset:4096\n"
    "ds_read_b128 v[108:111], v193 offset:6144\n"
    "s_mov_b32 s56, 0x0\n"
    "s_add_u32 m0, s59, 0x0\n"
    "s_nop 0\n"
    "buffer_load_dwordx4 v245, s[44:47], s56 offen offset:0 nt lds\n"
    "s_add_u32 m0, s59, 0x380\n"
    "s_nop 0\n"
    "buffer_load_dwordx4 v245, s[44:47], s56 offen offset:128 nt lds\n"
    "s_add_u32 m0, s59, 0x700\n"
    "s_nop 0\n"
    "buffer_load_dwordx4 v245, s[44:47], s56 offen offset:256 nt lds\n"
    "s_add_u32 m0, s59, 0xa80\n"
    "s_nop 0\n"
    "buffer_load_dwordx4 v245, s[44:47], s56 offen offset:384 nt lds\n"
    "s_waitcnt lgkmcnt(3)\n"
    "v_lshlrev_b32 v160, 16, v72\n"
    "v_and_b32 v161, 0xffff0000, v72\n"
    "v_mfma_f32_16x16x32_bf16 v[144:147], v[208:211], v[64:67], 0\n"
    "v_lshlrev_b32 v162, 16, v73\n"
    "v_and_b32 v163, 0xffff0000, v73\n"
    "v_lshlrev_b32 v164, 16, v74\n"
    "v_and_b32 v165, 0xffff0000, v74\n"
    "v_lshlrev_b32 v166, 16, v75\n"
    "v_and_b32 v167, 0xffff0000, v75\n"
    "v_mul_f32 v160, s66, v160\n"
    "v_mul_f32 v161, s66, v161\n"
    "v_mul_f32 v162, s66, v162\n"
    "v_mul_f32 v163, s66, v163\n"
    "v_mfma_f32_16x16x32_bf16 v[152:155], v[208:211], v[96:99], 0\n"
    "v_mul_f32 v164, s66, v164\n"
    "v_mul_f32 v165, s66, v165\n"
    "v_mul_f32 v166, s66, v166\n"
    "v_mul_f32 v167, s66, v167\n"
    "v_cvt_pk_bf16_f32 v72, v160, v161\n"
    "v_cvt_pk_bf16_f32 v73, v162, v163\n"
    "v_cvt_pk_bf16_f32 v74, v164, v165\n"
    "v_cvt_pk_bf16_f32 v75, v166, v167\n"
    "s_waitcnt lgkmcnt(2)\n"
    "v_lshlrev_b32 v168, 16, v104\n"
    "v_mfma_f32_16x16x32_bf16 v[148:151], v[212:215], v[64:67], 0\n"
    "v_and_b32 v169, 0xffff0000, v104\n"
    "v_lshlrev_b32 v170, 16, v105\n"
    "v_and_b32 v171, 0xffff0000, v105\n"
    "v_lshlrev_b32 v172, 16, v106\n"
    "v_and_b32 v173, 0xffff0000, v106\n"
    "v_lshlrev_b32 v174, 16, v107\n"
    "v_and_b32 v175, 0xffff0000, v107\n"
    "v_mul_f32 v168, s66, v168\n"
    "v_mul_f32 v169, s66, v169\n"
    "v_mul_f32 v170, s66, v170\n"
    "v_mfma_f32_16x16x32_bf16 v[156:159], v[212:215], v[96:99], 0\n"
    "v_mul_f32 v171, s66, v171\n"
    "v_mul_f32 v172, s66, v172\n"
    "v_mul_f32 v173, s66, v173\n"
    "v_mul_f32 v174, s66, v174\n"
    "v_mul_f32 v175, s66, v175\n"
    "v_cvt_pk_bf16_f32 v104, v168, v169\n"
    "v_cvt_pk_bf16_f32 v105, v170, v171\n"
    "v_cvt_pk_bf16_f32 v106, v172, v173\n"
    "v_cvt_pk_bf16_f32 v107, v174, v175\n"
    "s_waitcnt lgkmcnt(1)\n"
    "v_mfma_f32_16x16x32_bf16 v[144:147], v[216:219], v[68:71], v[144:147]\n"
    "v_lshlrev_b32 v160, 16, v76\n"
    "v_and_b32 v161, 0xffff0000, v76\n"
    "v_lshlrev_b32 v162, 16, v77\n"
    "v_and_b32 v163, 0xffff0000, v77\n"
    "v_lshlrev_b32 v164, 16, v78\n"
    "v_and_b32 v165, 0xffff0000, v78\n"
    "v_lshlrev_b32 v166, 16, v79\n"
    "v_and_b32 v167, 0xffff0000, v79\n"
    "v_mul_f32 v160, s66, v160\n"
    "v_mul_f32 v161, s66, v161\n"
    "v_mfma_f32_16x16x32_bf16 v[152:155], v[216:219], v[100:103], v[152:155]\n"
    "v_mul_f32 v162, s66, v162\n"
    "v_mul_f32 v163, s66, v163\n"
    "v_mul_f32 v164, s66, v164\n"
    "v_mul_f32 v165, s66, v165\n"
    "v_mul_f32 v166, s66, v166\n"
    "v_mul_f32 v167, s66, v167\n"
    "v_cvt_pk_bf16_f32 v76, v160, v161\n"
    "v_cvt_pk_bf16_f32 v77, v162, v163\n"
    "v_cvt_pk_bf16_f32 v78, v164, v165\n"
    "v_cvt_pk_bf16_f32 v79, v166, v167\n"
    "v_mfma_f32_16x16x32_bf16 v[148:151], v[220:223], v[68:71], v[148:151]\n"
    "s_waitcnt lgkmcnt(0)\n"
    "v_lshlrev_b32 v168, 16, v108\n"
    "v_and_b32 v169, 0xffff0000, v108\n"
    "v_lshlrev_b32 v170, 16, v109\n"
    "v_and_b32 v171, 0xffff0000, v109\n"
    "v_lshlrev_b32 v172, 16, v110\n"
    "v_and_b32 v173, 0xffff0000, v110\n"
    "v_lshlrev_b32 v174, 16, v111\n"
    "v_and_b32 v175, 0xffff0000, v111\n"
    "v_mul_f32 v168, s66, v168\n"
    "v_mfma_f32_16x16x32_bf16 v[156:159], v[220:223], v[100:103], v[156:159]\n"
    "v_mul_f32 v169, s66, v169\n"
    "v_mul_f32 v170, s66, v170\n"
    "v_mul_f32 v171, s66, v171\n"
    "v_mul_f32 v172, s66, v172\n"
    "v_mul_f32 v173, s66, v173\n"
    "v_mul_f32 v174, s66, v174\n"
    "v_mul_f32 v175, s66, v175\n"
    "v_cvt_pk_bf16_f32 v108, v168, v169\n"
    "v_cvt_pk_bf16_f32 v109, v170, v171\n"
    "v_cvt_pk_bf16_f32 v110, v172, v173\n"
    "v_cvt_pk_bf16_f32 v111, v174, v175\n"
    "ds_read_b128 v[208:211], v240 offset:8192\n"
    "ds_read_b128 v[212:215], v240 offset:10240\n"
    "ds_read_b128 v[216:219], v241 offset:8192\n"
    "ds_read_b128 v[220:223], v241 offset:10240\n"
    "s_waitcnt vmcnt(12)\n"
    "ds_read_b128 v[80:83], v192 offset:8192\n"
    "ds_read_b128 v[112:115], v192 offset:10240\n"
    "ds_read_b128 v[84:87], v193 offset:8192\n"
    "ds_read_b128 v[116:119], v193 offset:10240\n"
    "s_mov_b32 s56, 0x8000\n"
    "s_add_u32 m0, s58, 0x8000\n"
    "s_nop 0\n"
    "buffer_load_dwordx4 v244, s[40:43], s56 offen offset:0 nt lds\n"
    "s_add_u32 m0, s58, 0x8f80\n"
    "s_nop 0\n"
    "buffer_load_dwordx4 v244, s[40:43], s56 offen offset:128 nt lds\n"
    "s_add_u32 m0, s58, 0x9f00\n"
    "s_nop 0\n"
    "buffer_load_dwordx4 v244, s[40:43], s56 offen offset:256 nt lds\n"
    "s_add_u32 m0, s58, 0xae80\n"
    "s_nop 0\n"
    "buffer_load_dwordx4 v244, s[40:43], s56 offen offset:384 nt lds\n"
    "s_waitcnt lgkmcnt(3)\n"
    "v_lshlrev_b32 v160, 16, v80\n"
    "v_and_b32 v161, 0xffff0000, v80\n"
    "v_mfma_f32_16x16x32_bf16 v[144:147], v[224:227], v[72:75], v[144:147]\n"
    "v_lshlrev_b32 v162, 16, v81\n"
    "v_and_b32 v163, 0xffff0000, v81\n"
    "v_lshlrev_b32 v164, 16, v82\n"
    "v_and_b32 v165, 0xffff0000, v82\n"
    "v_lshlrev_b32 v166, 16, v83\n"
    "v_and_b32 v167, 0xffff0000, v83\n"
    "v_mul_f32 v160, s66, v160\n"
    "v_mul_f32 v161, s66, v161\n"
    "v_mul_f32 v162, s66, v162\n"
    "v_mul_f32 v163, s66, v163\n"
    "v_mfma_f32_16x16x32_bf16 v[152:155], v[224:227], v[104:107], v[152:155]\n"
    "v_mul_f32 v164, s66, v164\n"
    "v_mul_f32 v165, s66, v165\n"
    "v_mul_f32 v166, s66, v166\n"
    "v_mul_f32 v167, s66, v167\n"
    "v_cvt_pk_bf16_f32 v80, v160, v161\n"
    "v_cvt_pk_bf16_f32 v81, v162, v163\n"
    "v_cvt_pk_bf16_f32 v82, v164, v165\n"
    "v_cvt_pk_bf16_f32 v83, v166, v167\n"
    "s_waitcnt lgkmcnt(2)\n"
    "v_lshlrev_b32 v168, 16, v112\n"
    "v_mfma_f32_16x16x32_bf16 v[148:151], v[228:231], v[72:75], v[148:151]\n"
    "v_and_b32 v169, 0xffff0000, v112\n"
    "v_lshlrev_b32 v170, 16, v113\n"
    "v_and_b32 v171, 0xffff0000, v113\n"
    "v_lshlrev_b32 v172, 16, v114\n"
    "v_and_b32 v173, 0xffff0000, v114\n"
    "v_lshlrev_b32 v174, 16, v115\n"
    "v_and_b32 v175, 0xffff0000, v115\n"
    "v_mul_f32 v168, s66, v168\n"
    "v_mul_f32 v169, s66, v169\n"
    "v_mul_f32 v170, s66, v170\n"
    "v_mfma_f32_16x16x32_bf16 v[156:159], v[228:231], v[104:107], v[156:159]\n"
    "v_mul_f32 v171, s66, v171\n"
    "v_mul_f32 v172, s66, v172\n"
    "v_mul_f32 v173, s66, v173\n"
    "v_mul_f32 v174, s66, v174\n"
    "v_mul_f32 v175, s66, v175\n"
    "v_cvt_pk_bf16_f32 v112, v168, v169\n"
    "v_cvt_pk_bf16_f32 v113, v170, v171\n"
    "v_cvt_pk_bf16_f32 v114, v172, v173\n"
    "v_cvt_pk_bf16_f32 v115, v174, v175\n"
    "s_waitcnt lgkmcnt(1)\n"
    "v_mfma_f32_16x16x32_bf16 v[144:147], v[232:235], v[76:79], v[144:147]\n"
    "v_lshlrev_b32 v160, 16, v84\n"
    "v_and_b32 v161, 0xffff0000, v84\n"
    "v_lshlrev_b32 v162, 16, v85\n"
    "v_and_b32 v163, 0xffff0000, v85\n"
    "v_lshlrev_b32 v164, 16, v86\n"
    "v_and_b32 v165, 0xffff0000, v86\n"
    "v_lshlrev_b32 v166, 16, v87\n"
    "v_and_b32 v167, 0xffff0000, v87\n"
    "v_mul_f32 v160, s66, v160\n"
    "v_mul_f32 v161, s66, v161\n"
    "v_mfma_f32_16x16x32_bf16 v[152:155], v[232:235], v[108:111], v[152:155]\n"
    "v_mul_f32 v162, s66, v162\n"
    "v_mul_f32 v163, s66, v163\n"
    "v_mul_f32 v164, s66, v164\n"
    "v_mul_f32 v165, s66, v165\n"
    "v_mul_f32 v166, s66, v166\n"
    "v_mul_f32 v167, s66, v167\n"
    "v_cvt_pk_bf16_f32 v84, v160, v161\n"
    "v_cvt_pk_bf16_f32 v85, v162, v163\n"
    "v_cvt_pk_bf16_f32 v86, v164, v165\n"
    "v_cvt_pk_bf16_f32 v87, v166, v167\n"
    "v_mfma_f32_16x16x32_bf16 v[148:151], v[236:239], v[76:79], v[148:151]\n"
    "s_waitcnt lgkmcnt(0)\n"
    "v_lshlrev_b32 v168, 16, v116\n"
    "v_and_b32 v169, 0xffff0000, v116\n"
    "v_lshlrev_b32 v170, 16, v117\n"
    "v_and_b32 v171, 0xffff0000, v117\n"
    "v_lshlrev_b32 v172, 16, v118\n"
    "v_and_b32 v173, 0xffff0000, v118\n"
    "v_lshlrev_b32 v174, 16, v119\n"
    "v_and_b32 v175, 0xffff0000, v119\n"
    "v_mul_f32 v168, s66, v168\n"
    "v_mfma_f32_16x16x32_bf16 v[156:159], v[236:239], v[108:111], v[156:159]\n"
    "v_mul_f32 v169, s66, v169\n"
    "v_mul_f32 v170, s66, v170\n"
    "v_mul_f32 v171, s66, v171\n"
    "v_mul_f32 v172, s66, v172\n"
    "v_mul_f32 v173, s66, v173\n"
    "v_mul_f32 v174, s66, v174\n"
    "v_mul_f32 v175, s66, v175\n"
    "v_cvt_pk_bf16_f32 v116, v168, v169\n"
    "v_cvt_pk_bf16_f32 v117, v170, v171\n"
    "v_cvt_pk_bf16_f32 v118, v172, v173\n"
    "v_cvt_pk_bf16_f32 v119, v174, v175\n"
    "ds_read_b128 v[224:227], v240 offset:12288\n"
    "ds_read_b128 v[228:231], v240 offset:14336\n"
    "ds_read_b128 v[232:235], v241 offset:12288\n"
    "ds_read_b128 v[236:239], v241 offset:14336\n"
    "s_waitcnt vmcnt(12)\n"
    "ds_read_b128 v[88:91], v192 offset:12288\n"
    "ds_read_b128 v[120:123], v192 offset:14336\n"
    "ds_read_b128 v[92:95], v193 offset:12288\n"
    "ds_read_b128 v[124:127], v193 offset:14336\n"
    "s_waitcnt lgkmcnt(3)\n"
    "v_lshlrev_b32 v160, 16, v88\n"
    "v_and_b32 v161, 0xffff0000, v88\n"
    "v_mfma_f32_16x16x32_bf16 v[144:147], v[208:211], v[80:83], v[144:147]\n"
    "v_lshlrev_b32 v162, 16, v89\n"
    "v_and_b32 v163, 0xffff0000, v89\n"
    "v_lshlrev_b32 v164, 16, v90\n"
    "v_and_b32 v165, 0xffff0000, v90\n"
    "v_lshlrev_b32 v166, 16, v91\n"
    "v_and_b32 v167, 0xffff0000, v91\n"
    "v_mul_f32 v160, s66, v160\n"
    "v_mul_f32 v161, s66, v161\n"
    "v_mul_f32 v162, s66, v162\n"
    "v_mul_f32 v163, s66, v163\n"
    "v_mfma_f32_16x16x32_bf16 v[152:155], v[208:211], v[112:115], v[152:155]\n"
    "v_mul_f32 v164, s66, v164\n"
    "v_mul_f32 v165, s66, v165\n"
    "v_mul_f32 v166, s66, v166\n"
    "v_mul_f32 v167, s66, v167\n"
    "v_cvt_pk_bf16_f32 v88, v160, v161\n"
    "v_cvt_pk_bf16_f32 v89, v162, v163\n"
    "v_cvt_pk_bf16_f32 v90, v164, v165\n"
    "v_cvt_pk_bf16_f32 v91, v166, v167\n"
    "s_waitcnt lgkmcnt(2)\n"
    "v_lshlrev_b32 v168, 16, v120\n"
    "v_mfma_f32_16x16x32_bf16 v[148:151], v[212:215], v[80:83], v[148:151]\n"
    "v_and_b32 v169, 0xffff0000, v120\n"
    "v_lshlrev_b32 v170, 16, v121\n"
    "v_and_b32 v171, 0xffff0000, v121\n"
    "v_lshlrev_b32 v172, 16, v122\n"
    "v_and_b32 v173, 0xffff0000, v122\n"
    "v_lshlrev_b32 v174, 16, v123\n"
    "v_and_b32 v175, 0xffff0000, v123\n"
    "v_mul_f32 v168, s66, v168\n"
    "v_mul_f32 v169, s66, v169\n"
    "v_mul_f32 v170, s66, v170\n"
    "v_mfma_f32_16x16x32_bf16 v[156:159], v[212:215], v[112:115], v[156:159]\n"
    "v_mul_f32 v171, s66, v171\n"
    "v_mul_f32 v172, s66, v172\n"
    "v_mul_f32 v173, s66, v173\n"
    "v_mul_f32 v174, s66, v174\n"
    "v_mul_f32 v175, s66, v175\n"
    "v_cvt_pk_bf16_f32 v120, v168, v169\n"
    "v_cvt_pk_bf16_f32 v121, v170, v171\n"
    "v_cvt_pk_bf16_f32 v122, v172, v173\n"
    "v_cvt_pk_bf16_f32 v123, v174, v175\n"
    "s_waitcnt lgkmcnt(1)\n"
    "v_mfma_f32_16x16x32_bf16 v[144:147], v[216:219], v[84:87], v[144:147]\n"
    "v_lshlrev_b32 v160, 16, v92\n"
    "v_and_b32 v161, 0xffff0000, v92\n"
    "v_lshlrev_b32 v162, 16, v93\n"
    "v_and_b32 v163, 0xffff0000, v93\n"
    "v_lshlrev_b32 v164, 16, v94\n"
    "v_and_b32 v165, 0xffff0000, v94\n"
    "v_lshlrev_b32 v166, 16, v95\n"
    "v_and_b32 v167, 0xffff0000, v95\n"
    "v_mul_f32 v160, s66, v160\n"
    "v_mul_f32 v161, s66, v161\n"
    "v_mfma_f32_16x16x32_bf16 v[152:155], v[216:219], v[116:119], v[152:155]\n"
    "v_mul_f32 v162, s66, v162\n"
    "v_mul_f32 v163, s66, v163\n"
    "v_mul_f32 v164, s66, v164\n"
    "v_mul_f32 v165, s66, v165\n"
    "v_mul_f32 v166, s66, v166\n"
    "v_mul_f32 v167, s66, v167\n"
    "v_cvt_pk_bf16_f32 v92, v160, v161\n"
    "v_cvt_pk_bf16_f32 v93, v162, v163\n"
    "v_cvt_pk_bf16_f32 v94, v164, v165\n"
    "v_cvt_pk_bf16_f32 v95, v166, v167\n"
    "v_mfma_f32_16x16x32_bf16 v[148:151], v[220:223], v[84:87], v[148:151]\n"
    "s_waitcnt lgkmcnt(0)\n"
    "v_lshlrev_b32 v168, 16, v124\n"
    "v_and_b32 v169, 0xffff0000, v124\n"
    "v_lshlrev_b32 v170, 16, v125\n"
    "v_and_b32 v171, 0xffff0000, v125\n"
    "v_lshlrev_b32 v172, 16, v126\n"
    "v_and_b32 v173, 0xffff0000, v126\n"
    "v_lshlrev_b32 v174, 16, v127\n"
    "v_and_b32 v175, 0xffff0000, v127\n"
    "v_mul_f32 v168, s66, v168\n"
    "v_mfma_f32_16x16x32_bf16 v[156:159], v[220:223], v[116:119], v[156:159]\n"
    "v_mul_f32 v169, s66, v169\n"
    "v_mul_f32 v170, s66, v170\n"
    "v_mul_f32 v171, s66, v171\n"
    "v_mul_f32 v172, s66, v172\n"
    "v_mul_f32 v173, s66, v173\n"
    "v_mul_f32 v174, s66, v174\n"
    "v_mul_f32 v175, s66, v175\n"
    "v_cvt_pk_bf16_f32 v124, v168, v169\n"
    "v_cvt_pk_bf16_f32 v125, v170, v171\n"
    "v_cvt_pk_bf16_f32 v126, v172, v173\n"
    "v_cvt_pk_bf16_f32 v127, v174, v175\n"
    "v_mfma_f32_16x16x32_bf16 v[144:147], v[224:227], v[88:91], v[144:147]\n"
    "v_mfma_f32_16x16x32_bf16 v[152:155], v[224:227], v[120:123], v[152:155]\n"
    "v_mfma_f32_16x16x32_bf16 v[148:151], v[228:231], v[88:91], v[148:151]\n"
    "v_mfma_f32_16x16x32_bf16 v[156:159], v[228:231], v[120:123], v[156:159]\n"
    "v_mfma_f32_16x16x32_bf16 v[144:147], v[232:235], v[92:95], v[144:147]\n"
    "v_mfma_f32_16x16x32_bf16 v[152:155], v[232:235], v[124:127], v[152:155]\n"
    "v_mfma_f32_16x16x32_bf16 v[148:151], v[236:239], v[92:95], v[148:151]\n"
    "v_mfma_f32_16x16x32_bf16 v[156:159], v[236:239], v[124:127], v[156:159]\n"
    "s_nop 4\n"
    "v_max3_f32 v180, v144, v145, v146\n"
    "v_max3_f32 v188, v152, v153, v154\n"
    "v_max3_f32 v180, v180, v147, v148\n"
    "v_max3_f32 v188, v188, v155, v156\n"
    "v_max3_f32 v180, v180, v149, v150\n"
    "v_max3_f32 v188, v188, v157, v158\n"
    "v_max_f32 v180, v180, v151\n"
    "v_max_f32 v188, v188, v159\n"
    "v_mov_b32 v181, v180\n"
    "v_mov_b32 v189, v188\n"
    "s_nop 0\n"
    "v_permlane32_swap_b32 v180, v181\n"
    "v_permlane32_swap_b32 v188, v189\n"
    "v_max_f32 v180, v180, v181\n"
    "v_max_f32 v188, v188, v189\n"
    "v_mov_b32 v181, v180\n"
    "v_mov_b32 v189, v188\n"
    "s_nop 0\n"
    "v_permlane16_swap_b32 v180, v181\n"
    "v_permlane16_swap_b32 v188, v189\n"
    "v_max_f32 v180, v180, v181\n"
    "v_max_f32 v188, v188, v189\n"
    "v_sub_f32 v144, v144, v180\n"
    "v_sub_f32 v152, v152, v188\n"
    "v_sub_f32 v145, v145, v180\n"
    "v_sub_f32 v153, v153, v188\n"
    "v_sub_f32 v146, v146, v180\n"
    "v_sub_f32 v154, v154, v188\n"
    "v_sub_f32 v147, v147, v180\n"
    "v_sub_f32 v155, v155, v188\n"
    "v_sub_f32 v148, v148, v180\n"
    "v_sub_f32 v156, v156, v188\n"
    "v_sub_f32 v149, v149, v180\n"
    "v_sub_f32 v157, v157, v188\n"
    "v_sub_f32 v150, v150, v180\n"
    "v_sub_f32 v158, v158, v188\n"
    "v_sub_f32 v151, v151, v180\n"
    "v_sub_f32 v159, v159, v188\n"
    "v_sub_f32 v192, 0, v176\n"
    "v_sub_f32 v193, 0, v176\n"
    "v_sub_f32 v194, 0, v176\n"
    "v_sub_f32 v195, 0, v176\n"
    "v_sub_f32 v196, 0, v184\n"
    "v_sub_f32 v197, 0, v184\n"
    "v_sub_f32 v198, 0, v184\n"
    "v_sub_f32 v199, 0, v184\n"
    "v_sub_f32 v200, 0, v180\n"
    "v_sub_f32 v201, 0, v180\n"
    "v_sub_f32 v202, 0, v180\n"
    "v_sub_f32 v203, 0, v180\n"
    "v_sub_f32 v204, 0, v188\n"
    "v_sub_f32 v205, 0, v188\n"
    "v_sub_f32 v206, 0, v188\n"
    "v_sub_f32 v207, 0, v188\n"
    "s_mov_b32 s56, 0x4000\n"
    "s_add_u32 m0, s59, 0x4000\n"
    "s_nop 0\n"
    "buffer_load_dwordx4 v245, s[44:47], s56 offen offset:0 nt lds\n"
    "s_add_u32 m0, s59, 0x4380\n"
    "s_nop 0\n"
    "buffer_load_dwordx4 v245, s[44:47], s56 offen offset:128 nt lds\n"
    "s_add_u32 m0, s59, 0x4700\n"
    "s_nop 0\n"
    "buffer_load_dwordx4 v245, s[44:47], s56 offen offset:256 nt lds\n"
    "s_add_u32 m0, s59, 0x4a80\n"
    "s_nop 0\n"
    "buffer_load_dwordx4 v245, s[44:47], s56 offen offset:384 nt lds\n"
    "s_mov_b32 s56, 0xc000\n"
    "s_add_u32 m0, s58, 0xc000\n"
    "s_nop 0\n"
    "buffer_load_dwordx4 v244, s[40:43], s56 offen offset:0 nt lds\n"
    "s_add_u32 m0, s58, 0xcf80\n"
    "s_nop 0\n"
    "buffer_load_dwordx4 v244, s[40:43], s56 offen offset:128 nt lds\n"
    "s_add_u32 m0, s58, 0xdf00\n"
    "s_nop 0\n"
    "buffer_load_dwordx4 v244, s[40:43], s56 offen offset:256 nt lds\n"
    "s_add_u32 m0, s58, 0xee80\n"
    "s_nop 0\n"
    "buffer_load_dwordx4 v244, s[40:43], s56 offen offset:384 nt lds\n"
    "v_add_u32 v244, 0x8000, v244\n"
    "s_mov_b32 s56, 0x8000\n"
    "s_waitcnt vmcnt(12)\n"
    "s_barrier\n"
    "ds_read_b128 v[208:211], v240 offset:16384\n"
    "ds_read_b128 v[212:215], v241 offset:16384\n"
    "s_add_u32 m0, s59, 0x8000\n"
    "s_nop 0\n"
    "buffer_load_dwordx4 v245, s[44:47], s56 offen offset:0 nt lds\n"
    "ds_read_b128 v[216:219], v240 offset:20480\n"
    "ds_read_b128 v[220:223], v241 offset:20480\n"
    "s_add_u32 m0, s59, 0x8380\n"
    "s_nop 0\n"
    "buffer_load_dwordx4 v245, s[44:47], s56 offen offset:128 nt lds\n"
    "ds_read_b128 v[224:227], v240 offset:18432\n"
    "ds_read_b128 v[228:231], v241 offset:18432\n"
    "s_add_u32 m0, s59, 0x8700\n"
    "s_nop 0\n"
    "buffer_load_dwordx4 v245, s[44:47], s56 offen offset:256 nt lds\n"
    "s_waitcnt lgkmcnt(4)\n"
    "s_mov_b32 s60, 7\n"
    ".p2align 6\n"
    "L_loop_%=:\n"
    "v_mfma_f32_16x16x32_bf16 v[160:163], v[208:211], v[0:3], v[192:195]\n"
    "v_exp_f32 v141, v141\n"
    "v_mfma_f32_16x16x32_bf16 v[160:163], v[212:215], v[4:7], v[160:163]\n"
    "ds_read_b128 v[232:235], v240 offset:22528\n"
    "s_waitcnt lgkmcnt(3)\n"
    "v_mfma_f32_16x16x32_bf16 v[160:163], v[216:219], v[8:11], v[160:163]\n"
    "ds_read_b128 v[236:239], v241 offset:22528\n"
    "s_add_u32 m0, s59, 0x8a80\n"
    "v_mfma_f32_16x16x32_bf16 v[160:163], v[220:223], v[12:15], v[160:163]\n"
    "buffer_load_dwordx4 v245, s[44:47], s56 offen offset:384 nt lds\n"
    "v_add_f32 v247, v141, v247\n"
    "v_mfma_f32_16x16x32_bf16 v[168:171], v[220:223], v[44:47], v[196:199]\n"
    "v_cvt_pk_bf16_f32 v138, v140, v141\n"
    "v_mfma_f32_16x16x32_bf16 v[168:171], v[216:219], v[40:43], v[168:171]\n"
    "v_exp_f32 v142, v142\n"
    "v_mfma_f32_16x16x32_bf16 v[168:171], v[212:215], v[36:39], v[168:171]\n"
    "v_exp_f32 v143, v143\n"
    "v_mfma_f32_16x16x32_bf16 v[168:171], v[208:211], v[32:35], v[168:171]\n"
    "v_add_f32 v247, v142, v247\n"
    "v_add_f32 v247, v143, v247\n"
    "v_mfma_f32_16x16x32_bf16 v[176:179], v[208:211], v[64:67], v[200:203]\n"
    "v_exp_f32 v144, v144\n"
    "v_mfma_f32_16x16x32_bf16 v[176:179], v[212:215], v[68:71], v[176:179]\n"
    "v_cvt_pk_bf16_f32 v139, v142, v143\n"
    "v_add_f32 v248, v144, v248\n"
    "v_mfma_f32_16x16x32_bf16 v[176:179], v[216:219], v[72:75], v[176:179]\n"
    "s_add_u32 m0, s58, 0x0\n"
    "v_mfma_f32_16x16x32_bf16 v[176:179], v[220:223], v[76:79], v[176:179]\n"
    "buffer_load_dwordx4 v244, s[40:43], s56 offen offset:0 nt lds\n"
    "v_mfma_f32_16x16x32_bf16 v[184:187], v[220:223], v[108:111], v[204:207]\n"
    "v_exp_f32 v145, v145\n"
    "v_mfma_f32_16x16x32_bf16 v[184:187], v[216:219], v[104:107], v[184:187]\n"
    "v_exp_f32 v146, v146\n"
    "v_mfma_f32_16x16x32_bf16 v[184:187], v[212:215], v[100:103], v[184:187]\n"
    "v_add_f32 v248, v145, v248\n"
    "v_cvt_pk_bf16_f32 v144, v144, v145\n"
    "v_mfma_f32_16x16x32_bf16 v[184:187], v[208:211], v[96:99], v[184:187]\n"
    "v_add_f32 v248, v146, v248\n"
    "s_waitcnt lgkmcnt(2)\n"
    "v_mfma_f32_16x16x32_bf16 v[188:191], v[224:227], v[96:99], v[204:207]\n"
    "v_exp_f32 v147, v147\n"
    "v_mfma_f32_16x16x32_bf16 v[188:191], v[228:231], v[100:103], v[188:191]\n"
    "ds_read_b128 v[208:211], v240 offset:26624\n"
    "s_waitcnt lgkmcnt(1)\n"
    "v_mfma_f32_16x16x32_bf16 v[188:191], v[232:235], v[104:107], v[188:191]\n"
    "ds_read_b128 v[212:215], v241 offset:26624\n"
    "s_add_u32 m0, s58, 0xf80\n"
    "v_mfma_f32_16x16x32_bf16 v[188:191], v[236:239], v[108:111], v[188:191]\n"
    "ds_read_b128 v[216:219], v240 offset:30720\n"
    "buffer_load_dwordx4 v244, s[40:43], s56 offen offset:128 nt lds\n"
    "v_mfma_f32_16x16x32_bf16 v[180:183], v[236:239], v[76:79], v[200:203]\n"
    "ds_read_b128 v[220:223], v241 offset:30720\n"
    "v_add_f32 v248, v147, v248\n"
    "v_mfma_f32_16x16x32_bf16 v[180:183], v[232:235], v[72:75], v[180:183]\n"
    "v_exp_f32 v148, v148\n"
    "v_mfma_f32_16x16x32_bf16 v[180:183], v[228:231], v[68:71], v[180:183]\n"
    "v_cvt_pk_bf16_f32 v145, v146, v147\n"
    "v_add_f32 v248, v148, v248\n"
    "v_mfma_f32_16x16x32_bf16 v[180:183], v[224:227], v[64:67], v[180:183]\n"
    "v_exp_f32 v149, v149\n"
    "v_mfma_f32_16x16x32_bf16 v[172:175], v[224:227], v[32:35], v[196:199]\n"
    "v_add_f32 v248, v149, v248\n"
    "v_cvt_pk_bf16_f32 v146, v148, v149\n"
    "v_mfma_f32_16x16x32_bf16 v[172:175], v[228:231], v[36:39], v[172:175]\n"
    "v_exp_f32 v150, v150\n"
    "v_mfma_f32_16x16x32_bf16 v[172:175], v[232:235], v[40:43], v[172:175]\n"
    "s_add_u32 m0, s58, 0x1f00\n"
    "v_add_f32 v248, v150, v248\n"
    "v_mfma_f32_16x16x32_bf16 v[172:175], v[236:239], v[44:47], v[172:175]\n"
    "buffer_load_dwordx4 v244, s[40:43], s56 offen offset:256 nt lds\n"
    "v_mfma_f32_16x16x32_bf16 v[164:167], v[236:239], v[12:15], v[192:195]\n"
    "v_exp_f32 v151, v151\n"
    "v_mfma_f32_16x16x32_bf16 v[164:167], v[232:235], v[8:11], v[164:167]\n"
    "v_add_f32 v248, v151, v248\n"
    "v_cvt_pk_bf16_f32 v147, v150, v151\n"
    "v_mfma_f32_16x16x32_bf16 v[164:167], v[228:231], v[4:7], v[164:167]\n"
    "v_exp_f32 v152, v152\n"
    "v_mfma_f32_16x16x32_bf16 v[164:167], v[224:227], v[0:3], v[164:167]\n"
    "v_add_f32 v249, v152, v249\n"
    "s_waitcnt lgkmcnt(2)\n"
    "v_mfma_f32_16x16x32_bf16 v[164:167], v[208:211], v[16:19], v[164:167]\n"
    "v_exp_f32 v153, v153\n"
    "v_mfma_f32_16x16x32_bf16 v[164:167], v[212:215], v[20:23], v[164:167]\n"
    "ds_read_b128 v[224:227], v240 offset:24576\n"
    "s_waitcnt lgkmcnt(1)\n"
    "v_mfma_f32_16x16x32_bf16 v[164:167], v[216:219], v[24:27], v[164:167]\n"
    "ds_read_b128 v[228:231], v241 offset:24576\n"
    "s_add_u32 m0, s58, 0x2e80\n"
    "v_mfma_f32_16x16x32_bf16 v[164:167], v[220:223], v[28:31], v[164:167]\n"
    "ds_read_b128 v[232:235], v240 offset:28672\n"
    "buffer_load_dwordx4 v244, s[40:43], s56 offen offset:384 nt lds\n"
    "v_mfma_f32_16x16x32_bf16 v[172:175], v[220:223], v[60:63], v[172:175]\n"
    "ds_read_b128 v[236:239], v241 offset:28672\n"
    "v_add_f32 v249, v153, v249\n"
    "v_mfma_f32_16x16x32_bf16 v[172:175], v[216:219], v[56:59], v[172:175]\n"
    "v_cvt_pk_bf16_f32 v152, v152, v153\n"
    "v_mfma_f32_16x16x32_bf16 v[172:175], v[212:215], v[52:55], v[172:175]\n"
    "v_exp_f32 v154, v154\n"
    "v_mfma_f32_16x16x32_bf16 v[172:175], v[208:211], v[48:51], v[172:175]\n"
    "v_add_f32 v249, v154, v249\n"
    "v_mfma_f32_16x16x32_bf16 v[180:183], v[208:211], v[80:83], v[180:183]\n"
    "v_exp_f32 v155, v155\n"
    "v_mfma_f32_16x16x32_bf16 v[180:183], v[212:215], v[84:87], v[180:183]\n"
    "v_exp_f32 v156, v156\n"
    "v_mfma_f32_16x16x32_bf16 v[180:183], v[216:219], v[88:91], v[180:183]\n"
    "v_add_f32 v249, v155, v249\n"
    "v_cvt_pk_bf16_f32 v153, v154, v155\n"
    "v_mfma_f32_16x16x32_bf16 v[180:183], v[220:223], v[92:95], v[180:183]\n"
    "v_exp_f32 v157, v157\n"
    "v_mfma_f32_16x16x32_bf16 v[188:191], v[220:223], v[124:127], v[188:191]\n"
    "v_exp_f32 v158, v158\n"
    "v_mfma_f32_16x16x32_bf16 v[188:191], v[216:219], v[120:123], v[188:191]\n"
    "v_add_f32 v249, v156, v249\n"
    "v_add_f32 v249, v157, v249\n"
    "v_mfma_f32_16x16x32_bf16 v[188:191], v[212:215], v[116:119], v[188:191]\n"
    "v_exp_f32 v159, v159\n"
    "v_mfma_f32_16x16x32_bf16 v[188:191], v[208:211], v[112:115], v[188:191]\n"
    "s_add_u32 s56, s56, 0x4000\n"
    "s_waitcnt lgkmcnt(2)\n"
    "v_mfma_f32_16x16x32_bf16 v[184:187], v[224:227], v[112:115], v[184:187]\n"
    "v_cvt_pk_bf16_f32 v154, v156, v157\n"
    "v_add_f32 v249, v158, v249\n"
    "v_mfma_f32_16x16x32_bf16 v[184:187], v[228:231], v[116:119], v[184:187]\n"
    "ds_read_b64_tr_b16 v[208:209], v242 offset:0\n"
    "s_waitcnt lgkmcnt(1)\n"
    "v_mfma_f32_16x16x32_bf16 v[184:187], v[232:235], v[120:123], v[184:187]\n"
    "ds_read_b64_tr_b16 v[210:211], v242 offset:8192\n"
    "v_add_f32 v249, v159, v249\n"
    "v_mfma_f32_16x16x32_bf16 v[184:187], v[236:239], v[124:127], v[184:187]\n"
    "ds_read_b64_tr_b16 v[212:213], v243 offset:0\n"
    "v_cvt_pk_bf16_f32 v155, v158, v159\n"
    "v_mfma_f32_16x16x32_bf16 v[176:179], v[236:239], v[92:95], v[176:179]\n"
    "ds_read_b64_tr_b16 v[214:215], v243 offset:8192\n"
    "v_mfma_f32_16x16x32_bf16 v[176:179], v[232:235], v[88:91], v[176:179]\n"
    "ds_read_b64_tr_b16 v[216:217], v242 offset:512\n"
    "v_mfma_f32_16x16x32_bf16 v[176:179], v[228:231], v[84:87], v[176:179]\n"
    "ds_read_b64_tr_b16 v[218:219], v242 offset:8704\n"
    "v_mfma_f32_16x16x32_bf16 v[176:179], v[224:227], v[80:83], v[176:179]\n"
    "ds_read_b64_tr_b16 v[220:221], v243 offset:512\n"
    "v_mfma_f32_16x16x32_bf16 v[168:171], v[224:227], v[48:51], v[168:171]\n"
    "ds_read_b64_tr_b16 v[222:223], v243 offset:8704\n"
    "v_mfma_f32_16x16x32_bf16 v[168:171], v[228:231], v[52:55], v[168:171]\n"
    "v_mfma_f32_16x16x32_bf16 v[168:171], v[232:235], v[56:59], v[168:171]\n"
    "v_mfma_f32_16x16x32_bf16 v[168:171], v[236:239], v[60:63], v[168:171]\n"
    "v_mfma_f32_16x16x32_bf16 v[160:163], v[236:239], v[28:31], v[160:163]\n"
    "v_mfma_f32_16x16x32_bf16 v[160:163], v[232:235], v[24:27], v[160:163]\n"
    "v_mfma_f32_16x16x32_bf16 v[160:163], v[228:231], v[20:23], v[160:163]\n"
    "v_mfma_f32_16x16x32_bf16 v[160:163], v[224:227], v[16:19], v[160:163]\n"
    "s_waitcnt lgkmcnt(4)\n"
    "v_mfma_f32_16x16x32_bf16 a[0:3], v[208:211], v[128:131], a[0:3]\n"
    "ds_read_b64_tr_b16 v[224:225], v242 offset:1024\n"
    "v_mfma_f32_16x16x32_bf16 a[16:19], v[212:215], v[128:131], a[16:19]\n"
    "ds_read_b64_tr_b16 v[226:227], v242 offset:9216\n"
    "v_mfma_f32_16x16x32_bf16 a[20:23], v[212:215], v[136:139], a[20:23]\n"
    "ds_read_b64_tr_b16 v[228:229], v243 offset:1024\n"
    "v_mfma_f32_16x16x32_bf16 a[4:7], v[208:211], v[136:139], a[4:7]\n"
    "ds_read_b64_tr_b16 v[230:231], v243 offset:9216\n"
    "v_mfma_f32_16x16x32_bf16 a[8:11], v[208:211], v[144:147], a[8:11]\n"
    "ds_read_b64_tr_b16 v[232:233], v242 offset:1536\n"
    "v_mfma_f32_16x16x32_bf16 a[24:27], v[212:215], v[144:147], a[24:27]\n"
    "ds_read_b64_tr_b16 v[234:235], v242 offset:9728\n"
    "v_mfma_f32_16x16x32_bf16 a[28:31], v[212:215], v[152:155], a[28:31]\n"
    "v_exp_f32 v160, v160\n"
    "v_mfma_f32_16x16x32_bf16 a[12:15], v[208:211], v[152:155], a[12:15]\n"
    "ds_read_b64_tr_b16 v[236:237], v243 offset:1536\n"
    "s_waitcnt lgkmcnt(7)\n"
    "v_mfma_f32_16x16x32_bf16 a[44:47], v[216:219], v[152:155], a[44:47]\n"
    "ds_read_b64_tr_b16 v[238:239], v243 offset:9728\n"
    "v_add_f32 v246, v160, v246\n"
    "v_mfma_f32_16x16x32_bf16 a[60:63], v[220:223], v[152:155], a[60:63]\n"
    "ds_read_b64_tr_b16 v[208:209], v242 offset:2048\n"
    "v_mfma_f32_16x16x32_bf16 a[56:59], v[220:223], v[144:147], a[56:59]\n"
    "ds_read_b64_tr_b16 v[210:211], v242 offset:10240\n"
    "v_mfma_f32_16x16x32_bf16 a[40:43], v[216:219], v[144:147], a[40:43]\n"
    "ds_read_b64_tr_b16 v[212:213], v243 offset:2048\n"
    "v_mfma_f32_16x16x32_bf16 a[36:39], v[216:219], v[136:139], a[36:39]\n"
    "ds_read_b64_tr_b16 v[214:215], v243 offset:10240\n"
    "v_mfma_f32_16x16x32_bf16 a[52:55], v[220:223], v[136:139], a[52:55]\n"
    "v_exp_f32 v161, v161\n"
    "v_mfma_f32_16x16x32_bf16 a[48:51], v[220:223], v[128:131], a[48:51]\n"
    "v_add_f32 v246, v161, v246\n"
    "v_cvt_pk_bf16_f32 v160, v160, v161\n"
    "v_mfma_f32_16x16x32_bf16 a[32:35], v[216:219], v[128:131], a[32:35]\n"
    "s_waitcnt lgkmcnt(8)\n"
    "v_mfma_f32_16x16x32_bf16 a[64:67], v[224:227], v[128:131], a[64:67]\n"
    "ds_read_b64_tr_b16 v[216:217], v242 offset:2560\n"
    "v_mfma_f32_16x16x32_bf16 a[80:83], v[228:231], v[128:131], a[80:83]\n"
    "ds_read_b64_tr_b16 v[218:219], v242 offset:10752\n"
    "v_mfma_f32_16x16x32_bf16 a[84:87], v[228:231], v[136:139], a[84:87]\n"
    "ds_read_b64_tr_b16 v[220:221], v243 offset:2560\n"
    "v_mfma_f32_16x16x32_bf16 a[68:71], v[224:227], v[136:139], a[68:71]\n"
    "ds_read_b64_tr_b16 v[222:223], v243 offset:10752\n"
    "v_mfma_f32_16x16x32_bf16 a[72:75], v[224:227], v[144:147], a[72:75]\n"
    "v_exp_f32 v162, v162\n"
    "v_mfma_f32_16x16x32_bf16 a[88:91], v[228:231], v[144:147], a[88:91]\n"
    "v_exp_f32 v163, v163\n"
    "v_mfma_f32_16x16x32_bf16 a[92:95], v[228:231], v[152:155], a[92:95]\n"
    "v_add_f32 v246, v162, v246\n"
    "v_add_f32 v246, v163, v246\n"
    "v_mfma_f32_16x16x32_bf16 a[76:79], v[224:227], v[152:155], a[76:79]\n"
    "v_cvt_pk_bf16_f32 v161, v162, v163\n"
    "s_waitcnt lgkmcnt(8)\n"
    "v_mfma_f32_16x16x32_bf16 a[108:111], v[232:235], v[152:155], a[108:111]\n"
    "ds_read_b64_tr_b16 v[224:225], v242 offset:3072\n"
    "v_mfma_f32_16x16x32_bf16 a[124:127], v[236:239], v[152:155], a[124:127]\n"
    "ds_read_b64_tr_b16 v[226:227], v242 offset:11264\n"
    "v_mfma_f32_16x16x32_bf16 a[120:123], v[236:239], v[144:147], a[120:123]\n"
    "ds_read_b64_tr_b16 v[228:229], v243 offset:3072\n"
    "v_mfma_f32_16x16x32_bf16 a[104:107], v[232:235], v[144:147], a[104:107]\n"
    "ds_read_b64_tr_b16 v[230:231], v243 offset:11264\n"
    "v_mfma_f32_16x16x32_bf16 a[100:103], v[232:235], v[136:139], a[100:103]\n"
    "v_exp_f32 v164, v164\n"
    "v_mfma_f32_16x16x32_bf16 a[116:119], v[236:239], v[136:139], a[116:119]\n"
    "v_exp_f32 v165, v165\n"
    "v_mfma_f32_16x16x32_bf16 a[112:115], v[236:239], v[128:131], a[112:115]\n"
    "v_add_f32 v246, v164, v246\n"
    "v_add_f32 v246, v165, v246\n"
    "v_mfma_f32_16x16x32_bf16 a[96:99], v[232:235], v[128:131], a[96:99]\n"
    "v_cvt_pk_bf16_f32 v162, v164, v165\n"
    "s_waitcnt lgkmcnt(8)\n"
    "v_mfma_f32_16x16x32_bf16 a[128:131], v[208:211], v[128:131], a[128:131]\n"
    "ds_read_b64_tr_b16 v[232:233], v242 offset:3584\n"
    "v_mfma_f32_16x16x32_bf16 a[144:147], v[212:215], v[128:131], a[144:147]\n"
    "ds_read_b64_tr_b16 v[234:235], v242 offset:11776\n"
    "v_mfma_f32_16x16x32_bf16 a[148:151], v[212:215], v[136:139], a[148:151]\n"
    "ds_read_b64_tr_b16 v[236:237], v243 offset:3584\n"
    "v_mfma_f32_16x16x32_bf16 a[132:135], v[208:211], v[136:139], a[132:135]\n"
    "ds_read_b64_tr_b16 v[238:239], v243 offset:11776\n"
    "v_mfma_f32_16x16x32_bf16 a[136:139], v[208:211], v[144:147], a[136:139]\n"
    "v_exp_f32 v166, v166\n"
    "v_mfma_f32_16x16x32_bf16 a[152:155], v[212:215], v[144:147], a[152:155]\n"
    "v_add_f32 v246, v166, v246\n"
    "v_mfma_f32_16x16x32_bf16 a[156:159], v[212:215], v[152:155], a[156:159]\n"
    "s_waitcnt vmcnt(12)\n"
    "s_barrier\n"
    "v_mfma_f32_16x16x32_bf16 a[140:143], v[208:211], v[152:155], a[140:143]\n"
    "s_waitcnt lgkmcnt(8)\n"
    "v_mfma_f32_16x16x32_bf16 a[172:175], v[216:219], v[152:155], a[172:175]\n"
    "ds_read_b128 v[208:211], v240 offset:32768\n"
    "v_mfma_f32_16x16x32_bf16 a[188:191], v[220:223], v[152:155], a[188:191]\n"
    "ds_read_b128 v[212:215], v241 offset:32768\n"
    "v_mfma_f32_16x16x32_bf16 a[184:187], v[220:223], v[144:147], a[184:187]\n"
    "s_add_u32 m0, s59, 0xc000\n"
    "v_mfma_f32_16x16x32_bf16 a[168:171], v[216:219], v[144:147], a[168:171]\n"
    "buffer_load_dwordx4 v245, s[44:47], s56 offen offset:0 nt lds\n"
    "v_mfma_f32_16x16x32_bf16 a[164:167], v[216:219], v[136:139], a[164:167]\n"
    "v_exp_f32 v167, v167\n"
    "v_mfma_f32_16x16x32_bf16 a[180:183], v[220:223], v[136:139], a[180:183]\n"
    "v_exp_f32 v168, v168\n"
    "v_mfma_f32_16x16x32_bf16 a[176:179], v[220:223], v[128:131], a[176:179]\n"
    "v_add_f32 v246, v167, v246\n"
    "v_cvt_pk_bf16_f32 v163, v166, v167\n"
    "v_mfma_f32_16x16x32_bf16 a[160:163], v[216:219], v[128:131], a[160:163]\n"
    "v_add_f32 v247, v168, v247\n"
    "s_waitcnt lgkmcnt(6)\n"
    "v_mfma_f32_16x16x32_bf16 a[192:195], v[224:227], v[128:131], a[192:195]\n"
    "ds_read_b128 v[216:219], v240 offset:36864\n"
    "v_mfma_f32_16x16x32_bf16 a[208:211], v[228:231], v[128:131], a[208:211]\n"
    "ds_read_b128 v[220:223], v241 offset:36864\n"
    "v_mfma_f32_16x16x32_bf16 a[212:215], v[228:231], v[136:139], a[212:215]\n"
    "s_add_u32 m0, s59, 0xc380\n"
    "v_mfma_f32_16x16x32_bf16 a[196:199], v[224:227], v[136:139], a[196:199]\n"
    "buffer_load_dwordx4 v245, s[44:47], s56 offen offset:128 nt lds\n"
    "v_mfma_f32_16x16x32_bf16 a[200:203], v[224:227], v[144:147], a[200:203]\n"
    "v_exp_f32 v169, v169\n"
    "v_mfma_f32_16x16x32_bf16 a[216:219], v[228:231], v[144:147], a[216:219]\n"
    "v_exp_f32 v170, v170\n"
    "v_mfma_f32_16x16x32_bf16 a[220:223], v[228:231], v[152:155], a[220:223]\n"
    "v_add_f32 v247, v169, v247\n"
    "v_cvt_pk_bf16_f32 v168, v168, v169\n"
    "v_mfma_f32_16x16x32_bf16 a[204:207], v[224:227], v[152:155], a[204:207]\n"
    "v_add_f32 v247, v170, v247\n"
    "s_waitcnt lgkmcnt(4)\n"
    "v_mfma_f32_16x16x32_bf16 a[236:239], v[232:235], v[152:155], a[236:239]\n"
    "ds_read_b128 v[224:227], v240 offset:34816\n"
    "v_mfma_f32_16x16x32_bf16 a[252:255], v[236:239], v[152:155], a[252:255]\n"
    "ds_read_b128 v[228:231], v241 offset:34816\n"
    "v_mfma_f32_16x16x32_bf16 a[248:251], v[236:239], v[144:147], a[248:251]\n"
    "s_add_u32 m0, s59, 0xc700\n"
    "v_mfma_f32_16x16x32_bf16 a[232:235], v[232:235], v[144:147], a[232:235]\n"
    "buffer_load_dwordx4 v245, s[44:47], s56 offen offset:256 nt lds\n"
    "v_mfma_f32_16x16x32_bf16 a[228:231], v[232:235], v[136:139], a[228:231]\n"
    "v_exp_f32 v171, v171\n"
    "v_mfma_f32_16x16x32_bf16 a[244:247], v[236:239], v[136:139], a[244:247]\n"
    "v_add_f32 v247, v171, v247\n"
    "v_cvt_pk_bf16_f32 v169, v170, v171\n"
    "v_mfma_f32_16x16x32_bf16 a[240:243], v[236:239], v[128:131], a[240:243]\n"
    "v_exp_f32 v172, v172\n"
    "v_mfma_f32_16x16x32_bf16 a[224:227], v[232:235], v[128:131], a[224:227]\n"
    "v_add_f32 v247, v172, v247\n"
    "s_waitcnt lgkmcnt(4)\n"
    "v_mfma_f32_16x16x32_bf16 v[128:131], v[208:211], v[0:3], v[192:195]\n"
    "v_exp_f32 v173, v173\n"
    "v_mfma_f32_16x16x32_bf16 v[128:131], v[212:215], v[4:7], v[128:131]\n"
    "ds_read_b128 v[232:235], v240 offset:38912\n"
    "s_waitcnt lgkmcnt(3)\n"
    "v_mfma_f32_16x16x32_bf16 v[128:131], v[216:219], v[8:11], v[128:131]\n"
    "ds_read_b128 v[236:239], v241 offset:38912\n"
    "s_add_u32 m0, s59, 0xca80\n"
    "v_mfma_f32_16x16x32_bf16 v[128:131], v[220:223], v[12:15], v[128:131]\n"
    "buffer_load_dwordx4 v245, s[44:47], s56 offen offset:384 nt lds\n"
    "v_add_f32 v247, v173, v247\n"
    "v_mfma_f32_16x16x32_bf16 v[136:139], v[220:223], v[44:47], v[196:199]\n"
    "v_cvt_pk_bf16_f32 v170, v172, v173\n"
    "v_mfma_f32_16x16x32_bf16 v[136:139], v[216:219], v[40:43], v[136:139]\n"
    "v_exp_f32 v174, v174\n"
    "v_mfma_f32_16x16x32_bf16 v[136:139], v[212:215], v[36:39], v[136:139]\n"
    "v_exp_f32 v175, v175\n"
    "v_mfma_f32_16x16x32_bf16 v[136:139], v[208:211], v[32:35], v[136:139]\n"
    "v_add_f32 v247, v174, v247\n"
    "v_add_f32 v247, v175, v247\n"
    "v_mfma_f32_16x16x32_bf16 v[144:147], v[208:211], v[64:67], v[200:203]\n"
    "v_exp_f32 v176, v176\n"
    "v_mfma_f32_16x16x32_bf16 v[144:147], v[212:215], v[68:71], v[144:147]\n"
    "v_cvt_pk_bf16_f32 v171, v174, v175\n"
    "v_add_f32 v248, v176, v248\n"
    "v_mfma_f32_16x16x32_bf16 v[144:147], v[216:219], v[72:75], v[144:147]\n"
    "s_add_u32 m0, s58, 0x4000\n"
    "v_mfma_f32_16x16x32_bf16 v[144:147], v[220:223], v[76:79], v[144:147]\n"
    "buffer_load_dwordx4 v244, s[40:43], s56 offen offset:0 nt lds\n"
    "v_mfma_f32_16x16x32_bf16 v[152:155], v[220:223], v[108:111], v[204:207]\n"
    "v_exp_f32 v177, v177\n"
    "v_mfma_f32_16x16x32_bf16 v[152:155], v[216:219], v[104:107], v[152:155]\n"
    "v_exp_f32 v178, v178\n"
    "v_mfma_f32_16x16x32_bf16 v[152:155], v[212:215], v[100:103], v[152:155]\n"
    "v_add_f32 v248, v177, v248\n"
    "v_cvt_pk_bf16_f32 v176, v176, v177\n"
    "v_mfma_f32_16x16x32_bf16 v[152:155], v[208:211], v[96:99], v[152:155]\n"
    "v_add_f32 v248, v178, v248\n"
    "s_waitcnt lgkmcnt(2)\n"
    "v_mfma_f32_16x16x32_bf16 v[156:159], v[224:227], v[96:99], v[204:207]\n"
    "v_exp_f32 v179, v179\n"
    "v_mfma_f32_16x16x32_bf16 v[156:159], v[228:231], v[100:103], v[156:159]\n"
    "ds_read_b128 v[208:211], v240 offset:43008\n"
    "s_waitcnt lgkmcnt(1)\n"
    "v_mfma_f32_16x16x32_bf16 v[156:159], v[232:235], v[104:107], v[156:159]\n"
    "ds_read_b128 v[212:215], v241 offset:43008\n"
    "s_add_u32 m0, s58, 0x4f80\n"
    "v_mfma_f32_16x16x32_bf16 v[156:159], v[236:239], v[108:111], v[156:159]\n"
    "ds_read_b128 v[216:219], v240 offset:47104\n"
    "buffer_load_dwordx4 v244, s[40:43], s56 offen offset:128 nt lds\n"
    "v_mfma_f32_16x16x32_bf16 v[148:151], v[236:239], v[76:79], v[200:203]\n"
    "ds_read_b128 v[220:223], v241 offset:47104\n"
    "v_add_f32 v248, v179, v248\n"
    "v_mfma_f32_16x16x32_bf16 v[148:151], v[232:235], v[72:75], v[148:151]\n"
    "v_exp_f32 v180, v180\n"
    "v_mfma_f32_16x16x32_bf16 v[148:151], v[228:231], v[68:71], v[148:151]\n"
    "v_cvt_pk_bf16_f32 v177, v178, v179\n"
    "v_add_f32 v248, v180, v248\n"
    "v_mfma_f32_16x16x32_bf16 v[148:151], v[224:227], v[64:67], v[148:151]\n"
    "v_exp_f32 v181, v181\n"
    "v_mfma_f32_16x16x32_bf16 v[140:143], v[224:227], v[32:35], v[196:199]\n"
    "v_add_f32 v248, v181, v248\n"
    "v_cvt_pk_bf16_f32 v178, v180, v181\n"
    "v_mfma_f32_16x16x32_bf16 v[140:143], v[228:231], v[36:39], v[140:143]\n"
    "v_exp_f32 v182, v182\n"
    "v_mfma_f32_16x16x32_bf16 v[140:143], v[232:235], v[40:43], v[140:143]\n"
    "s_add_u32 m0, s58, 0x5f00\n"
    "v_add_f32 v248, v182, v248\n"
    "v_mfma_f32_16x16x32_bf16 v[140:143], v[236:239], v[44:47], v[140:143]\n"
    "buffer_load_dwordx4 v244, s[40:43], s56 offen offset:256 nt lds\n"
    "v_mfma_f32_16x16x32_bf16 v[132:135], v[236:239], v[12:15], v[192:195]\n"
    "v_exp_f32 v183, v183\n"
    "v_mfma_f32_16x16x32_bf16 v[132:135], v[232:235], v[8:11], v[132:135]\n"
    "v_add_f32 v248, v183, v248\n"
    "v_cvt_pk_bf16_f32 v179, v182, v183\n"
    "v_mfma_f32_16x16x32_bf16 v[132:135], v[228:231], v[4:7], v[132:135]\n"
    "v_exp_f32 v184, v184\n"
    "v_mfma_f32_16x16x32_bf16 v[132:135], v[224:227], v[0:3], v[132:135]\n"
    "v_add_f32 v249, v184, v249\n"
    "s_waitcnt lgkmcnt(2)\n"
    "v_mfma_f32_16x16x32_bf16 v[132:135], v[208:211], v[16:19], v[132:135]\n"
    "v_exp_f32 v185, v185\n"
    "v_mfma_f32_16x16x32_bf16 v[132:135], v[212:215], v[20:23], v[132:135]\n"
    "ds_read_b128 v[224:227], v240 offset:40960\n"
    "s_waitcnt lgkmcnt(1)\n"
    "v_mfma_f32_16x16x32_bf16 v[132:135], v[216:219], v[24:27], v[132:135]\n"
    "ds_read_b128 v[228:231], v241 offset:40960\n"
    "s_add_u32 m0, s58, 0x6e80\n"
    "v_mfma_f32_16x16x32_bf16 v[132:135], v[220:223], v[28:31], v[132:135]\n"
    "ds_read_b128 v[232:235], v240 offset:45056\n"
    "buffer_load_dwordx4 v244, s[40:43], s56 offen offset:384 nt lds\n"
    "v_mfma_f32_16x16x32_bf16 v[140:143], v[220:223], v[60:63], v[140:143]\n"
    "ds_read_b128 v[236:239], v241 offset:45056\n"
    "v_add_f32 v249, v185, v249\n"
    "v_mfma_f32_16x16x32_bf16 v[140:143], v[216:219], v[56:59], v[140:143]\n"
    "v_cvt_pk_bf16_f32 v184, v184, v185\n"
    "v_mfma_f32_16x16x32_bf16 v[140:143], v[212:215], v[52:55], v[140:143]\n"
    "v_exp_f32 v186, v186\n"
    "v_mfma_f32_16x16x32_bf16 v[140:143], v[208:211], v[48:51], v[140:143]\n"
    "v_add_f32 v249, v186, v249\n"
    "v_mfma_f32_16x16x32_bf16 v[148:151], v[208:211], v[80:83], v[148:151]\n"
    "v_exp_f32 v187, v187\n"
    "v_mfma_f32_16x16x32_bf16 v[148:151], v[212:215], v[84:87], v[148:151]\n"
    "v_exp_f32 v188, v188\n"
    "v_mfma_f32_16x16x32_bf16 v[148:151], v[216:219], v[88:91], v[148:151]\n"
    "v_add_f32 v249, v187, v249\n"
    "v_cvt_pk_bf16_f32 v185, v186, v187\n"
    "v_mfma_f32_16x16x32_bf16 v[148:151], v[220:223], v[92:95], v[148:151]\n"
    "v_exp_f32 v189, v189\n"
    "v_mfma_f32_16x16x32_bf16 v[156:159], v[220:223], v[124:127], v[156:159]\n"
    "v_exp_f32 v190, v190\n"
    "v_mfma_f32_16x16x32_bf16 v[156:159], v[216:219], v[120:123], v[156:159]\n"
    "v_add_f32 v249, v188, v249\n"
    "v_add_f32 v249, v189, v249\n"
    "v_mfma_f32_16x16x32_bf16 v[156:159], v[212:215], v[116:119], v[156:159]\n"
    "v_exp_f32 v191, v191\n"
    "v_mfma_f32_16x16x32_bf16 v[156:159], v[208:211], v[112:115], v[156:159]\n"
    "s_add_u32 s56, s56, 0x4000\n"
    "s_waitcnt lgkmcnt(2)\n"
    "v_mfma_f32_16x16x32_bf16 v[152:155], v[224:227], v[112:115], v[152:155]\n"
    "v_cvt_pk_bf16_f32 v186, v188, v189\n"
    "v_add_f32 v249, v190, v249\n"
    "v_mfma_f32_16x16x32_bf16 v[152:155], v[228:231], v[116:119], v[152:155]\n"
    "ds_read_b64_tr_b16 v[208:209], v242 offset:16384\n"
    "s_waitcnt lgkmcnt(1)\n"
    "v_mfma_f32_16x16x32_bf16 v[152:155], v[232:235], v[120:123], v[152:155]\n"
    "ds_read_b64_tr_b16 v[210:211], v242 offset:24576\n"
    "v_add_f32 v249, v191, v249\n"
    "v_mfma_f32_16x16x32_bf16 v[152:155], v[236:239], v[124:127], v[152:155]\n"
    "ds_read_b64_tr_b16 v[212:213], v243 offset:16384\n"
    "v_cvt_pk_bf16_f32 v187, v190, v191\n"
    "v_mfma_f32_16x16x32_bf16 v[144:147], v[236:239], v[92:95], v[144:147]\n"
    "ds_read_b64_tr_b16 v[214:215], v243 offset:24576\n"
    "v_mfma_f32_16x16x32_bf16 v[144:147], v[232:235], v[88:91], v[144:147]\n"
    "ds_read_b64_tr_b16 v[216:217], v242 offset:16896\n"
    "v_mfma_f32_16x16x32_bf16 v[144:147], v[228:231], v[84:87], v[144:147]\n"
    "ds_read_b64_tr_b16 v[218:219], v242 offset:25088\n"
    "v_mfma_f32_16x16x32_bf16 v[144:147], v[224:227], v[80:83], v[144:147]\n"
    "ds_read_b64_tr_b16 v[220:221], v243 offset:16896\n"
    "v_mfma_f32_16x16x32_bf16 v[136:139], v[224:227], v[48:51], v[136:139]\n"
    "ds_read_b64_tr_b16 v[222:223], v243 offset:25088\n"
    "v_mfma_f32_16x16x32_bf16 v[136:139], v[228:231], v[52:55], v[136:139]\n"
    "v_mfma_f32_16x16x32_bf16 v[136:139], v[232:235], v[56:59], v[136:139]\n"
    "v_mfma_f32_16x16x32_bf16 v[136:139], v[236:239], v[60:63], v[136:139]\n"
    "v_mfma_f32_16x16x32_bf16 v[128:131], v[236:239], v[28:31], v[128:131]\n"
    "v_mfma_f32_16x16x32_bf16 v[128:131], v[232:235], v[24:27], v[128:131]\n"
    "v_mfma_f32_16x16x32_bf16 v[128:131], v[228:231], v[20:23], v[128:131]\n"
    "v_mfma_f32_16x16x32_bf16 v[128:131], v[224:227], v[16:19], v[128:131]\n"
    "s_waitcnt lgkmcnt(4)\n"
    "v_mfma_f32_16x16x32_bf16 a[0:3], v[208:211], v[160:163], a[0:3]\n"
    "ds_read_b64_tr_b16 v[224:225], v242 offset:17408\n"
    "v_mfma_f32_16x16x32_bf16 a[16:19], v[212:215], v[160:163], a[16:19]\n"
    "ds_read_b64_tr_b16 v[226:227], v242 offset:25600\n"
    "v_mfma_f32_16x16x32_bf16 a[20:23], v[212:215], v[168:171], a[20:23]\n"
    "ds_read_b64_tr_b16 v[228:229], v243 offset:17408\n"
    "v_mfma_f32_16x16x32_bf16 a[4:7], v[208:211], v[168:171], a[4:7]\n"
    "ds_read_b64_tr_b16 v[230:231], v243 offset:25600\n"
    "v_mfma_f32_16x16x32_bf16 a[8:11], v[208:211], v[176:179], a[8:11]\n"
    "ds_read_b64_tr_b16 v[232:233], v242 offset:17920\n"
    "v_mfma_f32_16x16x32_bf16 a[24:27], v[212:215], v[176:179], a[24:27]\n"
    "ds_read_b64_tr_b16 v[234:235], v242 offset:26112\n"
    "v_mfma_f32_16x16x32_bf16 a[28:31], v[212:215], v[184:187], a[28:31]\n"
    "v_exp_f32 v128, v128\n"
    "v_mfma_f32_16x16x32_bf16 a[12:15], v[208:211], v[184:187], a[12:15]\n"
    "ds_read_b64_tr_b16 v[236:237], v243 offset:17920\n"
    "s_waitcnt lgkmcnt(7)\n"
    "v_mfma_f32_16x16x32_bf16 a[44:47], v[216:219], v[184:187], a[44:47]\n"
    "ds_read_b64_tr_b16 v[238:239], v243 offset:26112\n"
    "v_add_f32 v246, v128, v246\n"
    "v_mfma_f32_16x16x32_bf16 a[60:63], v[220:223], v[184:187], a[60:63]\n"
    "ds_read_b64_tr_b16 v[208:209], v242 offset:18432\n"
    "v_mfma_f32_16x16x32_bf16 a[56:59], v[220:223], v[176:179], a[56:59]\n"
    "ds_read_b64_tr_b16 v[210:211], v242 offset:26624\n"
    "v_mfma_f32_16x16x32_bf16 a[40:43], v[216:219], v[176:179], a[40:43]\n"
    "ds_read_b64_tr_b16 v[212:213], v243 offset:18432\n"
    "v_mfma_f32_16x16x32_bf16 a[36:39], v[216:219], v[168:171], a[36:39]\n"
    "ds_read_b64_tr_b16 v[214:215], v243 offset:26624\n"
    "v_mfma_f32_16x16x32_bf16 a[52:55], v[220:223], v[168:171], a[52:55]\n"
    "v_exp_f32 v129, v129\n"
    "v_mfma_f32_16x16x32_bf16 a[48:51], v[220:223], v[160:163], a[48:51]\n"
    "v_add_f32 v246, v129, v246\n"
    "v_cvt_pk_bf16_f32 v128, v128, v129\n"
    "v_mfma_f32_16x16x32_bf16 a[32:35], v[216:219], v[160:163], a[32:35]\n"
    "s_waitcnt lgkmcnt(8)\n"
    "v_mfma_f32_16x16x32_bf16 a[64:67], v[224:227], v[160:163], a[64:67]\n"
    "ds_read_b64_tr_b16 v[216:217], v242 offset:18944\n"
    "v_mfma_f32_16x16x32_bf16 a[80:83], v[228:231], v[160:163], a[80:83]\n"
    "ds_read_b64_tr_b16 v[218:219], v242 offset:27136\n"
    "v_mfma_f32_16x16x32_bf16 a[84:87], v[228:231], v[168:171], a[84:87]\n"
    "ds_read_b64_tr_b16 v[220:221], v243 offset:18944\n"
    "v_mfma_f32_16x16x32_bf16 a[68:71], v[224:227], v[168:171], a[68:71]\n"
    "ds_read_b64_tr_b16 v[222:223], v243 offset:27136\n"
    "v_mfma_f32_16x16x32_bf16 a[72:75], v[224:227], v[176:179], a[72:75]\n"
    "v_exp_f32 v130, v130\n"
    "v_mfma_f32_16x16x32_bf16 a[88:91], v[228:231], v[176:179], a[88:91]\n"
    "v_exp_f32 v131, v131\n"
    "v_mfma_f32_16x16x32_bf16 a[92:95], v[228:231], v[184:187], a[92:95]\n"
    "v_add_f32 v246, v130, v246\n"
    "v_add_f32 v246, v131, v246\n"
    "v_mfma_f32_16x16x32_bf16 a[76:79], v[224:227], v[184:187], a[76:79]\n"
    "v_cvt_pk_bf16_f32 v129, v130, v131\n"
    "s_waitcnt lgkmcnt(8)\n"
    "v_mfma_f32_16x16x32_bf16 a[108:111], v[232:235], v[184:187], a[108:111]\n"
    "ds_read_b64_tr_b16 v[224:225], v242 offset:19456\n"
    "v_mfma_f32_16x16x32_bf16 a[124:127], v[236:239], v[184:187], a[124:127]\n"
    "ds_read_b64_tr_b16 v[226:227], v242 offset:27648\n"
    "v_mfma_f32_16x16x32_bf16 a[120:123], v[236:239], v[176:179], a[120:123]\n"
    "ds_read_b64_tr_b16 v[228:229], v243 offset:19456\n"
    "v_mfma_f32_16x16x32_bf16 a[104:107], v[232:235], v[176:179], a[104:107]\n"
    "ds_read_b64_tr_b16 v[230:231], v243 offset:27648\n"
    "v_mfma_f32_16x16x32_bf16 a[100:103], v[232:235], v[168:171], a[100:103]\n"
    "v_exp_f32 v132, v132\n"
    "v_mfma_f32_16x16x32_bf16 a[116:119], v[236:239], v[168:171], a[116:119]\n"
    "v_exp_f32 v133, v133\n"
    "v_mfma_f32_16x16x32_bf16 a[112:115], v[236:239], v[160:163], a[112:115]\n"
    "v_add_f32 v246, v132, v246\n"
    "v_add_f32 v246, v133, v246\n"
    "v_mfma_f32_16x16x32_bf16 a[96:99], v[232:235], v[160:163], a[96:99]\n"
    "v_cvt_pk_bf16_f32 v130, v132, v133\n"
    "s_waitcnt lgkmcnt(8)\n"
    "v_mfma_f32_16x16x32_bf16 a[128:131], v[208:211], v[160:163], a[128:131]\n"
    "ds_read_b64_tr_b16 v[232:233], v242 offset:19968\n"
    "v_mfma_f32_16x16x32_bf16 a[144:147], v[212:215], v[160:163], a[144:147]\n"
    "ds_read_b64_tr_b16 v[234:235], v242 offset:28160\n"
    "v_mfma_f32_16x16x32_bf16 a[148:151], v[212:215], v[168:171], a[148:151]\n"
    "ds_read_b64_tr_b16 v[236:237], v243 offset:19968\n"
    "v_mfma_f32_16x16x32_bf16 a[132:135], v[208:211], v[168:171], a[132:135]\n"
    "ds_read_b64_tr_b16 v[238:239], v243 offset:28160\n"
    "v_mfma_f32_16x16x32_bf16 a[136:139], v[208:211], v[176:179], a[136:139]\n"
    "v_exp_f32 v134, v134\n"
    "v_mfma_f32_16x16x32_bf16 a[152:155], v[212:215], v[176:179], a[152:155]\n"
    "v_add_f32 v246, v134, v246\n"
    "v_mfma_f32_16x16x32_bf16 a[156:159], v[212:215], v[184:187], a[156:159]\n"
    "s_waitcnt vmcnt(12)\n"
    "s_barrier\n"
    "v_mfma_f32_16x16x32_bf16 a[140:143], v[208:211], v[184:187], a[140:143]\n"
    "s_waitcnt lgkmcnt(8)\n"
    "v_mfma_f32_16x16x32_bf16 a[172:175], v[216:219], v[184:187], a[172:175]\n"
    "ds_read_b128 v[208:211], v240 offset:49152\n"
    "v_mfma_f32_16x16x32_bf16 a[188:191], v[220:223], v[184:187], a[188:191]\n"
    "ds_read_b128 v[212:215], v241 offset:49152\n"
    "v_mfma_f32_16x16x32_bf16 a[184:187], v[220:223], v[176:179], a[184:187]\n"
    "s_add_u32 m0, s59, 0x0\n"
    "v_mfma_f32_16x16x32_bf16 a[168:171], v[216:219], v[176:179], a[168:171]\n"
    "buffer_load_dwordx4 v245, s[44:47], s56 offen offset:0 nt lds\n"
    "v_mfma_f32_16x16x32_bf16 a[164:167], v[216:219], v[168:171], a[164:167]\n"
    "v_exp_f32 v135, v135\n"
    "v_mfma_f32_16x16x32_bf16 a[180:183], v[220:223], v[168:171], a[180:183]\n"
    "v_exp_f32 v136, v136\n"
    "v_mfma_f32_16x16x32_bf16 a[176:179], v[220:223], v[160:163], a[176:179]\n"
    "v_add_f32 v246, v135, v246\n"
    "v_cvt_pk_bf16_f32 v131, v134, v135\n"
    "v_mfma_f32_16x16x32_bf16 a[160:163], v[216:219], v[160:163], a[160:163]\n"
    "v_add_f32 v247, v136, v247\n"
    "s_waitcnt lgkmcnt(6)\n"
    "v_mfma_f32_16x16x32_bf16 a[192:195], v[224:227], v[160:163], a[192:195]\n"
    "ds_read_b128 v[216:219], v240 offset:53248\n"
    "v_mfma_f32_16x16x32_bf16 a[208:211], v[228:231], v[160:163], a[208:211]\n"
    "ds_read_b128 v[220:223], v241 offset:53248\n"
    "v_mfma_f32_16x16x32_bf16 a[212:215], v[228:231], v[168:171], a[212:215]\n"
    "s_add_u32 m0, s59, 0x380\n"
    "v_mfma_f32_16x16x32_bf16 a[196:199], v[224:227], v[168:171], a[196:199]\n"
    "buffer_load_dwordx4 v245, s[44:47], s56 offen offset:128 nt lds\n"
    "v_mfma_f32_16x16x32_bf16 a[200:203], v[224:227], v[176:179], a[200:203]\n"
    "v_exp_f32 v137, v137\n"
    "v_mfma_f32_16x16x32_bf16 a[216:219], v[228:231], v[176:179], a[216:219]\n"
    "v_exp_f32 v138, v138\n"
    "v_mfma_f32_16x16x32_bf16 a[220:223], v[228:231], v[184:187], a[220:223]\n"
    "v_add_f32 v247, v137, v247\n"
    "v_cvt_pk_bf16_f32 v136, v136, v137\n"
    "v_mfma_f32_16x16x32_bf16 a[204:207], v[224:227], v[184:187], a[204:207]\n"
    "v_add_f32 v247, v138, v247\n"
    "s_waitcnt lgkmcnt(4)\n"
    "v_mfma_f32_16x16x32_bf16 a[236:239], v[232:235], v[184:187], a[236:239]\n"
    "ds_read_b128 v[224:227], v240 offset:51200\n"
    "v_mfma_f32_16x16x32_bf16 a[252:255], v[236:239], v[184:187], a[252:255]\n"
    "ds_read_b128 v[228:231], v241 offset:51200\n"
    "v_mfma_f32_16x16x32_bf16 a[248:251], v[236:239], v[176:179], a[248:251]\n"
    "s_add_u32 m0, s59, 0x700\n"
    "v_mfma_f32_16x16x32_bf16 a[232:235], v[232:235], v[176:179], a[232:235]\n"
    "buffer_load_dwordx4 v245, s[44:47], s56 offen offset:256 nt lds\n"
    "v_mfma_f32_16x16x32_bf16 a[228:231], v[232:235], v[168:171], a[228:231]\n"
    "v_exp_f32 v139, v139\n"
    "v_mfma_f32_16x16x32_bf16 a[244:247], v[236:239], v[168:171], a[244:247]\n"
    "v_add_f32 v247, v139, v247\n"
    "v_cvt_pk_bf16_f32 v137, v138, v139\n"
    "v_mfma_f32_16x16x32_bf16 a[240:243], v[236:239], v[160:163], a[240:243]\n"
    "v_exp_f32 v140, v140\n"
    "v_mfma_f32_16x16x32_bf16 a[224:227], v[232:235], v[160:163], a[224:227]\n"
    "v_add_f32 v247, v140, v247\n"
    "s_waitcnt lgkmcnt(4)\n"
    "v_mfma_f32_16x16x32_bf16 v[160:163], v[208:211], v[0:3], v[192:195]\n"
    "v_exp_f32 v141, v141\n"
    "v_mfma_f32_16x16x32_bf16 v[160:163], v[212:215], v[4:7], v[160:163]\n"
    "ds_read_b128 v[232:235], v240 offset:55296\n"
    "s_waitcnt lgkmcnt(3)\n"
    "v_mfma_f32_16x16x32_bf16 v[160:163], v[216:219], v[8:11], v[160:163]\n"
    "ds_read_b128 v[236:239], v241 offset:55296\n"
    "s_add_u32 m0, s59, 0xa80\n"
    "v_mfma_f32_16x16x32_bf16 v[160:163], v[220:223], v[12:15], v[160:163]\n"
    "buffer_load_dwordx4 v245, s[44:47], s56 offen offset:384 nt lds\n"
    "v_add_f32 v247, v141, v247\n"
    "v_mfma_f32_16x16x32_bf16 v[168:171], v[220:223], v[44:47], v[196:199]\n"
    "v_cvt_pk_bf16_f32 v138, v140, v141\n"
    "v_mfma_f32_16x16x32_bf16 v[168:171], v[216:219], v[40:43], v[168:171]\n"
    "v_exp_f32 v142, v142\n"
    "v_mfma_f32_16x16x32_bf16 v[168:171], v[212:215], v[36:39], v[168:171]\n"
    "v_exp_f32 v143, v143\n"
    "v_mfma_f32_16x16x32_bf16 v[168:171], v[208:211], v[32:35], v[168:171]\n"
    "v_add_f32 v247, v142, v247\n"
    "v_add_f32 v247, v143, v247\n"
    "v_mfma_f32_16x16x32_bf16 v[176:179], v[208:211], v[64:67], v[200:203]\n"
    "v_exp_f32 v144, v144\n"
    "v_mfma_f32_16x16x32_bf16 v[176:179], v[212:215], v[68:71], v[176:179]\n"
    "v_cvt_pk_bf16_f32 v139, v142, v143\n"
    "v_add_f32 v248, v144, v248\n"
    "v_mfma_f32_16x16x32_bf16 v[176:179], v[216:219], v[72:75], v[176:179]\n"
    "s_add_u32 m0, s58, 0x8000\n"
    "v_mfma_f32_16x16x32_bf16 v[176:179], v[220:223], v[76:79], v[176:179]\n"
    "buffer_load_dwordx4 v244, s[40:43], s56 offen offset:0 nt lds\n"
    "v_mfma_f32_16x16x32_bf16 v[184:187], v[220:223], v[108:111], v[204:207]\n"
    "v_exp_f32 v145, v145\n"
    "v_mfma_f32_16x16x32_bf16 v[184:187], v[216:219], v[104:107], v[184:187]\n"
    "v_exp_f32 v146, v146\n"
    "v_mfma_f32_16x16x32_bf16 v[184:187], v[212:215], v[100:103], v[184:187]\n"
    "v_add_f32 v248, v145, v248\n"
    "v_cvt_pk_bf16_f32 v144, v144, v145\n"
    "v_mfma_f32_16x16x32_bf16 v[184:187], v[208:211], v[96:99], v[184:187]\n"
    "v_add_f32 v248, v146, v248\n"
    "s_waitcnt lgkmcnt(2)\n"
    "v_mfma_f32_16x16x32_bf16 v[188:191], v[224:227], v[96:99], v[204:207]\n"
    "v_exp_f32 v147, v147\n"
    "v_mfma_f32_16x16x32_bf16 v[188:191], v[228:231], v[100:103], v[188:191]\n"
    "ds_read_b128 v[208:211], v240 offset:59392\n"
    "s_waitcnt lgkmcnt(1)\n"
    "v_mfma_f32_16x16x32_bf16 v[188:191], v[232:235], v[104:107], v[188:191]\n"
    "ds_read_b128 v[212:215], v241 offset:59392\n"
    "s_add_u32 m0, s58, 0x8f80\n"
    "v_mfma_f32_16x16x32_bf16 v[188:191], v[236:239], v[108:111], v[188:191]\n"
    "ds_read_b128 v[216:219], v240 offset:63488\n"
    "buffer_load_dwordx4 v244, s[40:43], s56 offen offset:128 nt lds\n"
    "v_mfma_f32_16x16x32_bf16 v[180:183], v[236:239], v[76:79], v[200:203]\n"
    "ds_read_b128 v[220:223], v241 offset:63488\n"
    "v_add_f32 v248, v147, v248\n"
    "v_mfma_f32_16x16x32_bf16 v[180:183], v[232:235], v[72:75], v[180:183]\n"
    "v_exp_f32 v148, v148\n"
    "v_mfma_f32_16x16x32_bf16 v[180:183], v[228:231], v[68:71], v[180:183]\n"
    "v_cvt_pk_bf16_f32 v145, v146, v147\n"
    "v_add_f32 v248, v148, v248\n"
    "v_mfma_f32_16x16x32_bf16 v[180:183], v[224:227], v[64:67], v[180:183]\n"
    "v_exp_f32 v149, v149\n"
    "v_mfma_f32_16x16x32_bf16 v[172:175], v[224:227], v[32:35], v[196:199]\n"
    "v_add_f32 v248, v149, v248\n"
    "v_cvt_pk_bf16_f32 v146, v148, v149\n"
    "v_mfma_f32_16x16x32_bf16 v[172:175], v[228:231], v[36:39], v[172:175]\n"
    "v_exp_f32 v150, v150\n"
    "v_mfma_f32_16x16x32_bf16 v[172:175], v[232:235], v[40:43], v[172:175]\n"
    "s_add_u32 m0, s58, 0x9f00\n"
    "v_add_f32 v248, v150, v248\n"
    "v_mfma_f32_16x16x32_bf16 v[172:175], v[236:239], v[44:47], v[172:175]\n"
    "buffer_load_dwordx4 v244, s[40:43], s56 offen offset:256 nt lds\n"
    "v_mfma_f32_16x16x32_bf16 v[164:167], v[236:239], v[12:15], v[192:195]\n"
    "v_exp_f32 v151, v151\n"
    "v_mfma_f32_16x16x32_bf16 v[164:167], v[232:235], v[8:11], v[164:167]\n"
    "v_add_f32 v248, v151, v248\n"
    "v_cvt_pk_bf16_f32 v147, v150, v151\n"
    "v_mfma_f32_16x16x32_bf16 v[164:167], v[228:231], v[4:7], v[164:167]\n"
    "v_exp_f32 v152, v152\n"
    "v_mfma_f32_16x16x32_bf16 v[164:167], v[224:227], v[0:3], v[164:167]\n"
    "v_add_f32 v249, v152, v249\n"
    "s_waitcnt lgkmcnt(2)\n"
    "v_mfma_f32_16x16x32_bf16 v[164:167], v[208:211], v[16:19], v[164:167]\n"
    "v_exp_f32 v153, v153\n"
    "v_mfma_f32_16x16x32_bf16 v[164:167], v[212:215], v[20:23], v[164:167]\n"
    "ds_read_b128 v[224:227], v240 offset:57344\n"
    "s_waitcnt lgkmcnt(1)\n"
    "v_mfma_f32_16x16x32_bf16 v[164:167], v[216:219], v[24:27], v[164:167]\n"
    "ds_read_b128 v[228:231], v241 offset:57344\n"
    "s_add_u32 m0, s58, 0xae80\n"
    "v_mfma_f32_16x16x32_bf16 v[164:167], v[220:223], v[28:31], v[164:167]\n"
    "ds_read_b128 v[232:235], v240 offset:61440\n"
    "buffer_load_dwordx4 v244, s[40:43], s56 offen offset:384 nt lds\n"
    "v_mfma_f32_16x16x32_bf16 v[172:175], v[220:223], v[60:63], v[172:175]\n"
    "ds_read_b128 v[236:239], v241 offset:61440\n"
    "v_add_f32 v249, v153, v249\n"
    "v_mfma_f32_16x16x32_bf16 v[172:175], v[216:219], v[56:59], v[172:175]\n"
    "v_cvt_pk_bf16_f32 v152, v152, v153\n"
    "v_mfma_f32_16x16x32_bf16 v[172:175], v[212:215], v[52:55], v[172:175]\n"
    "v_exp_f32 v154, v154\n"
    "v_mfma_f32_16x16x32_bf16 v[172:175], v[208:211], v[48:51], v[172:175]\n"
    "v_add_f32 v249, v154, v249\n"
    "v_mfma_f32_16x16x32_bf16 v[180:183], v[208:211], v[80:83], v[180:183]\n"
    "v_exp_f32 v155, v155\n"
    "v_mfma_f32_16x16x32_bf16 v[180:183], v[212:215], v[84:87], v[180:183]\n"
    "v_exp_f32 v156, v156\n"
    "v_mfma_f32_16x16x32_bf16 v[180:183], v[216:219], v[88:91], v[180:183]\n"
    "v_add_f32 v249, v155, v249\n"
    "v_cvt_pk_bf16_f32 v153, v154, v155\n"
    "v_mfma_f32_16x16x32_bf16 v[180:183], v[220:223], v[92:95], v[180:183]\n"
    "v_exp_f32 v157, v157\n"
    "v_mfma_f32_16x16x32_bf16 v[188:191], v[220:223], v[124:127], v[188:191]\n"
    "v_exp_f32 v158, v158\n"
    "v_mfma_f32_16x16x32_bf16 v[188:191], v[216:219], v[120:123], v[188:191]\n"
    "v_add_f32 v249, v156, v249\n"
    "v_add_f32 v249, v157, v249\n"
    "v_mfma_f32_16x16x32_bf16 v[188:191], v[212:215], v[116:119], v[188:191]\n"
    "v_exp_f32 v159, v159\n"
    "v_mfma_f32_16x16x32_bf16 v[188:191], v[208:211], v[112:115], v[188:191]\n"
    "s_add_u32 s56, s56, 0x4000\n"
    "s_waitcnt lgkmcnt(2)\n"
    "v_mfma_f32_16x16x32_bf16 v[184:187], v[224:227], v[112:115], v[184:187]\n"
    "v_cvt_pk_bf16_f32 v154, v156, v157\n"
    "v_add_f32 v249, v158, v249\n"
    "v_mfma_f32_16x16x32_bf16 v[184:187], v[228:231], v[116:119], v[184:187]\n"
    "ds_read_b64_tr_b16 v[208:209], v242 offset:32768\n"
    "s_waitcnt lgkmcnt(1)\n"
    "v_mfma_f32_16x16x32_bf16 v[184:187], v[232:235], v[120:123], v[184:187]\n"
    "ds_read_b64_tr_b16 v[210:211], v242 offset:40960\n"
    "v_add_f32 v249, v159, v249\n"
    "v_mfma_f32_16x16x32_bf16 v[184:187], v[236:239], v[124:127], v[184:187]\n"
    "ds_read_b64_tr_b16 v[212:213], v243 offset:32768\n"
    "v_cvt_pk_bf16_f32 v155, v158, v159\n"
    "v_mfma_f32_16x16x32_bf16 v[176:179], v[236:239], v[92:95], v[176:179]\n"
    "ds_read_b64_tr_b16 v[214:215], v243 offset:40960\n"
    "v_mfma_f32_16x16x32_bf16 v[176:179], v[232:235], v[88:91], v[176:179]\n"
    "ds_read_b64_tr_b16 v[216:217], v242 offset:33280\n"
    "v_mfma_f32_16x16x32_bf16 v[176:179], v[228:231], v[84:87], v[176:179]\n"
    "ds_read_b64_tr_b16 v[218:219], v242 offset:41472\n"
    "v_mfma_f32_16x16x32_bf16 v[176:179], v[224:227], v[80:83], v[176:179]\n"
    "ds_read_b64_tr_b16 v[220:221], v243 offset:33280\n"
    "v_mfma_f32_16x16x32_bf16 v[168:171], v[224:227], v[48:51], v[168:171]\n"
    "ds_read_b64_tr_b16 v[222:223], v243 offset:41472\n"
    "v_mfma_f32_16x16x32_bf16 v[168:171], v[228:231], v[52:55], v[168:171]\n"
    "v_mfma_f32_16x16x32_bf16 v[168:171], v[232:235], v[56:59], v[168:171]\n"
    "v_mfma_f32_16x16x32_bf16 v[168:171], v[236:239], v[60:63], v[168:171]\n"
    "v_mfma_f32_16x16x32_bf16 v[160:163], v[236:239], v[28:31], v[160:163]\n"
    "v_mfma_f32_16x16x32_bf16 v[160:163], v[232:235], v[24:27], v[160:163]\n"
    "v_mfma_f32_16x16x32_bf16 v[160:163], v[228:231], v[20:23], v[160:163]\n"
    "v_mfma_f32_16x16x32_bf16 v[160:163], v[224:227], v[16:19], v[160:163]\n"
    "s_waitcnt lgkmcnt(4)\n"
    "v_mfma_f32_16x16x32_bf16 a[0:3], v[208:211], v[128:131], a[0:3]\n"
    "ds_read_b64_tr_b16 v[224:225], v242 offset:33792\n"
    "v_mfma_f32_16x16x32_bf16 a[16:19], v[212:215], v[128:131], a[16:19]\n"
    "ds_read_b64_tr_b16 v[226:227], v242 offset:41984\n"
    "v_mfma_f32_16x16x32_bf16 a[20:23], v[212:215], v[136:139], a[20:23]\n"
    "ds_read_b64_tr_b16 v[228:229], v243 offset:33792\n"
    "v_mfma_f32_16x16x32_bf16 a[4:7], v[208:211], v[136:139], a[4:7]\n"
    "ds_read_b64_tr_b16 v[230:231], v243 offset:41984\n"
    "v_mfma_f32_16x16x32_bf16 a[8:11], v[208:211], v[144:147], a[8:11]\n"
    "ds_read_b64_tr_b16 v[232:233], v242 offset:34304\n"
    "v_mfma_f32_16x16x32_bf16 a[24:27], v[212:215], v[144:147], a[24:27]\n"
    "ds_read_b64_tr_b16 v[234:235], v242 offset:42496\n"
    "v_mfma_f32_16x16x32_bf16 a[28:31], v[212:215], v[152:155], a[28:31]\n"
    "v_exp_f32 v160, v160\n"
    "v_mfma_f32_16x16x32_bf16 a[12:15], v[208:211], v[152:155], a[12:15]\n"
    "ds_read_b64_tr_b16 v[236:237], v243 offset:34304\n"
    "s_waitcnt lgkmcnt(7)\n"
    "v_mfma_f32_16x16x32_bf16 a[44:47], v[216:219], v[152:155], a[44:47]\n"
    "ds_read_b64_tr_b16 v[238:239], v243 offset:42496\n"
    "v_add_f32 v246, v160, v246\n"
    "v_mfma_f32_16x16x32_bf16 a[60:63], v[220:223], v[152:155], a[60:63]\n"
    "ds_read_b64_tr_b16 v[208:209], v242 offset:34816\n"
    "v_mfma_f32_16x16x32_bf16 a[56:59], v[220:223], v[144:147], a[56:59]\n"
    "ds_read_b64_tr_b16 v[210:211], v242 offset:43008\n"
    "v_mfma_f32_16x16x32_bf16 a[40:43], v[216:219], v[144:147], a[40:43]\n"
    "ds_read_b64_tr_b16 v[212:213], v243 offset:34816\n"
    "v_mfma_f32_16x16x32_bf16 a[36:39], v[216:219], v[136:139], a[36:39]\n"
    "ds_read_b64_tr_b16 v[214:215], v243 offset:43008\n"
    "v_mfma_f32_16x16x32_bf16 a[52:55], v[220:223], v[136:139], a[52:55]\n"
    "v_exp_f32 v161, v161\n"
    "v_mfma_f32_16x16x32_bf16 a[48:51], v[220:223], v[128:131], a[48:51]\n"
    "v_add_f32 v246, v161, v246\n"
    "v_cvt_pk_bf16_f32 v160, v160, v161\n"
    "v_mfma_f32_16x16x32_bf16 a[32:35], v[216:219], v[128:131], a[32:35]\n"
    "s_waitcnt lgkmcnt(8)\n"
    "v_mfma_f32_16x16x32_bf16 a[64:67], v[224:227], v[128:131], a[64:67]\n"
    "ds_read_b64_tr_b16 v[216:217], v242 offset:35328\n"
    "v_mfma_f32_16x16x32_bf16 a[80:83], v[228:231], v[128:131], a[80:83]\n"
    "ds_read_b64_tr_b16 v[218:219], v242 offset:43520\n"
    "v_mfma_f32_16x16x32_bf16 a[84:87], v[228:231], v[136:139], a[84:87]\n"
    "ds_read_b64_tr_b16 v[220:221], v243 offset:35328\n"
    "v_mfma_f32_16x16x32_bf16 a[68:71], v[224:227], v[136:139], a[68:71]\n"
    "ds_read_b64_tr_b16 v[222:223], v243 offset:43520\n"
    "v_mfma_f32_16x16x32_bf16 a[72:75], v[224:227], v[144:147], a[72:75]\n"
    "v_exp_f32 v162, v162\n"
    "v_mfma_f32_16x16x32_bf16 a[88:91], v[228:231], v[144:147], a[88:91]\n"
    "v_exp_f32 v163, v163\n"
    "v_mfma_f32_16x16x32_bf16 a[92:95], v[228:231], v[152:155], a[92:95]\n"
    "v_add_f32 v246, v162, v246\n"
    "v_add_f32 v246, v163, v246\n"
    "v_mfma_f32_16x16x32_bf16 a[76:79], v[224:227], v[152:155], a[76:79]\n"
    "v_cvt_pk_bf16_f32 v161, v162, v163\n"
    "s_waitcnt lgkmcnt(8)\n"
    "v_mfma_f32_16x16x32_bf16 a[108:111], v[232:235], v[152:155], a[108:111]\n"
    "ds_read_b64_tr_b16 v[224:225], v242 offset:35840\n"
    "v_mfma_f32_16x16x32_bf16 a[124:127], v[236:239], v[152:155], a[124:127]\n"
    "ds_read_b64_tr_b16 v[226:227], v242 offset:44032\n"
    "v_mfma_f32_16x16x32_bf16 a[120:123], v[236:239], v[144:147], a[120:123]\n"
    "ds_read_b64_tr_b16 v[228:229], v243 offset:35840\n"
    "v_mfma_f32_16x16x32_bf16 a[104:107], v[232:235], v[144:147], a[104:107]\n"
    "ds_read_b64_tr_b16 v[230:231], v243 offset:44032\n"
    "v_mfma_f32_16x16x32_bf16 a[100:103], v[232:235], v[136:139], a[100:103]\n"
    "v_exp_f32 v164, v164\n"
    "v_mfma_f32_16x16x32_bf16 a[116:119], v[236:239], v[136:139], a[116:119]\n"
    "v_exp_f32 v165, v165\n"
    "v_mfma_f32_16x16x32_bf16 a[112:115], v[236:239], v[128:131], a[112:115]\n"
    "v_add_f32 v246, v164, v246\n"
    "v_add_f32 v246, v165, v246\n"
    "v_mfma_f32_16x16x32_bf16 a[96:99], v[232:235], v[128:131], a[96:99]\n"
    "v_cvt_pk_bf16_f32 v162, v164, v165\n"
    "s_waitcnt lgkmcnt(8)\n"
    "v_mfma_f32_16x16x32_bf16 a[128:131], v[208:211], v[128:131], a[128:131]\n"
    "ds_read_b64_tr_b16 v[232:233], v242 offset:36352\n"
    "v_mfma_f32_16x16x32_bf16 a[144:147], v[212:215], v[128:131], a[144:147]\n"
    "ds_read_b64_tr_b16 v[234:235], v242 offset:44544\n"
    "v_mfma_f32_16x16x32_bf16 a[148:151], v[212:215], v[136:139], a[148:151]\n"
    "ds_read_b64_tr_b16 v[236:237], v243 offset:36352\n"
    "v_mfma_f32_16x16x32_bf16 a[132:135], v[208:211], v[136:139], a[132:135]\n"
    "ds_read_b64_tr_b16 v[238:239], v243 offset:44544\n"
    "v_mfma_f32_16x16x32_bf16 a[136:139], v[208:211], v[144:147], a[136:139]\n"
    "v_exp_f32 v166, v166\n"
    "v_mfma_f32_16x16x32_bf16 a[152:155], v[212:215], v[144:147], a[152:155]\n"
    "v_add_f32 v246, v166, v246\n"
    "v_mfma_f32_16x16x32_bf16 a[156:159], v[212:215], v[152:155], a[156:159]\n"
    "s_waitcnt vmcnt(12)\n"
    "s_barrier\n"
    "v_mfma_f32_16x16x32_bf16 a[140:143], v[208:211], v[152:155], a[140:143]\n"
    "s_waitcnt lgkmcnt(8)\n"
    "v_mfma_f32_16x16x32_bf16 a[172:175], v[216:219], v[152:155], a[172:175]\n"
    "ds_read_b128 v[208:211], v240 offset:0\n"
    "v_mfma_f32_16x16x32_bf16 a[188:191], v[220:223], v[152:155], a[188:191]\n"
    "ds_read_b128 v[212:215], v241 offset:0\n"
    "v_mfma_f32_16x16x32_bf16 a[184:187], v[220:223], v[144:147], a[184:187]\n"
    "s_add_u32 m0, s59, 0x4000\n"
    "v_mfma_f32_16x16x32_bf16 a[168:171], v[216:219], v[144:147], a[168:171]\n"
    "buffer_load_dwordx4 v245, s[44:47], s56 offen offset:0 nt lds\n"
    "v_mfma_f32_16x16x32_bf16 a[164:167], v[216:219], v[136:139], a[164:167]\n"
    "v_exp_f32 v167, v167\n"
    "v_mfma_f32_16x16x32_bf16 a[180:183], v[220:223], v[136:139], a[180:183]\n"
    "v_exp_f32 v168, v168\n"
    "v_mfma_f32_16x16x32_bf16 a[176:179], v[220:223], v[128:131], a[176:179]\n"
    "v_add_f32 v246, v167, v246\n"
    "v_cvt_pk_bf16_f32 v163, v166, v167\n"
    "v_mfma_f32_16x16x32_bf16 a[160:163], v[216:219], v[128:131], a[160:163]\n"
    "v_add_f32 v247, v168, v247\n"
    "s_waitcnt lgkmcnt(6)\n"
    "v_mfma_f32_16x16x32_bf16 a[192:195], v[224:227], v[128:131], a[192:195]\n"
    "ds_read_b128 v[216:219], v240 offset:4096\n"
    "v_mfma_f32_16x16x32_bf16 a[208:211], v[228:231], v[128:131], a[208:211]\n"
    "ds_read_b128 v[220:223], v241 offset:4096\n"
    "v_mfma_f32_16x16x32_bf16 a[212:215], v[228:231], v[136:139], a[212:215]\n"
    "s_add_u32 m0, s59, 0x4380\n"
    "v_mfma_f32_16x16x32_bf16 a[196:199], v[224:227], v[136:139], a[196:199]\n"
    "buffer_load_dwordx4 v245, s[44:47], s56 offen offset:128 nt lds\n"
    "v_mfma_f32_16x16x32_bf16 a[200:203], v[224:227], v[144:147], a[200:203]\n"
    "v_exp_f32 v169, v169\n"
    "v_mfma_f32_16x16x32_bf16 a[216:219], v[228:231], v[144:147], a[216:219]\n"
    "v_exp_f32 v170, v170\n"
    "v_mfma_f32_16x16x32_bf16 a[220:223], v[228:231], v[152:155], a[220:223]\n"
    "v_add_f32 v247, v169, v247\n"
    "v_cvt_pk_bf16_f32 v168, v168, v169\n"
    "v_mfma_f32_16x16x32_bf16 a[204:207], v[224:227], v[152:155], a[204:207]\n"
    "v_add_f32 v247, v170, v247\n"
    "s_waitcnt lgkmcnt(4)\n"
    "v_mfma_f32_16x16x32_bf16 a[236:239], v[232:235], v[152:155], a[236:239]\n"
    "ds_read_b128 v[224:227], v240 offset:2048\n"
    "v_mfma_f32_16x16x32_bf16 a[252:255], v[236:239], v[152:155], a[252:255]\n"
    "ds_read_b128 v[228:231], v241 offset:2048\n"
    "v_mfma_f32_16x16x32_bf16 a[248:251], v[236:239], v[144:147], a[248:251]\n"
    "s_add_u32 m0, s59, 0x4700\n"
    "v_mfma_f32_16x16x32_bf16 a[232:235], v[232:235], v[144:147], a[232:235]\n"
    "buffer_load_dwordx4 v245, s[44:47], s56 offen offset:256 nt lds\n"
    "v_mfma_f32_16x16x32_bf16 a[228:231], v[232:235], v[136:139], a[228:231]\n"
    "v_exp_f32 v171, v171\n"
    "v_mfma_f32_16x16x32_bf16 a[244:247], v[236:239], v[136:139], a[244:247]\n"
    "v_add_f32 v247, v171, v247\n"
    "v_cvt_pk_bf16_f32 v169, v170, v171\n"
    "v_mfma_f32_16x16x32_bf16 a[240:243], v[236:239], v[128:131], a[240:243]\n"
    "v_exp_f32 v172, v172\n"
    "v_mfma_f32_16x16x32_bf16 a[224:227], v[232:235], v[128:131], a[224:227]\n"
    "v_add_f32 v247, v172, v247\n"
    "s_waitcnt lgkmcnt(4)\n"
    "s_sub_u32 s60, s60, 1\n"
    "s_cbranch_scc1 L_tail_%=\n"
    "v_mfma_f32_16x16x32_bf16 v[128:131], v[208:211], v[0:3], v[192:195]\n"
    "v_exp_f32 v173, v173\n"
    "v_mfma_f32_16x16x32_bf16 v[128:131], v[212:215], v[4:7], v[128:131]\n"
    "ds_read_b128 v[232:235], v240 offset:6144\n"
    "s_waitcnt lgkmcnt(3)\n"
    "v_mfma_f32_16x16x32_bf16 v[128:131], v[216:219], v[8:11], v[128:131]\n"
    "ds_read_b128 v[236:239], v241 offset:6144\n"
    "s_add_u32 m0, s59, 0x4a80\n"
    "v_mfma_f32_16x16x32_bf16 v[128:131], v[220:223], v[12:15], v[128:131]\n"
    "buffer_load_dwordx4 v245, s[44:47], s56 offen offset:384 nt lds\n"
    "v_add_f32 v247, v173, v247\n"
    "v_mfma_f32_16x16x32_bf16 v[136:139], v[220:223], v[44:47], v[196:199]\n"
    "v_cvt_pk_bf16_f32 v170, v172, v173\n"
    "v_mfma_f32_16x16x32_bf16 v[136:139], v[216:219], v[40:43], v[136:139]\n"
    "v_exp_f32 v174, v174\n"
    "v_mfma_f32_16x16x32_bf16 v[136:139], v[212:215], v[36:39], v[136:139]\n"
    "v_exp_f32 v175, v175\n"
    "v_mfma_f32_16x16x32_bf16 v[136:139], v[208:211], v[32:35], v[136:139]\n"
    "v_add_f32 v247, v174, v247\n"
    "v_add_f32 v247, v175, v247\n"
    "v_mfma_f32_16x16x32_bf16 v[144:147], v[208:211], v[64:67], v[200:203]\n"
    "v_exp_f32 v176, v176\n"
    "v_mfma_f32_16x16x32_bf16 v[144:147], v[212:215], v[68:71], v[144:147]\n"
    "v_cvt_pk_bf16_f32 v171, v174, v175\n"
    "v_add_f32 v248, v176, v248\n"
    "v_mfma_f32_16x16x32_bf16 v[144:147], v[216:219], v[72:75], v[144:147]\n"
    "s_add_u32 m0, s58, 0xc000\n"
    "v_mfma_f32_16x16x32_bf16 v[144:147], v[220:223], v[76:79], v[144:147]\n"
    "buffer_load_dwordx4 v244, s[40:43], s56 offen offset:0 nt lds\n"
    "v_mfma_f32_16x16x32_bf16 v[152:155], v[220:223], v[108:111], v[204:207]\n"
    "v_exp_f32 v177, v177\n"
    "v_mfma_f32_16x16x32_bf16 v[152:155], v[216:219], v[104:107], v[152:155]\n"
    "v_exp_f32 v178, v178\n"
    "v_mfma_f32_16x16x32_bf16 v[152:155], v[212:215], v[100:103], v[152:155]\n"
    "v_add_f32 v248, v177, v248\n"
    "v_cvt_pk_bf16_f32 v176, v176, v177\n"
    "v_mfma_f32_16x16x32_bf16 v[152:155], v[208:211], v[96:99], v[152:155]\n"
    "v_add_f32 v248, v178, v248\n"
    "s_waitcnt lgkmcnt(2)\n"
    "v_mfma_f32_16x16x32_bf16 v[156:159], v[224:227], v[96:99], v[204:207]\n"
    "v_exp_f32 v179, v179\n"
    "v_mfma_f32_16x16x32_bf16 v[156:159], v[228:231], v[100:103], v[156:159]\n"
    "ds_read_b128 v[208:211], v240 offset:10240\n"
    "s_waitcnt lgkmcnt(1)\n"
    "v_mfma_f32_16x16x32_bf16 v[156:159], v[232:235], v[104:107], v[156:159]\n"
    "ds_read_b128 v[212:215], v241 offset:10240\n"
    "s_add_u32 m0, s58, 0xcf80\n"
    "v_mfma_f32_16x16x32_bf16 v[156:159], v[236:239], v[108:111], v[156:159]\n"
    "ds_read_b128 v[216:219], v240 offset:14336\n"
    "buffer_load_dwordx4 v244, s[40:43], s56 offen offset:128 nt lds\n"
    "v_mfma_f32_16x16x32_bf16 v[148:151], v[236:239], v[76:79], v[200:203]\n"
    "ds_read_b128 v[220:223], v241 offset:14336\n"
    "v_add_f32 v248, v179, v248\n"
    "v_mfma_f32_16x16x32_bf16 v[148:151], v[232:235], v[72:75], v[148:151]\n"
    "v_exp_f32 v180, v180\n"
    "v_mfma_f32_16x16x32_bf16 v[148:151], v[228:231], v[68:71], v[148:151]\n"
    "v_cvt_pk_bf16_f32 v177, v178, v179\n"
    "v_add_f32 v248, v180, v248\n"
    "v_mfma_f32_16x16x32_bf16 v[148:151], v[224:227], v[64:67], v[148:151]\n"
    "v_exp_f32 v181, v181\n"
    "v_mfma_f32_16x16x32_bf16 v[140:143], v[224:227], v[32:35], v[196:199]\n"
    "v_add_f32 v248, v181, v248\n"
    "v_cvt_pk_bf16_f32 v178, v180, v181\n"
    "v_mfma_f32_16x16x32_bf16 v[140:143], v[228:231], v[36:39], v[140:143]\n"
    "v_exp_f32 v182, v182\n"
    "v_mfma_f32_16x16x32_bf16 v[140:143], v[232:235], v[40:43], v[140:143]\n"
    "s_add_u32 m0, s58, 0xdf00\n"
    "v_add_f32 v248, v182, v248\n"
    "v_mfma_f32_16x16x32_bf16 v[140:143], v[236:239], v[44:47], v[140:143]\n"
    "buffer_load_dwordx4 v244, s[40:43], s56 offen offset:256 nt lds\n"
    "v_mfma_f32_16x16x32_bf16 v[132:135], v[236:239], v[12:15], v[192:195]\n"
    "v_exp_f32 v183, v183\n"
    "v_mfma_f32_16x16x32_bf16 v[132:135], v[232:235], v[8:11], v[132:135]\n"
    "v_add_f32 v248, v183, v248\n"
    "v_cvt_pk_bf16_f32 v179, v182, v183\n"
    "v_mfma_f32_16x16x32_bf16 v[132:135], v[228:231], v[4:7], v[132:135]\n"
    "v_exp_f32 v184, v184\n"
    "v_mfma_f32_16x16x32_bf16 v[132:135], v[224:227], v[0:3], v[132:135]\n"
    "v_add_f32 v249, v184, v249\n"
    "s_waitcnt lgkmcnt(2)\n"
    "v_mfma_f32_16x16x32_bf16 v[132:135], v[208:211], v[16:19], v[132:135]\n"
    "v_exp_f32 v185, v185\n"
    "v_mfma_f32_16x16x32_bf16 v[132:135], v[212:215], v[20:23], v[132:135]\n"
    "ds_read_b128 v[224:227], v240 offset:8192\n"
    "s_waitcnt lgkmcnt(1)\n"
    "v_mfma_f32_16x16x32_bf16 v[132:135], v[216:219], v[24:27], v[132:135]\n"
    "ds_read_b128 v[228:231], v241 offset:8192\n"
    "s_add_u32 m0, s58, 0xee80\n"
    "v_mfma_f32_16x16x32_bf16 v[132:135], v[220:223], v[28:31], v[132:135]\n"
    "ds_read_b128 v[232:235], v240 offset:12288\n"
    "buffer_load_dwordx4 v244, s[40:43], s56 offen offset:384 nt lds\n"
    "v_mfma_f32_16x16x32_bf16 v[140:143], v[220:223], v[60:63], v[140:143]\n"
    "ds_read_b128 v[236:239], v241 offset:12288\n"
    "v_add_f32 v249, v185, v249\n"
    "v_mfma_f32_16x16x32_bf16 v[140:143], v[216:219], v[56:59], v[140:143]\n"
    "v_cvt_pk_bf16_f32 v184, v184, v185\n"
    "v_mfma_f32_16x16x32_bf16 v[140:143], v[212:215], v[52:55], v[140:143]\n"
    "v_exp_f32 v186, v186\n"
    "v_mfma_f32_16x16x32_bf16 v[140:143], v[208:211], v[48:51], v[140:143]\n"
    "v_add_f32 v249, v186, v249\n"
    "v_mfma_f32_16x16x32_bf16 v[148:151], v[208:211], v[80:83], v[148:151]\n"
    "v_exp_f32 v187, v187\n"
    "v_mfma_f32_16x16x32_bf16 v[148:151], v[212:215], v[84:87], v[148:151]\n"
    "v_exp_f32 v188, v188\n"
    "v_mfma_f32_16x16x32_bf16 v[148:151], v[216:219], v[88:91], v[148:151]\n"
    "v_add_f32 v249, v187, v249\n"
    "v_cvt_pk_bf16_f32 v185, v186, v187\n"
    "v_mfma_f32_16x16x32_bf16 v[148:151], v[220:223], v[92:95], v[148:151]\n"
    "v_exp_f32 v189, v189\n"
    "v_mfma_f32_16x16x32_bf16 v[156:159], v[220:223], v[124:127], v[156:159]\n"
    "v_exp_f32 v190, v190\n"
    "v_mfma_f32_16x16x32_bf16 v[156:159], v[216:219], v[120:123], v[156:159]\n"
    "v_add_f32 v249, v188, v249\n"
    "v_add_f32 v249, v189, v249\n"
    "v_mfma_f32_16x16x32_bf16 v[156:159], v[212:215], v[116:119], v[156:159]\n"
    "v_exp_f32 v191, v191\n"
    "v_mfma_f32_16x16x32_bf16 v[156:159], v[208:211], v[112:115], v[156:159]\n"
    "s_add_u32 s56, s56, 0x4000\n"
    "s_waitcnt lgkmcnt(2)\n"
    "v_mfma_f32_16x16x32_bf16 v[152:155], v[224:227], v[112:115], v[152:155]\n"
    "v_cvt_pk_bf16_f32 v186, v188, v189\n"
    "v_add_f32 v249, v190, v249\n"
    "v_mfma_f32_16x16x32_bf16 v[152:155], v[228:231], v[116:119], v[152:155]\n"
    "ds_read_b64_tr_b16 v[208:209], v242 offset:49152\n"
    "s_waitcnt lgkmcnt(1)\n"
    "v_mfma_f32_16x16x32_bf16 v[152:155], v[232:235], v[120:123], v[152:155]\n"
    "ds_read_b64_tr_b16 v[210:211], v242 offset:57344\n"
    "v_add_f32 v249, v191, v249\n"
    "v_mfma_f32_16x16x32_bf16 v[152:155], v[236:239], v[124:127], v[152:155]\n"
    "ds_read_b64_tr_b16 v[212:213], v243 offset:49152\n"
    "v_cvt_pk_bf16_f32 v187, v190, v191\n"
    "v_mfma_f32_16x16x32_bf16 v[144:147], v[236:239], v[92:95], v[144:147]\n"
    "ds_read_b64_tr_b16 v[214:215], v243 offset:57344\n"
    "v_mfma_f32_16x16x32_bf16 v[144:147], v[232:235], v[88:91], v[144:147]\n"
    "ds_read_b64_tr_b16 v[216:217], v242 offset:49664\n"
    "v_mfma_f32_16x16x32_bf16 v[144:147], v[228:231], v[84:87], v[144:147]\n"
    "ds_read_b64_tr_b16 v[218:219], v242 offset:57856\n"
    "v_mfma_f32_16x16x32_bf16 v[144:147], v[224:227], v[80:83], v[144:147]\n"
    "ds_read_b64_tr_b16 v[220:221], v243 offset:49664\n"
    "v_mfma_f32_16x16x32_bf16 v[136:139], v[224:227], v[48:51], v[136:139]\n"
    "ds_read_b64_tr_b16 v[222:223], v243 offset:57856\n"
    "v_mfma_f32_16x16x32_bf16 v[136:139], v[228:231], v[52:55], v[136:139]\n"
    "v_mfma_f32_16x16x32_bf16 v[136:139], v[232:235], v[56:59], v[136:139]\n"
    "v_mfma_f32_16x16x32_bf16 v[136:139], v[236:239], v[60:63], v[136:139]\n"
    "v_mfma_f32_16x16x32_bf16 v[128:131], v[236:239], v[28:31], v[128:131]\n"
    "v_mfma_f32_16x16x32_bf16 v[128:131], v[232:235], v[24:27], v[128:131]\n"
    "v_mfma_f32_16x16x32_bf16 v[128:131], v[228:231], v[20:23], v[128:131]\n"
    "v_mfma_f32_16x16x32_bf16 v[128:131], v[224:227], v[16:19], v[128:131]\n"
    "s_waitcnt lgkmcnt(4)\n"
    "v_mfma_f32_16x16x32_bf16 a[0:3], v[208:211], v[160:163], a[0:3]\n"
    "ds_read_b64_tr_b16 v[224:225], v242 offset:50176\n"
    "v_mfma_f32_16x16x32_bf16 a[16:19], v[212:215], v[160:163], a[16:19]\n"
    "ds_read_b64_tr_b16 v[226:227], v242 offset:58368\n"
    "v_mfma_f32_16x16x32_bf16 a[20:23], v[212:215], v[168:171], a[20:23]\n"
    "ds_read_b64_tr_b16 v[228:229], v243 offset:50176\n"
    "v_mfma_f32_16x16x32_bf16 a[4:7], v[208:211], v[168:171], a[4:7]\n"
    "ds_read_b64_tr_b16 v[230:231], v243 offset:58368\n"
    "v_mfma_f32_16x16x32_bf16 a[8:11], v[208:211], v[176:179], a[8:11]\n"
    "ds_read_b64_tr_b16 v[232:233], v242 offset:50688\n"
    "v_mfma_f32_16x16x32_bf16 a[24:27], v[212:215], v[176:179], a[24:27]\n"
    "ds_read_b64_tr_b16 v[234:235], v242 offset:58880\n"
    "v_mfma_f32_16x16x32_bf16 a[28:31], v[212:215], v[184:187], a[28:31]\n"
    "v_exp_f32 v128, v128\n"
    "v_mfma_f32_16x16x32_bf16 a[12:15], v[208:211], v[184:187], a[12:15]\n"
    "ds_read_b64_tr_b16 v[236:237], v243 offset:50688\n"
    "s_waitcnt lgkmcnt(7)\n"
    "v_mfma_f32_16x16x32_bf16 a[44:47], v[216:219], v[184:187], a[44:47]\n"
    "ds_read_b64_tr_b16 v[238:239], v243 offset:58880\n"
    "v_add_f32 v246, v128, v246\n"
    "v_mfma_f32_16x16x32_bf16 a[60:63], v[220:223], v[184:187], a[60:63]\n"
    "ds_read_b64_tr_b16 v[208:209], v242 offset:51200\n"
    "v_mfma_f32_16x16x32_bf16 a[56:59], v[220:223], v[176:179], a[56:59]\n"
    "ds_read_b64_tr_b16 v[210:211], v242 offset:59392\n"
    "v_mfma_f32_16x16x32_bf16 a[40:43], v[216:219], v[176:179], a[40:43]\n"
    "ds_read_b64_tr_b16 v[212:213], v243 offset:51200\n"
    "v_mfma_f32_16x16x32_bf16 a[36:39], v[216:219], v[168:171], a[36:39]\n"
    "ds_read_b64_tr_b16 v[214:215], v243 offset:59392\n"
    "v_mfma_f32_16x16x32_bf16 a[52:55], v[220:223], v[168:171], a[52:55]\n"
    "v_exp_f32 v129, v129\n"
    "v_mfma_f32_16x16x32_bf16 a[48:51], v[220:223], v[160:163], a[48:51]\n"
    "v_add_f32 v246, v129, v246\n"
    "v_cvt_pk_bf16_f32 v128, v128, v129\n"
    "v_mfma_f32_16x16x32_bf16 a[32:35], v[216:219], v[160:163], a[32:35]\n"
    "s_waitcnt lgkmcnt(8)\n"
    "v_mfma_f32_16x16x32_bf16 a[64:67], v[224:227], v[160:163], a[64:67]\n"
    "ds_read_b64_tr_b16 v[216:217], v242 offset:51712\n"
    "v_mfma_f32_16x16x32_bf16 a[80:83], v[228:231], v[160:163], a[80:83]\n"
    "ds_read_b64_tr_b16 v[218:219], v242 offset:59904\n"
    "v_mfma_f32_16x16x32_bf16 a[84:87], v[228:231], v[168:171], a[84:87]\n"
    "ds_read_b64_tr_b16 v[220:221], v243 offset:51712\n"
    "v_mfma_f32_16x16x32_bf16 a[68:71], v[224:227], v[168:171], a[68:71]\n"
    "ds_read_b64_tr_b16 v[222:223], v243 offset:59904\n"
    "v_mfma_f32_16x16x32_bf16 a[72:75], v[224:227], v[176:179], a[72:75]\n"
    "v_exp_f32 v130, v130\n"
    "v_mfma_f32_16x16x32_bf16 a[88:91], v[228:231], v[176:179], a[88:91]\n"
    "v_exp_f32 v131, v131\n"
    "v_mfma_f32_16x16x32_bf16 a[92:95], v[228:231], v[184:187], a[92:95]\n"
    "v_add_f32 v246, v130, v246\n"
    "v_add_f32 v246, v131, v246\n"
    "v_mfma_f32_16x16x32_bf16 a[76:79], v[224:227], v[184:187], a[76:79]\n"
    "v_cvt_pk_bf16_f32 v129, v130, v131\n"
    "s_waitcnt lgkmcnt(8)\n"
    "v_mfma_f32_16x16x32_bf16 a[108:111], v[232:235], v[184:187], a[108:111]\n"
    "ds_read_b64_tr_b16 v[224:225], v242 offset:52224\n"
    "v_mfma_f32_16x16x32_bf16 a[124:127], v[236:239], v[184:187], a[124:127]\n"
    "ds_read_b64_tr_b16 v[226:227], v242 offset:60416\n"
    "v_mfma_f32_16x16x32_bf16 a[120:123], v[236:239], v[176:179], a[120:123]\n"
    "ds_read_b64_tr_b16 v[228:229], v243 offset:52224\n"
    "v_mfma_f32_16x16x32_bf16 a[104:107], v[232:235], v[176:179], a[104:107]\n"
    "ds_read_b64_tr_b16 v[230:231], v243 offset:60416\n"
    "v_mfma_f32_16x16x32_bf16 a[100:103], v[232:235], v[168:171], a[100:103]\n"
    "v_exp_f32 v132, v132\n"
    "v_mfma_f32_16x16x32_bf16 a[116:119], v[236:239], v[168:171], a[116:119]\n"
    "v_exp_f32 v133, v133\n"
    "v_mfma_f32_16x16x32_bf16 a[112:115], v[236:239], v[160:163], a[112:115]\n"
    "v_add_f32 v246, v132, v246\n"
    "v_add_f32 v246, v133, v246\n"
    "v_mfma_f32_16x16x32_bf16 a[96:99], v[232:235], v[160:163], a[96:99]\n"
    "v_cvt_pk_bf16_f32 v130, v132, v133\n"
    "s_waitcnt lgkmcnt(8)\n"
    "v_mfma_f32_16x16x32_bf16 a[128:131], v[208:211], v[160:163], a[128:131]\n"
    "ds_read_b64_tr_b16 v[232:233], v242 offset:52736\n"
    "v_mfma_f32_16x16x32_bf16 a[144:147], v[212:215], v[160:163], a[144:147]\n"
    "ds_read_b64_tr_b16 v[234:235], v242 offset:60928\n"
    "v_mfma_f32_16x16x32_bf16 a[148:151], v[212:215], v[168:171], a[148:151]\n"
    "ds_read_b64_tr_b16 v[236:237], v243 offset:52736\n"
    "v_mfma_f32_16x16x32_bf16 a[132:135], v[208:211], v[168:171], a[132:135]\n"
    "ds_read_b64_tr_b16 v[238:239], v243 offset:60928\n"
    "v_mfma_f32_16x16x32_bf16 a[136:139], v[208:211], v[176:179], a[136:139]\n"
    "v_exp_f32 v134, v134\n"
    "v_mfma_f32_16x16x32_bf16 a[152:155], v[212:215], v[176:179], a[152:155]\n"
    "v_add_f32 v246, v134, v246\n"
    "v_mfma_f32_16x16x32_bf16 a[156:159], v[212:215], v[184:187], a[156:159]\n"
    "s_waitcnt vmcnt(12)\n"
    "s_barrier\n"
    "v_mfma_f32_16x16x32_bf16 a[140:143], v[208:211], v[184:187], a[140:143]\n"
    "s_waitcnt lgkmcnt(8)\n"
    "v_mfma_f32_16x16x32_bf16 a[172:175], v[216:219], v[184:187], a[172:175]\n"
    "ds_read_b128 v[208:211], v240 offset:16384\n"
    "v_mfma_f32_16x16x32_bf16 a[188:191], v[220:223], v[184:187], a[188:191]\n"
    "ds_read_b128 v[212:215], v241 offset:16384\n"
    "v_mfma_f32_16x16x32_bf16 a[184:187], v[220:223], v[176:179], a[184:187]\n"
    "s_add_u32 m0, s59, 0x8000\n"
    "v_mfma_f32_16x16x32_bf16 a[168:171], v[216:219], v[176:179], a[168:171]\n"
    "buffer_load_dwordx4 v245, s[44:47], s56 offen offset:0 nt lds\n"
    "v_mfma_f32_16x16x32_bf16 a[164:167], v[216:219], v[168:171], a[164:167]\n"
    "v_exp_f32 v135, v135\n"
    "v_mfma_f32_16x16x32_bf16 a[180:183], v[220:223], v[168:171], a[180:183]\n"
    "v_exp_f32 v136, v136\n"
    "v_mfma_f32_16x16x32_bf16 a[176:179], v[220:223], v[160:163], a[176:179]\n"
    "v_add_f32 v246, v135, v246\n"
    "v_cvt_pk_bf16_f32 v131, v134, v135\n"
    "v_mfma_f32_16x16x32_bf16 a[160:163], v[216:219], v[160:163], a[160:163]\n"
    "v_add_f32 v247, v136, v247\n"
    "s_waitcnt lgkmcnt(6)\n"
    "v_mfma_f32_16x16x32_bf16 a[192:195], v[224:227], v[160:163], a[192:195]\n"
    "ds_read_b128 v[216:219], v240 offset:20480\n"
    "v_mfma_f32_16x16x32_bf16 a[208:211], v[228:231], v[160:163], a[208:211]\n"
    "ds_read_b128 v[220:223], v241 offset:20480\n"
    "v_mfma_f32_16x16x32_bf16 a[212:215], v[228:231], v[168:171], a[212:215]\n"
    "s_add_u32 m0, s59, 0x8380\n"
    "v_mfma_f32_16x16x32_bf16 a[196:199], v[224:227], v[168:171], a[196:199]\n"
    "buffer_load_dwordx4 v245, s[44:47], s56 offen offset:128 nt lds\n"
    "v_mfma_f32_16x16x32_bf16 a[200:203], v[224:227], v[176:179], a[200:203]\n"
    "v_exp_f32 v137, v137\n"
    "v_mfma_f32_16x16x32_bf16 a[216:219], v[228:231], v[176:179], a[216:219]\n"
    "v_exp_f32 v138, v138\n"
    "v_mfma_f32_16x16x32_bf16 a[220:223], v[228:231], v[184:187], a[220:223]\n"
    "v_add_f32 v247, v137, v247\n"
    "v_cvt_pk_bf16_f32 v136, v136, v137\n"
    "v_mfma_f32_16x16x32_bf16 a[204:207], v[224:227], v[184:187], a[204:207]\n"
    "v_add_f32 v247, v138, v247\n"
    "s_waitcnt lgkmcnt(4)\n"
    "v_mfma_f32_16x16x32_bf16 a[236:239], v[232:235], v[184:187], a[236:239]\n"
    "ds_read_b128 v[224:227], v240 offset:18432\n"
    "v_mfma_f32_16x16x32_bf16 a[252:255], v[236:239], v[184:187], a[252:255]\n"
    "ds_read_b128 v[228:231], v241 offset:18432\n"
    "v_mfma_f32_16x16x32_bf16 a[248:251], v[236:239], v[176:179], a[248:251]\n"
    "s_add_u32 m0, s59, 0x8700\n"
    "v_mfma_f32_16x16x32_bf16 a[232:235], v[232:235], v[176:179], a[232:235]\n"
    "buffer_load_dwordx4 v245, s[44:47], s56 offen offset:256 nt lds\n"
    "v_mfma_f32_16x16x32_bf16 a[228:231], v[232:235], v[168:171], a[228:231]\n"
    "v_exp_f32 v139, v139\n"
    "v_mfma_f32_16x16x32_bf16 a[244:247], v[236:239], v[168:171], a[244:247]\n"
    "v_add_f32 v247, v139, v247\n"
    "v_cvt_pk_bf16_f32 v137, v138, v139\n"
    "v_mfma_f32_16x16x32_bf16 a[240:243], v[236:239], v[160:163], a[240:243]\n"
    "v_exp_f32 v140, v140\n"
    "v_mfma_f32_16x16x32_bf16 a[224:227], v[232:235], v[160:163], a[224:227]\n"
    "v_add_f32 v247, v140, v247\n"
    "s_waitcnt lgkmcnt(4)\n"
    "s_branch L_loop_%=\n"
    "L_tail_%=:\n"
    "v_exp_f32 v173, v173\n"
    "s_nop 0\n"
    "v_add_f32 v247, v173, v247\n"
    "v_cvt_pk_bf16_f32 v170, v172, v173\n"
    "v_exp_f32 v174, v174\n"
    "v_exp_f32 v175, v175\n"
    "v_add_f32 v247, v174, v247\n"
    "v_add_f32 v247, v175, v247\n"
    "v_exp_f32 v176, v176\n"
    "v_cvt_pk_bf16_f32 v171, v174, v175\n"
    "v_add_f32 v248, v176, v248\n"
    "v_exp_f32 v177, v177\n"
    "v_exp_f32 v178, v178\n"
    "v_add_f32 v248, v177, v248\n"
    "v_cvt_pk_bf16_f32 v176, v176, v177\n"
    "v_add_f32 v248, v178, v248\n"
    "v_exp_f32 v179, v179\n"
    "s_nop 0\n"
    "v_add_f32 v248, v179, v248\n"
    "v_exp_f32 v180, v180\n"
    "v_cvt_pk_bf16_f32 v177, v178, v179\n"
    "v_add_f32 v248, v180, v248\n"
    "v_exp_f32 v181, v181\n"
    "s_nop 0\n"
    "v_add_f32 v248, v181, v248\n"
    "v_cvt_pk_bf16_f32 v178, v180, v181\n"
    "v_exp_f32 v182, v182\n"
    "s_nop 0\n"
    "v_add_f32 v248, v182, v248\n"
    "v_exp_f32 v183, v183\n"
    "s_nop 0\n"
    "v_add_f32 v248, v183, v248\n"
    "v_cvt_pk_bf16_f32 v179, v182, v183\n"
    "v_exp_f32 v184, v184\n"
    "s_nop 0\n"
    "v_add_f32 v249, v184, v249\n"
    "v_exp_f32 v185, v185\n"
    "s_nop 0\n"
    "v_add_f32 v249, v185, v249\n"
    "v_cvt_pk_bf16_f32 v184, v184, v185\n"
    "v_exp_f32 v186, v186\n"
    "s_nop 0\n"
    "v_add_f32 v249, v186, v249\n"
    "v_exp_f32 v187, v187\n"
    "v_exp_f32 v188, v188\n"
    "v_add_f32 v249, v187, v249\n"
    "v_cvt_pk_bf16_f32 v185, v186, v187\n"
    "v_exp_f32 v189, v189\n"
    "v_exp_f32 v190, v190\n"
    "v_add_f32 v249, v188, v249\n"
    "v_add_f32 v249, v189, v249\n"
    "v_exp_f32 v191, v191\n"
    "v_cvt_pk_bf16_f32 v186, v188, v189\n"
    "v_add_f32 v249, v190, v249\n"
    "ds_read_b64_tr_b16 v[208:209], v242 offset:49152\n"
    "ds_read_b64_tr_b16 v[210:211], v242 offset:57344\n"
    "v_add_f32 v249, v191, v249\n"
    "ds_read_b64_tr_b16 v[212:213], v243 offset:49152\n"
    "v_cvt_pk_bf16_f32 v187, v190, v191\n"
    "ds_read_b64_tr_b16 v[214:215], v243 offset:57344\n"
    "ds_read_b64_tr_b16 v[216:217], v242 offset:49664\n"
    "ds_read_b64_tr_b16 v[218:219], v242 offset:57856\n"
    "ds_read_b64_tr_b16 v[220:221], v243 offset:49664\n"
    "ds_read_b64_tr_b16 v[222:223], v243 offset:57856\n"
    "s_waitcnt lgkmcnt(4)\n"
    "v_mfma_f32_16x16x32_bf16 a[0:3], v[208:211], v[160:163], a[0:3]\n"
    "v_mov_b32 v192, v246\n"
    "v_mov_b32 v196, v247\n"
    "ds_read_b64_tr_b16 v[224:225], v242 offset:50176\n"
    "v_mfma_f32_16x16x32_bf16 a[16:19], v[212:215], v[160:163], a[16:19]\n"
    "v_mov_b32 v200, v248\n"
    "v_mov_b32 v204, v249\n"
    "ds_read_b64_tr_b16 v[226:227], v242 offset:58368\n"
    "v_mfma_f32_16x16x32_bf16 a[20:23], v[212:215], v[168:171], a[20:23]\n"
    "v_permlane32_swap_b32 v246, v192\n"
    "v_permlane32_swap_b32 v247, v196\n"
    "ds_read_b64_tr_b16 v[228:229], v243 offset:50176\n"
    "v_mfma_f32_16x16x32_bf16 a[4:7], v[208:211], v[168:171], a[4:7]\n"
    "v_permlane32_swap_b32 v248, v200\n"
    "v_permlane32_swap_b32 v249, v204\n"
    "ds_read_b64_tr_b16 v[230:231], v243 offset:58368\n"
    "v_mfma_f32_16x16x32_bf16 a[8:11], v[208:211], v[176:179], a[8:11]\n"
    "v_add_f32 v246, v246, v192\n"
    "v_add_f32 v247, v247, v196\n"
    "ds_read_b64_tr_b16 v[232:233], v242 offset:50688\n"
    "v_mfma_f32_16x16x32_bf16 a[24:27], v[212:215], v[176:179], a[24:27]\n"
    "v_add_f32 v248, v248, v200\n"
    "v_add_f32 v249, v249, v204\n"
    "ds_read_b64_tr_b16 v[234:235], v242 offset:58880\n"
    "v_mfma_f32_16x16x32_bf16 a[28:31], v[212:215], v[184:187], a[28:31]\n"
    "v_mov_b32 v192, v246\n"
    "v_mov_b32 v196, v247\n"
    "v_mov_b32 v200, v248\n"
    "v_mfma_f32_16x16x32_bf16 a[12:15], v[208:211], v[184:187], a[12:15]\n"
    "v_mov_b32 v204, v249\n"
    "ds_read_b64_tr_b16 v[236:237], v243 offset:50688\n"
    "s_waitcnt lgkmcnt(7)\n"
    "v_mfma_f32_16x16x32_bf16 a[44:47], v[216:219], v[184:187], a[44:47]\n"
    "v_permlane16_swap_b32 v246, v192\n"
    "v_permlane16_swap_b32 v247, v196\n"
    "ds_read_b64_tr_b16 v[238:239], v243 offset:58880\n"
    "v_mfma_f32_16x16x32_bf16 a[60:63], v[220:223], v[184:187], a[60:63]\n"
    "v_permlane16_swap_b32 v248, v200\n"
    "v_permlane16_swap_b32 v249, v204\n"
    "ds_read_b64_tr_b16 v[208:209], v242 offset:51200\n"
    "v_mfma_f32_16x16x32_bf16 a[56:59], v[220:223], v[176:179], a[56:59]\n"
    "v_add_f32 v246, v246, v192\n"
    "v_add_f32 v247, v247, v196\n"
    "ds_read_b64_tr_b16 v[210:211], v242 offset:59392\n"
    "v_mfma_f32_16x16x32_bf16 a[40:43], v[216:219], v[176:179], a[40:43]\n"
    "v_add_f32 v248, v248, v200\n"
    "v_add_f32 v249, v249, v204\n"
    "ds_read_b64_tr_b16 v[212:213], v243 offset:51200\n"
    "v_mfma_f32_16x16x32_bf16 a[36:39], v[216:219], v[168:171], a[36:39]\n"
    "v_max3_f32 v193, v246, v247, v248\n"
    "v_max_f32 v193, v249, v193\n"
    "ds_read_b64_tr_b16 v[214:215], v243 offset:59392\n"
    "v_mfma_f32_16x16x32_bf16 a[52:55], v[220:223], v[168:171], a[52:55]\n"
    "s_lshl_b32 s65, %[w], 2\n"
    "v_cmp_lt_f32 vcc, 0x67800000, v193\n"
    "s_add_u32 s65, s65, 0x18000\n"
    "v_mfma_f32_16x16x32_bf16 a[48:51], v[220:223], v[160:163], a[48:51]\n"
    "v_mov_b32 v194, s65\n"
    "s_cmp_lg_u64 vcc, 0\n"
    "s_cselect_b32 s65, 1, 0\n"
    "v_mfma_f32_16x16x32_bf16 a[32:35], v[216:219], v[160:163], a[32:35]\n"
    "v_mov_b32 v193, s65\n"
    "ds_write_b32 v194, v193\n"
    "s_waitcnt lgkmcnt(9)\n"
    "v_mfma_f32_16x16x32_bf16 a[64:67], v[224:227], v[160:163], a[64:67]\n"
    "v_mov_b32 v195, 0x18000\n"
    "v_rcp_f32 v246, v246\n"
    "ds_read_b64_tr_b16 v[216:217], v242 offset:51712\n"
    "v_mfma_f32_16x16x32_bf16 a[80:83], v[228:231], v[160:163], a[80:83]\n"
    "v_rcp_f32 v247, v247\n"
    "v_rcp_f32 v248, v248\n"
    "ds_read_b64_tr_b16 v[218:219], v242 offset:59904\n"
    "v_mfma_f32_16x16x32_bf16 a[84:87], v[228:231], v[168:171], a[84:87]\n"
    "v_rcp_f32 v249, v249\n"
    "v_xor_b32 v240, 32, v251\n"
    "ds_read_b64_tr_b16 v[220:221], v243 offset:51712\n"
    "v_mfma_f32_16x16x32_bf16 a[68:71], v[224:227], v[168:171], a[68:71]\n"
    "v_xor_b32 v241, 64, v251\n"
    "v_xor_b32 v244, 96, v251\n"
    "ds_read_b64_tr_b16 v[222:223], v243 offset:59904\n"
    "v_mfma_f32_16x16x32_bf16 a[72:75], v[224:227], v[176:179], a[72:75]\n"
    "s_waitcnt lgkmcnt(4)\n"
    "s_barrier\n"
    "ds_read_b128 v[196:199], v195\n"
    "v_mfma_f32_16x16x32_bf16 a[88:91], v[228:231], v[176:179], a[88:91]\n"
    "v_accvgpr_read_b32 v64, a0\n"
    "v_accvgpr_read_b32 v65, a1\n"
    "v_accvgpr_read_b32 v66, a2\n"
    "v_mfma_f32_16x16x32_bf16 a[92:95], v[228:231], v[184:187], a[92:95]\n"
    "v_accvgpr_read_b32 v67, a3\n"
    "v_accvgpr_read_b32 v72, a16\n"
    "v_accvgpr_read_b32 v73, a17\n"
    "v_mfma_f32_16x16x32_bf16 a[76:79], v[224:227], v[184:187], a[76:79]\n"
    "v_accvgpr_read_b32 v74, a18\n"
    "v_accvgpr_read_b32 v75, a19\n"
    "s_waitcnt lgkmcnt(5)\n"
    "v_mfma_f32_16x16x32_bf16 a[108:111], v[232:235], v[184:187], a[108:111]\n"
    "v_mul_f32 v64, v246, v64\n"
    "v_mul_f32 v65, v246, v65\n"
    "v_mul_f32 v66, v246, v66\n"
    "v_mul_f32 v67, v246, v67\n"
    "ds_read_b64_tr_b16 v[224:225], v242 offset:52224\n"
    "v_mfma_f32_16x16x32_bf16 a[124:127], v[236:239], v[184:187], a[124:127]\n"
    "v_accvgpr_read_b32 v80, a20\n"
    "v_accvgpr_read_b32 v81, a21\n"
    "ds_read_b64_tr_b16 v[226:227], v242 offset:60416\n"
    "v_mfma_f32_16x16x32_bf16 a[120:123], v[236:239], v[176:179], a[120:123]\n"
    "v_accvgpr_read_b32 v82, a22\n"
    "v_accvgpr_read_b32 v83, a23\n"
    "ds_read_b64_tr_b16 v[228:229], v243 offset:52224\n"
    "v_mfma_f32_16x16x32_bf16 a[104:107], v[232:235], v[176:179], a[104:107]\n"
    "v_mul_f32 v72, v246, v72\n"
    "v_mul_f32 v73, v246, v73\n"
    "v_mul_f32 v74, v246, v74\n"
    "v_mul_f32 v75, v246, v75\n"
    "ds_read_b64_tr_b16 v[230:231], v243 offset:60416\n"
    "v_mfma_f32_16x16x32_bf16 a[100:103], v[232:235], v[168:171], a[100:103]\n"
    "v_cvt_pk_bf16_f32 v68, v64, v65\n"
    "v_cvt_pk_bf16_f32 v69, v66, v67\n"
    "v_accvgpr_read_b32 v88, a4\n"
    "v_mfma_f32_16x16x32_bf16 a[116:119], v[236:239], v[168:171], a[116:119]\n"
    "v_accvgpr_read_b32 v89, a5\n"
    "v_accvgpr_read_b32 v90, a6\n"
    "v_accvgpr_read_b32 v91, a7\n"
    "v_mfma_f32_16x16x32_bf16 a[112:115], v[236:239], v[160:163], a[112:115]\n"
    "v_mul_f32 v80, v247, v80\n"
    "v_mul_f32 v81, v247, v81\n"
    "v_mul_f32 v82, v247, v82\n"
    "v_mul_f32 v83, v247, v83\n"
    "v_mfma_f32_16x16x32_bf16 a[96:99], v[232:235], v[160:163], a[96:99]\n"
    "v_cvt_pk_bf16_f32 v76, v72, v73\n"
    "v_cvt_pk_bf16_f32 v77, v74, v75\n"
    "s_waitcnt lgkmcnt(9)\n"
    "v_mfma_f32_16x16x32_bf16 a[128:131], v[208:211], v[160:163], a[128:131]\n"
    "s_waitcnt lgkmcnt(4)\n"
    "v_or3_b32 v193, v196, v197, v198\n"
    "ds_read_b64_tr_b16 v[232:233], v242 offset:52736\n"
    "v_mfma_f32_16x16x32_bf16 a[144:147], v[212:215], v[160:163], a[144:147]\n"
    "v_or_b32 v193, v199, v193\n"
    "v_cmp_ne_u32 vcc, 0, v193\n"
    "ds_read_b64_tr_b16 v[234:235], v242 offset:60928\n"
    "v_mfma_f32_16x16x32_bf16 a[148:151], v[212:215], v[168:171], a[148:151]\n"
    "ds_write_b64 v251, v[68:69] offset:0\n"
    "v_accvgpr_read_b32 v96, a8\n"
    "ds_read_b64_tr_b16 v[236:237], v243 offset:52736\n"
    "v_mfma_f32_16x16x32_bf16 a[132:135], v[208:211], v[168:171], a[132:135]\n"
    "v_accvgpr_read_b32 v97, a9\n"
    "v_accvgpr_read_b32 v98, a10\n"
    "ds_read_b64_tr_b16 v[238:239], v243 offset:60928\n"
    "v_mfma_f32_16x16x32_bf16 a[136:139], v[208:211], v[176:179], a[136:139]\n"
    "v_accvgpr_read_b32 v99, a11\n"
    "v_mul_f32 v88, v247, v88\n"
    "v_mul_f32 v89, v247, v89\n"
    "v_mul_f32 v90, v247, v90\n"
    "v_mul_f32 v91, v247, v91\n"
    "v_mfma_f32_16x16x32_bf16 a[152:155], v[212:215], v[176:179], a[152:155]\n"
    "v_cvt_pk_bf16_f32 v84, v80, v81\n"
    "v_cvt_pk_bf16_f32 v85, v82, v83\n"
    "ds_write_b64 v240, v[76:77] offset:0\n"
    "v_mfma_f32_16x16x32_bf16 a[156:159], v[212:215], v[184:187], a[156:159]\n"
    "v_accvgpr_read_b32 v104, a24\n"
    "v_accvgpr_read_b32 v105, a25\n"
    "v_accvgpr_read_b32 v106, a26\n"
    "v_mfma_f32_16x16x32_bf16 a[140:143], v[208:211], v[184:187], a[140:143]\n"
    "v_accvgpr_read_b32 v107, a27\n"
    "v_mul_f32 v96, v248, v96\n"
    "v_mul_f32 v97, v248, v97\n"
    "v_mul_f32 v98, v248, v98\n"
    "v_mul_f32 v99, v248, v99\n"
    "s_waitcnt lgkmcnt(10)\n"
    "v_mfma_f32_16x16x32_bf16 a[172:175], v[216:219], v[184:187], a[172:175]\n"
    "v_cvt_pk_bf16_f32 v92, v88, v89\n"
    "v_cvt_pk_bf16_f32 v93, v90, v91\n"
    "s_cbranch_vccnz L_redo_%=\n"
    "v_mfma_f32_16x16x32_bf16 a[188:191], v[220:223], v[184:187], a[188:191]\n"
    "ds_write_b64 v240, v[84:85] offset:2048\n"
    "v_accvgpr_read_b32 v112, a28\n"
    "v_accvgpr_read_b32 v113, a29\n"
    "v_mfma_f32_16x16x32_bf16 a[184:187], v[220:223], v[176:179], a[184:187]\n"
    "v_accvgpr_read_b32 v114, a30\n"
    "v_accvgpr_read_b32 v115, a31\n"
    "v_mul_f32 v104, v248, v104\n"
    "v_mul_f32 v105, v248, v105\n"
    "v_mul_f32 v106, v248, v106\n"
    "v_mul_f32 v107, v248, v107\n"
    "v_mfma_f32_16x16x32_bf16 a[168:171], v[216:219], v[176:179], a[168:171]\n"
    "v_cvt_pk_bf16_f32 v100, v96, v97\n"
    "v_cvt_pk_bf16_f32 v101, v98, v99\n"
    "ds_write_b64 v251, v[92:93] offset:2048\n"
    "v_mfma_f32_16x16x32_bf16 a[164:167], v[216:219], v[168:171], a[164:167]\n"
    "v_accvgpr_read_b32 v120, a12\n"
    "v_accvgpr_read_b32 v121, a13\n"
    "v_accvgpr_read_b32 v122, a14\n"
    "v_mfma_f32_16x16x32_bf16 a[180:183], v[220:223], v[168:171], a[180:183]\n"
    "v_accvgpr_read_b32 v123, a15\n"
    "v_mul_f32 v112, v249, v112\n"
    "v_mul_f32 v113, v249, v113\n"
    "v_mul_f32 v114, v249, v114\n"
    "v_mul_f32 v115, v249, v115\n"
    "v_mfma_f32_16x16x32_bf16 a[176:179], v[220:223], v[160:163], a[176:179]\n"
    "v_cvt_pk_bf16_f32 v108, v104, v105\n"
    "v_cvt_pk_bf16_f32 v109, v106, v107\n"
    "ds_write_b64 v251, v[100:101] offset:4096\n"
    "v_mfma_f32_16x16x32_bf16 a[160:163], v[216:219], v[160:163], a[160:163]\n"
    "v_accvgpr_read_b32 v64, a44\n"
    "v_accvgpr_read_b32 v65, a45\n"
    "s_waitcnt lgkmcnt(9)\n"
    "v_mfma_f32_16x16x32_bf16 a[192:195], v[224:227], v[160:163], a[192:195]\n"
    "v_accvgpr_read_b32 v66, a46\n"
    "v_accvgpr_read_b32 v67, a47\n"
    "v_mul_f32 v120, v249, v120\n"
    "v_mul_f32 v121, v249, v121\n"
    "v_mul_f32 v122, v249, v122\n"
    "v_mul_f32 v123, v249, v123\n"
    "v_mfma_f32_16x16x32_bf16 a[208:211], v[228:231], v[160:163], a[208:211]\n"
    "v_cvt_pk_bf16_f32 v116, v112, v113\n"
    "v_cvt_pk_bf16_f32 v117, v114, v115\n"
    "ds_write_b64 v240, v[108:109] offset:4096\n"
    "v_mfma_f32_16x16x32_bf16 a[212:215], v[228:231], v[168:171], a[212:215]\n"
    "v_accvgpr_read_b32 v72, a60\n"
    "v_accvgpr_read_b32 v73, a61\n"
    "v_accvgpr_read_b32 v74, a62\n"
    "v_mfma_f32_16x16x32_bf16 a[196:199], v[224:227], v[168:171], a[196:199]\n"
    "v_accvgpr_read_b32 v75, a63\n"
    "v_mul_f32 v64, v249, v64\n"
    "v_mul_f32 v65, v249, v65\n"
    "v_mul_f32 v66, v249, v66\n"
    "v_mul_f32 v67, v249, v67\n"
    "v_mfma_f32_16x16x32_bf16 a[200:203], v[224:227], v[176:179], a[200:203]\n"
    "v_cvt_pk_bf16_f32 v124, v120, v121\n"
    "v_cvt_pk_bf16_f32 v125, v122, v123\n"
    "ds_write_b64 v240, v[116:117] offset:6144\n"
    "v_mfma_f32_16x16x32_bf16 a[216:219], v[228:231], v[176:179], a[216:219]\n"
    "v_accvgpr_read_b32 v80, a56\n"
    "v_accvgpr_read_b32 v81, a57\n"
    "v_accvgpr_read_b32 v82, a58\n"
    "v_mfma_f32_16x16x32_bf16 a[220:223], v[228:231], v[184:187], a[220:223]\n"
    "v_accvgpr_read_b32 v83, a59\n"
    "v_mul_f32 v72, v249, v72\n"
    "v_mul_f32 v73, v249, v73\n"
    "v_mul_f32 v74, v249, v74\n"
    "v_mul_f32 v75, v249, v75\n"
    "v_mfma_f32_16x16x32_bf16 a[204:207], v[224:227], v[184:187], a[204:207]\n"
    "v_cvt_pk_bf16_f32 v68, v64, v65\n"
    "v_cvt_pk_bf16_f32 v69, v66, v67\n"
    "s_waitcnt lgkmcnt(6)\n"
    "v_mfma_f32_16x16x32_bf16 a[236:239], v[232:235], v[184:187], a[236:239]\n"
    "ds_write_b64 v251, v[124:125] offset:6144\n"
    "v_accvgpr_read_b32 v88, a40\n"
    "v_accvgpr_read_b32 v89, a41\n"
    "v_mfma_f32_16x16x32_bf16 a[252:255], v[236:239], v[184:187], a[252:255]\n"
    "v_accvgpr_read_b32 v90, a42\n"
    "v_accvgpr_read_b32 v91, a43\n"
    "v_mul_f32 v80, v248, v80\n"
    "v_mul_f32 v81, v248, v81\n"
    "v_mul_f32 v82, v248, v82\n"
    "v_mul_f32 v83, v248, v83\n"
    "v_mfma_f32_16x16x32_bf16 a[248:251], v[236:239], v[176:179], a[248:251]\n"
    "v_cvt_pk_bf16_f32 v76, v72, v73\n"
    "v_cvt_pk_bf16_f32 v77, v74, v75\n"
    "ds_write_b64 v241, v[68:69] offset:6144\n"
    "v_mfma_f32_16x16x32_bf16 a[232:235], v[232:235], v[176:179], a[232:235]\n"
    "v_accvgpr_read_b32 v96, a36\n"
    "v_accvgpr_read_b32 v97, a37\n"
    "v_accvgpr_read_b32 v98, a38\n"
    "v_mfma_f32_16x16x32_bf16 a[228:231], v[232:235], v[168:171], a[228:231]\n"
    "v_accvgpr_read_b32 v99, a39\n"
    "v_mul_f32 v88, v248, v88\n"
    "v_mul_f32 v89, v248, v89\n"
    "v_mul_f32 v90, v248, v90\n"
    "v_mul_f32 v91, v248, v91\n"
    "v_mfma_f32_16x16x32_bf16 a[244:247], v[236:239], v[168:171], a[244:247]\n"
    "v_cvt_pk_bf16_f32 v84, v80, v81\n"
    "v_cvt_pk_bf16_f32 v85, v82, v83\n"
    "ds_write_b64 v244, v[76:77] offset:6144\n"
    "v_mfma_f32_16x16x32_bf16 a[240:243], v[236:239], v[160:163], a[240:243]\n"
    "v_accvgpr_read_b32 v104, a52\n"
    "v_accvgpr_read_b32 v105, a53\n"
    "v_accvgpr_read_b32 v106, a54\n"
    "v_mfma_f32_16x16x32_bf16 a[224:227], v[232:235], v[160:163], a[224:227]\n"
    "v_accvgpr_read_b32 v107, a55\n"
    "v_pk_mul_f32 v[96:97], v[96:97], v[246:247] op_sel:[0,1]\n"
    "v_pk_mul_f32 v[98:99], v[98:99], v[246:247] op_sel:[0,1]\n"
    "v_cvt_pk_bf16_f32 v92, v88, v89\n"
    "v_cvt_pk_bf16_f32 v93, v90, v91\n"
    "ds_write_b64 v244, v[84:85] offset:4096\n"
    "v_accvgpr_read_b32 v112, a48\n"
    "v_accvgpr_read_b32 v113, a49\n"
    "v_accvgpr_read_b32 v114, a50\n"
    "v_accvgpr_read_b32 v115, a51\n"
    "v_pk_mul_f32 v[104:105], v[104:105], v[246:247] op_sel:[0,1]\n"
    "v_pk_mul_f32 v[106:107], v[106:107], v[246:247] op_sel:[0,1]\n"
    "v_cvt_pk_bf16_f32 v100, v96, v97\n"
    "v_cvt_pk_bf16_f32 v101, v98, v99\n"
    "ds_write_b64 v241, v[92:93] offset:4096\n"
    "v_accvgpr_read_b32 v120, a32\n"
    "v_accvgpr_read_b32 v121, a33\n"
    "v_accvgpr_read_b32 v122, a34\n"
    "v_accvgpr_read_b32 v123, a35\n"
    "v_pk_mul_f32 v[112:113], v[112:113], v[246:247] op_sel_hi:[1,0]\n"
    "v_pk_mul_f32 v[114:115], v[114:115], v[246:247] op_sel_hi:[1,0]\n"
    "v_cvt_pk_bf16_f32 v108, v104, v105\n"
    "v_cvt_pk_bf16_f32 v109, v106, v107\n"
    "ds_write_b64 v241, v[100:101] offset:2048\n"
    "v_accvgpr_read_b32 v64, a64\n"
    "v_accvgpr_read_b32 v65, a65\n"
    "v_accvgpr_read_b32 v66, a66\n"
    "v_accvgpr_read_b32 v67, a67\n"
    "v_pk_mul_f32 v[120:121], v[120:121], v[246:247] op_sel_hi:[1,0]\n"
    "v_pk_mul_f32 v[122:123], v[122:123], v[246:247] op_sel_hi:[1,0]\n"
    "v_cvt_pk_bf16_f32 v116, v112, v113\n"
    "v_cvt_pk_bf16_f32 v117, v114, v115\n"
    "ds_write_b64 v244, v[108:109] offset:2048\n"
    "v_accvgpr_read_b32 v72, a80\n"
    "v_accvgpr_read_b32 v73, a81\n"
    "v_accvgpr_read_b32 v74, a82\n"
    "v_accvgpr_read_b32 v75, a83\n"
    "v_pk_mul_f32 v[64:65], v[64:65], v[246:247] op_sel_hi:[1,0]\n"
    "v_pk_mul_f32 v[66:67], v[66:67], v[246:247] op_sel_hi:[1,0]\n"
    "v_cvt_pk_bf16_f32 v124, v120, v121\n"
    "v_cvt_pk_bf16_f32 v125, v122, v123\n"
    "ds_write_b64 v244, v[116:117] offset:0\n"
    "v_accvgpr_read_b32 v80, a84\n"
    "v_accvgpr_read_b32 v81, a85\n"
    "v_accvgpr_read_b32 v82, a86\n"
    "v_accvgpr_read_b32 v83, a87\n"
    "v_pk_mul_f32 v[72:73], v[72:73], v[246:247] op_sel_hi:[1,0]\n"
    "v_pk_mul_f32 v[74:75], v[74:75], v[246:247] op_sel_hi:[1,0]\n"
    "v_cvt_pk_bf16_f32 v68, v64, v65\n"
    "v_cvt_pk_bf16_f32 v69, v66, v67\n"
    "ds_write_b64 v241, v[124:125] offset:0\n"
    "ds_read_b128 v[0:3], v252 offset:0\n"
    "ds_read_b128 v[4:7], v252 offset:1024\n"
    "ds_read_b128 v[8:11], v252 offset:2048\n"
    "ds_read_b128 v[12:15], v252 offset:3072\n"
    "ds_read_b128 v[16:19], v252 offset:4096\n"
    "ds_read_b128 v[20:23], v252 offset:5120\n"
    "ds_read_b128 v[24:27], v252 offset:6144\n"
    "ds_read_b128 v[28:31], v252 offset:7168\n"
    "v_accvgpr_read_b32 v88, a68\n"
    "v_accvgpr_read_b32 v89, a69\n"
    "v_accvgpr_read_b32 v90, a70\n"
    "v_accvgpr_read_b32 v91, a71\n"
    "v_pk_mul_f32 v[80:81], v[80:81], v[246:247] op_sel:[0,1]\n"
    "v_pk_mul_f32 v[82:83], v[82:83], v[246:247] op_sel:[0,1]\n"
    "v_cvt_pk_bf16_f32 v76, v72, v73\n"
    "v_cvt_pk_bf16_f32 v77, v74, v75\n"
    "ds_write_b64 v251, v[68:69] offset:0\n"
    "v_accvgpr_read_b32 v96, a72\n"
    "v_accvgpr_read_b32 v97, a73\n"
    "v_accvgpr_read_b32 v98, a74\n"
    "v_accvgpr_read_b32 v99, a75\n"
    "v_pk_mul_f32 v[88:89], v[88:89], v[246:247] op_sel:[0,1]\n"
    "v_pk_mul_f32 v[90:91], v[90:91], v[246:247] op_sel:[0,1]\n"
    "v_cvt_pk_bf16_f32 v84, v80, v81\n"
    "v_cvt_pk_bf16_f32 v85, v82, v83\n"
    "ds_write_b64 v240, v[76:77] offset:0\n"
    "s_waitcnt lgkmcnt(9)\n"
    "buffer_store_dwordx4 v[0:3], v253, s[52:55], s67 offen offset:0 nt\n"
    "s_waitcnt lgkmcnt(8)\n"
    "buffer_store_dwordx4 v[4:7], v253, s[52:55], s48 offen offset:0 nt\n"
    "s_waitcnt lgkmcnt(7)\n"
    "buffer_store_dwordx4 v[8:11], v253, s[52:55], s49 offen offset:0 nt\n"
    "s_waitcnt lgkmcnt(6)\n"
    "buffer_store_dwordx4 v[12:15], v253, s[52:55], s50 offen offset:0 nt\n"
    "s_waitcnt lgkmcnt(5)\n"
    "buffer_store_dwordx4 v[16:19], v253, s[52:55], s51 offen offset:0 nt\n"
    "s_waitcnt lgkmcnt(4)\n"
    "buffer_store_dwordx4 v[20:23], v253, s[52:55], s36 offen offset:0 nt\n"
    "s_waitcnt lgkmcnt(3)\n"
    "buffer_store_dwordx4 v[24:27], v253, s[52:55], s37 offen offset:0 nt\n"
    "s_waitcnt lgkmcnt(2)\n"
    "buffer_store_dwordx4 v[28:31], v253, s[52:55], s38 offen offset:0 nt\n"
    "v_accvgpr_read_b32 v104, a88\n"
    "v_accvgpr_read_b32 v105, a89\n"
    "v_accvgpr_read_b32 v106, a90\n"
    "v_accvgpr_read_b32 v107, a91\n"
    "v_pk_mul_f32 v[96:97], v[96:97], v[248:249] op_sel_hi:[1,0]\n"
    "v_pk_mul_f32 v[98:99], v[98:99], v[248:249] op_sel_hi:[1,0]\n"
    "v_cvt_pk_bf16_f32 v92, v88, v89\n"
    "v_cvt_pk_bf16_f32 v93, v90, v91\n"
    "ds_write_b64 v240, v[84:85] offset:2048\n"
    "v_accvgpr_read_b32 v112, a92\n"
    "v_accvgpr_read_b32 v113, a93\n"
    "v_accvgpr_read_b32 v114, a94\n"
    "v_accvgpr_read_b32 v115, a95\n"
    "v_pk_mul_f32 v[104:105], v[104:105], v[248:249] op_sel_hi:[1,0]\n"
    "v_pk_mul_f32 v[106:107], v[106:107], v[248:249] op_sel_hi:[1,0]\n"
    "v_cvt_pk_bf16_f32 v100, v96, v97\n"
    "v_cvt_pk_bf16_f32 v101, v98, v99\n"
    "ds_write_b64 v251, v[92:93] offset:2048\n"
    "v_accvgpr_read_b32 v120, a76\n"
    "v_accvgpr_read_b32 v121, a77\n"
    "v_accvgpr_read_b32 v122, a78\n"
    "v_accvgpr_read_b32 v123, a79\n"
    "v_pk_mul_f32 v[112:113], v[112:113], v[248:249] op_sel:[0,1]\n"
    "v_pk_mul_f32 v[114:115], v[114:115], v[248:249] op_sel:[0,1]\n"
    "v_cvt_pk_bf16_f32 v108, v104, v105\n"
    "v_cvt_pk_bf16_f32 v109, v106, v107\n"
    "ds_write_b64 v251, v[100:101] offset:4096\n"
    "v_accvgpr_read_b32 v64, a108\n"
    "v_accvgpr_read_b32 v65, a109\n"
    "v_accvgpr_read_b32 v66, a110\n"
    "v_accvgpr_read_b32 v67, a111\n"
    "v_pk_mul_f32 v[120:121], v[120:121], v[248:249] op_sel:[0,1]\n"
    "v_pk_mul_f32 v[122:123], v[122:123], v[248:249] op_sel:[0,1]\n"
    "v_cvt_pk_bf16_f32 v116, v112, v113\n"
    "v_cvt_pk_bf16_f32 v117, v114, v115\n"
    "ds_write_b64 v240, v[108:109] offset:4096\n"
    "v_accvgpr_read_b32 v72, a124\n"
    "v_accvgpr_read_b32 v73, a125\n"
    "v_accvgpr_read_b32 v74, a126\n"
    "v_accvgpr_read_b32 v75, a127\n"
    "v_pk_mul_f32 v[64:65], v[64:65], v[248:249] op_sel:[0,1]\n"
    "v_pk_mul_f32 v[66:67], v[66:67], v[248:249] op_sel:[0,1]\n"
    "v_cvt_pk_bf16_f32 v124, v120, v121\n"
    "v_cvt_pk_bf16_f32 v125, v122, v123\n"
    "ds_write_b64 v240, v[116:117] offset:6144\n"
    "v_accvgpr_read_b32 v80, a120\n"
    "v_accvgpr_read_b32 v81, a121\n"
    "v_accvgpr_read_b32 v82, a122\n"
    "v_accvgpr_read_b32 v83, a123\n"
    "v_pk_mul_f32 v[72:73], v[72:73], v[248:249] op_sel:[0,1]\n"
    "v_pk_mul_f32 v[74:75], v[74:75], v[248:249] op_sel:[0,1]\n"
    "v_cvt_pk_bf16_f32 v68, v64, v65\n"
    "v_cvt_pk_bf16_f32 v69, v66, v67\n"
    "ds_write_b64 v251, v[124:125] offset:6144\n"
    "v_accvgpr_read_b32 v88, a104\n"
    "v_accvgpr_read_b32 v89, a105\n"
    "v_accvgpr_read_b32 v90, a106\n"
    "v_accvgpr_read_b32 v91, a107\n"
    "v_pk_mul_f32 v[80:81], v[80:81], v[248:249] op_sel_hi:[1,0]\n"
    "v_pk_mul_f32 v[82:83], v[82:83], v[248:249] op_sel_hi:[1,0]\n"
    "v_cvt_pk_bf16_f32 v76, v72, v73\n"
    "v_cvt_pk_bf16_f32 v77, v74, v75\n"
    "ds_write_b64 v241, v[68:69] offset:6144\n"
    "v_accvgpr_read_b32 v96, a100\n"
    "v_accvgpr_read_b32 v97, a101\n"
    "v_accvgpr_read_b32 v98, a102\n"
    "v_accvgpr_read_b32 v99, a103\n"
    "v_pk_mul_f32 v[88:89], v[88:89], v[248:249] op_sel_hi:[1,0]\n"
    "v_pk_mul_f32 v[90:91], v[90:91], v[248:249] op_sel_hi:[1,0]\n"
    "v_cvt_pk_bf16_f32 v84, v80, v81\n"
    "v_cvt_pk_bf16_f32 v85, v82, v83\n"
    "ds_write_b64 v244, v[76:77] offset:6144\n"
    "v_accvgpr_read_b32 v104, a116\n"
    "v_accvgpr_read_b32 v105, a117\n"
    "v_accvgpr_read_b32 v106, a118\n"
    "v_accvgpr_read_b32 v107, a119\n"
    "v_pk_mul_f32 v[96:97], v[96:97], v[246:247] op_sel:[0,1]\n"
    "v_pk_mul_f32 v[98:99], v[98:99], v[246:247] op_sel:[0,1]\n"
    "v_cvt_pk_bf16_f32 v92, v88, v89\n"
    "v_cvt_pk_bf16_f32 v93, v90, v91\n"
    "ds_write_b64 v244, v[84:85] offset:4096\n"
    "v_accvgpr_read_b32 v112, a112\n"
    "v_accvgpr_read_b32 v113, a113\n"
    "v_accvgpr_read_b32 v114, a114\n"
    "v_accvgpr_read_b32 v115, a115\n"
    "v_pk_mul_f32 v[104:105], v[104:105], v[246:247] op_sel:[0,1]\n"
    "v_pk_mul_f32 v[106:107], v[106:107], v[246:247] op_sel:[0,1]\n"
    "v_cvt_pk_bf16_f32 v100, v96, v97\n"
    "v_cvt_pk_bf16_f32 v101, v98, v99\n"
    "ds_write_b64 v241, v[92:93] offset:4096\n"
    "v_accvgpr_read_b32 v120, a96\n"
    "v_accvgpr_read_b32 v121, a97\n"
    "v_accvgpr_read_b32 v122, a98\n"
    "v_accvgpr_read_b32 v123, a99\n"
    "v_pk_mul_f32 v[112:113], v[112:113], v[246:247] op_sel_hi:[1,0]\n"
    "v_pk_mul_f32 v[114:115], v[114:115], v[246:247] op_sel_hi:[1,0]\n"
    "v_cvt_pk_bf16_f32 v108, v104, v105\n"
    "v_cvt_pk_bf16_f32 v109, v106, v107\n"
    "ds_write_b64 v241, v[100:101] offset:2048\n"
    "v_accvgpr_read_b32 v64, a128\n"
    "v_accvgpr_read_b32 v65, a129\n"
    "v_accvgpr_read_b32 v66, a130\n"
    "v_accvgpr_read_b32 v67, a131\n"
    "v_pk_mul_f32 v[120:121], v[120:121], v[246:247] op_sel_hi:[1,0]\n"
    "v_pk_mul_f32 v[122:123], v[122:123], v[246:247] op_sel_hi:[1,0]\n"
    "v_cvt_pk_bf16_f32 v116, v112, v113\n"
    "v_cvt_pk_bf16_f32 v117, v114, v115\n"
    "ds_write_b64 v244, v[108:109] offset:2048\n"
    "v_accvgpr_read_b32 v72, a144\n"
    "v_accvgpr_read_b32 v73, a145\n"
    "v_accvgpr_read_b32 v74, a146\n"
    "v_accvgpr_read_b32 v75, a147\n"
    "v_pk_mul_f32 v[64:65], v[64:65], v[246:247] op_sel_hi:[1,0]\n"
    "v_pk_mul_f32 v[66:67], v[66:67], v[246:247] op_sel_hi:[1,0]\n"
    "v_cvt_pk_bf16_f32 v124, v120, v121\n"
    "v_cvt_pk_bf16_f32 v125, v122, v123\n"
    "ds_write_b64 v244, v[116:117] offset:0\n"
    "v_accvgpr_read_b32 v80, a148\n"
    "v_accvgpr_read_b32 v81, a149\n"
    "v_accvgpr_read_b32 v82, a150\n"
    "v_accvgpr_read_b32 v83, a151\n"
    "v_pk_mul_f32 v[72:73], v[72:73], v[246:247] op_sel_hi:[1,0]\n"
    "v_pk_mul_f32 v[74:75], v[74:75], v[246:247] op_sel_hi:[1,0]\n"
    "v_cvt_pk_bf16_f32 v68, v64, v65\n"
    "v_cvt_pk_bf16_f32 v69, v66, v67\n"
    "ds_write_b64 v241, v[124:125] offset:0\n"
    "ds_read_b128 v[32:35], v252 offset:0\n"
    "ds_read_b128 v[36:39], v252 offset:1024\n"
    "ds_read_b128 v[40:43], v252 offset:2048\n"
    "ds_read_b128 v[44:47], v252 offset:3072\n"
    "ds_read_b128 v[48:51], v252 offset:4096\n"
    "ds_read_b128 v[52:55], v252 offset:5120\n"
    "ds_read_b128 v[56:59], v252 offset:6144\n"
    "ds_read_b128 v[60:63], v252 offset:7168\n"
    "v_accvgpr_read_b32 v88, a132\n"
    "v_accvgpr_read_b32 v89, a133\n"
    "v_accvgpr_read_b32 v90, a134\n"
    "v_accvgpr_read_b32 v91, a135\n"
    "v_pk_mul_f32 v[80:81], v[80:81], v[246:247] op_sel:[0,1]\n"
    "v_pk_mul_f32 v[82:83], v[82:83], v[246:247] op_sel:[0,1]\n"
    "v_cvt_pk_bf16_f32 v76, v72, v73\n"
    "v_cvt_pk_bf16_f32 v77, v74, v75\n"
    "ds_write_b64 v251, v[68:69] offset:0\n"
    "v_accvgpr_read_b32 v96, a136\n"
    "v_accvgpr_read_b32 v97, a137\n"
    "v_accvgpr_read_b32 v98, a138\n"
    "v_accvgpr_read_b32 v99, a139\n"
    "v_pk_mul_f32 v[88:89], v[88:89], v[246:247] op_sel:[0,1]\n"
    "v_pk_mul_f32 v[90:91], v[90:91], v[246:247] op_sel:[0,1]\n"
    "v_cvt_pk_bf16_f32 v84, v80, v81\n"
    "v_cvt_pk_bf16_f32 v85, v82, v83\n"
    "ds_write_b64 v240, v[76:77] offset:0\n"
    "s_waitcnt lgkmcnt(9)\n"
    "buffer_store_dwordx4 v[32:35], v253, s[52:55], s67 offen offset:128 nt\n"
    "s_waitcnt lgkmcnt(8)\n"
    "buffer_store_dwordx4 v[36:39], v253, s[52:55], s48 offen offset:128 nt\n"
    "s_waitcnt lgkmcnt(7)\n"
    "buffer_store_dwordx4 v[40:43], v253, s[52:55], s49 offen offset:128 nt\n"
    "s_waitcnt lgkmcnt(6)\n"
    "buffer_store_dwordx4 v[44:47], v253, s[52:55], s50 offen offset:128 nt\n"
    "s_waitcnt lgkmcnt(5)\n"
    "buffer_store_dwordx4 v[48:51], v253, s[52:55], s51 offen offset:128 nt\n"
    "s_waitcnt lgkmcnt(4)\n"
    "buffer_store_dwordx4 v[52:55], v253, s[52:55], s36 offen offset:128 nt\n"
    "s_waitcnt lgkmcnt(3)\n"
    "buffer_store_dwordx4 v[56:59], v253, s[52:55], s37 offen offset:128 nt\n"
    "s_waitcnt lgkmcnt(2)\n"
    "buffer_store_dwordx4 v[60:63], v253, s[52:55], s38 offen offset:128 nt\n"
    "v_accvgpr_read_b32 v104, a152\n"
    "v_accvgpr_read_b32 v105, a153\n"
    "v_accvgpr_read_b32 v106, a154\n"
    "v_accvgpr_read_b32 v107, a155\n"
    "v_pk_mul_f32 v[96:97], v[96:97], v[248:249] op_sel_hi:[1,0]\n"
    "v_pk_mul_f32 v[98:99], v[98:99], v[248:249] op_sel_hi:[1,0]\n"
    "v_cvt_pk_bf16_f32 v92, v88, v89\n"
    "v_cvt_pk_bf16_f32 v93, v90, v91\n"
    "ds_write_b64 v240, v[84:85] offset:2048\n"
    "v_accvgpr_read_b32 v112, a156\n"
    "v_accvgpr_read_b32 v113, a157\n"
    "v_accvgpr_read_b32 v114, a158\n"
    "v_accvgpr_read_b32 v115, a159\n"
    "v_pk_mul_f32 v[104:105], v[104:105], v[248:249] op_sel_hi:[1,0]\n"
    "v_pk_mul_f32 v[106:107], v[106:107], v[248:249] op_sel_hi:[1,0]\n"
    "v_cvt_pk_bf16_f32 v100, v96, v97\n"
    "v_cvt_pk_bf16_f32 v101, v98, v99\n"
    "ds_write_b64 v251, v[92:93] offset:2048\n"
    "v_accvgpr_read_b32 v120, a140\n"
    "v_accvgpr_read_b32 v121, a141\n"
    "v_accvgpr_read_b32 v122, a142\n"
    "v_accvgpr_read_b32 v123, a143\n"
    "v_pk_mul_f32 v[112:113], v[112:113], v[248:249] op_sel:[0,1]\n"
    "v_pk_mul_f32 v[114:115], v[114:115], v[248:249] op_sel:[0,1]\n"
    "v_cvt_pk_bf16_f32 v108, v104, v105\n"
    "v_cvt_pk_bf16_f32 v109, v106, v107\n"
    "ds_write_b64 v251, v[100:101] offset:4096\n"
    "v_accvgpr_read_b32 v64, a172\n"
    "v_accvgpr_read_b32 v65, a173\n"
    "v_accvgpr_read_b32 v66, a174\n"
    "v_accvgpr_read_b32 v67, a175\n"
    "v_pk_mul_f32 v[120:121], v[120:121], v[248:249] op_sel:[0,1]\n"
    "v_pk_mul_f32 v[122:123], v[122:123], v[248:249] op_sel:[0,1]\n"
    "v_cvt_pk_bf16_f32 v116, v112, v113\n"
    "v_cvt_pk_bf16_f32 v117, v114, v115\n"
    "ds_write_b64 v240, v[108:109] offset:4096\n"
    "v_accvgpr_read_b32 v72, a188\n"
    "v_accvgpr_read_b32 v73, a189\n"
    "v_accvgpr_read_b32 v74, a190\n"
    "v_accvgpr_read_b32 v75, a191\n"
    "v_pk_mul_f32 v[64:65], v[64:65], v[248:249] op_sel:[0,1]\n"
    "v_pk_mul_f32 v[66:67], v[66:67], v[248:249] op_sel:[0,1]\n"
    "v_cvt_pk_bf16_f32 v124, v120, v121\n"
    "v_cvt_pk_bf16_f32 v125, v122, v123\n"
    "ds_write_b64 v240, v[116:117] offset:6144\n"
    "v_accvgpr_read_b32 v80, a184\n"
    "v_accvgpr_read_b32 v81, a185\n"
    "v_accvgpr_read_b32 v82, a186\n"
    "v_accvgpr_read_b32 v83, a187\n"
    "v_pk_mul_f32 v[72:73], v[72:73], v[248:249] op_sel:[0,1]\n"
    "v_pk_mul_f32 v[74:75], v[74:75], v[248:249] op_sel:[0,1]\n"
    "v_cvt_pk_bf16_f32 v68, v64, v65\n"
    "v_cvt_pk_bf16_f32 v69, v66, v67\n"
    "ds_write_b64 v251, v[124:125] offset:6144\n"
    "v_accvgpr_read_b32 v88, a168\n"
    "v_accvgpr_read_b32 v89, a169\n"
    "v_accvgpr_read_b32 v90, a170\n"
    "v_accvgpr_read_b32 v91, a171\n"
    "v_pk_mul_f32 v[80:81], v[80:81], v[248:249] op_sel_hi:[1,0]\n"
    "v_pk_mul_f32 v[82:83], v[82:83], v[248:249] op_sel_hi:[1,0]\n"
    "v_cvt_pk_bf16_f32 v76, v72, v73\n"
    "v_cvt_pk_bf16_f32 v77, v74, v75\n"
    "ds_write_b64 v241, v[68:69] offset:6144\n"
    "v_accvgpr_read_b32 v96, a164\n"
    "v_accvgpr_read_b32 v97, a165\n"
    "v_accvgpr_read_b32 v98, a166\n"
    "v_accvgpr_read_b32 v99, a167\n"
    "v_pk_mul_f32 v[88:89], v[88:89], v[248:249] op_sel_hi:[1,0]\n"
    "v_pk_mul_f32 v[90:91], v[90:91], v[248:249] op_sel_hi:[1,0]\n"
    "v_cvt_pk_bf16_f32 v84, v80, v81\n"
    "v_cvt_pk_bf16_f32 v85, v82, v83\n"
    "ds_write_b64 v244, v[76:77] offset:6144\n"
    "v_accvgpr_read_b32 v104, a180\n"
    "v_accvgpr_read_b32 v105, a181\n"
    "v_accvgpr_read_b32 v106, a182\n"
    "v_accvgpr_read_b32 v107, a183\n"
    "v_pk_mul_f32 v[96:97], v[96:97], v[246:247] op_sel:[0,1]\n"
    "v_pk_mul_f32 v[98:99], v[98:99], v[246:247] op_sel:[0,1]\n"
    "v_cvt_pk_bf16_f32 v92, v88, v89\n"
    "v_cvt_pk_bf16_f32 v93, v90, v91\n"
    "ds_write_b64 v244, v[84:85] offset:4096\n"
    "v_accvgpr_read_b32 v112, a176\n"
    "v_accvgpr_read_b32 v113, a177\n"
    "v_accvgpr_read_b32 v114, a178\n"
    "v_accvgpr_read_b32 v115, a179\n"
    "v_pk_mul_f32 v[104:105], v[104:105], v[246:247] op_sel:[0,1]\n"
    "v_pk_mul_f32 v[106:107], v[106:107], v[246:247] op_sel:[0,1]\n"
    "v_cvt_pk_bf16_f32 v100, v96, v97\n"
    "v_cvt_pk_bf16_f32 v101, v98, v99\n"
    "ds_write_b64 v241, v[92:93] offset:4096\n"
    "v_accvgpr_read_b32 v120, a160\n"
    "v_accvgpr_read_b32 v121, a161\n"
    "v_accvgpr_read_b32 v122, a162\n"
    "v_accvgpr_read_b32 v123, a163\n"
    "v_pk_mul_f32 v[112:113], v[112:113], v[246:247] op_sel_hi:[1,0]\n"
    "v_pk_mul_f32 v[114:115], v[114:115], v[246:247] op_sel_hi:[1,0]\n"
    "v_cvt_pk_bf16_f32 v108, v104, v105\n"
    "v_cvt_pk_bf16_f32 v109, v106, v107\n"
    "ds_write_b64 v241, v[100:101] offset:2048\n"
    "v_accvgpr_read_b32 v64, a192\n"
    "v_accvgpr_read_b32 v65, a193\n"
    "v_accvgpr_read_b32 v66, a194\n"
    "v_accvgpr_read_b32 v67, a195\n"
    "v_pk_mul_f32 v[120:121], v[120:121], v[246:247] op_sel_hi:[1,0]\n"
    "v_pk_mul_f32 v[122:123], v[122:123], v[246:247] op_sel_hi:[1,0]\n"
    "v_cvt_pk_bf16_f32 v116, v112, v113\n"
    "v_cvt_pk_bf16_f32 v117, v114, v115\n"
    "ds_write_b64 v244, v[108:109] offset:2048\n"
    "v_accvgpr_read_b32 v72, a208\n"
    "v_accvgpr_read_b32 v73, a209\n"
    "v_accvgpr_read_b32 v74, a210\n"
    "v_accvgpr_read_b32 v75, a211\n"
    "v_pk_mul_f32 v[64:65], v[64:65], v[246:247] op_sel_hi:[1,0]\n"
    "v_pk_mul_f32 v[66:67], v[66:67], v[246:247] op_sel_hi:[1,0]\n"
    "v_cvt_pk_bf16_f32 v124, v120, v121\n"
    "v_cvt_pk_bf16_f32 v125, v122, v123\n"
    "ds_write_b64 v244, v[116:117] offset:0\n"
    "v_accvgpr_read_b32 v80, a212\n"
    "v_accvgpr_read_b32 v81, a213\n"
    "v_accvgpr_read_b32 v82, a214\n"
    "v_accvgpr_read_b32 v83, a215\n"
    "v_pk_mul_f32 v[72:73], v[72:73], v[246:247] op_sel_hi:[1,0]\n"
    "v_pk_mul_f32 v[74:75], v[74:75], v[246:247] op_sel_hi:[1,0]\n"
    "v_cvt_pk_bf16_f32 v68, v64, v65\n"
    "v_cvt_pk_bf16_f32 v69, v66, v67\n"
    "ds_write_b64 v241, v[124:125] offset:0\n"
    "ds_read_b128 v[0:3], v252 offset:0\n"
    "ds_read_b128 v[4:7], v252 offset:1024\n"
    "ds_read_b128 v[8:11], v252 offset:2048\n"
    "ds_read_b128 v[12:15], v252 offset:3072\n"
    "ds_read_b128 v[16:19], v252 offset:4096\n"
    "ds_read_b128 v[20:23], v252 offset:5120\n"
    "ds_read_b128 v[24:27], v252 offset:6144\n"
    "ds_read_b128 v[28:31], v252 offset:7168\n"
    "v_accvgpr_read_b32 v88, a196\n"
    "v_accvgpr_read_b32 v89, a197\n"
    "v_accvgpr_read_b32 v90, a198\n"
    "v_accvgpr_read_b32 v91, a199\n"
    "v_pk_mul_f32 v[80:81], v[80:81], v[246:247] op_sel:[0,1]\n"
    "v_pk_mul_f32 v[82:83], v[82:83], v[246:247] op_sel:[0,1]\n"
    "v_cvt_pk_bf16_f32 v76, v72, v73\n"
    "v_cvt_pk_bf16_f32 v77, v74, v75\n"
    "ds_write_b64 v251, v[68:69] offset:0\n"
    "v_accvgpr_read_b32 v96, a200\n"
    "v_accvgpr_read_b32 v97, a201\n"
    "v_accvgpr_read_b32 v98, a202\n"
    "v_accvgpr_read_b32 v99, a203\n"
    "v_pk_mul_f32 v[88:89], v[88:89], v[246:247] op_sel:[0,1]\n"
    "v_pk_mul_f32 v[90:91], v[90:91], v[246:247] op_sel:[0,1]\n"
    "v_cvt_pk_bf16_f32 v84, v80, v81\n"
    "v_cvt_pk_bf16_f32 v85, v82, v83\n"
    "ds_write_b64 v240, v[76:77] offset:0\n"
    "s_waitcnt lgkmcnt(9)\n"
    "buffer_store_dwordx4 v[0:3], v253, s[52:55], s67 offen offset:256 nt\n"
    "s_waitcnt lgkmcnt(8)\n"
    "buffer_store_dwordx4 v[4:7], v253, s[52:55], s48 offen offset:256 nt\n"
    "s_waitcnt lgkmcnt(7)\n"
    "buffer_store_dwordx4 v[8:11], v253, s[52:55], s49 offen offset:256 nt\n"
    "s_waitcnt lgkmcnt(6)\n"
    "buffer_store_dwordx4 v[12:15], v253, s[52:55], s50 offen offset:256 nt\n"
    "s_waitcnt lgkmcnt(5)\n"
    "buffer_store_dwordx4 v[16:19], v253, s[52:55], s51 offen offset:256 nt\n"
    "s_waitcnt lgkmcnt(4)\n"
    "buffer_store_dwordx4 v[20:23], v253, s[52:55], s36 offen offset:256 nt\n"
    "s_waitcnt lgkmcnt(3)\n"
    "buffer_store_dwordx4 v[24:27], v253, s[52:55], s37 offen offset:256 nt\n"
    "s_waitcnt lgkmcnt(2)\n"
    "buffer_store_dwordx4 v[28:31], v253, s[52:55], s38 offen offset:256 nt\n"
    "v_accvgpr_read_b32 v104, a216\n"
    "v_accvgpr_read_b32 v105, a217\n"
    "v_accvgpr_read_b32 v106, a218\n"
    "v_accvgpr_read_b32 v107, a219\n"
    "v_pk_mul_f32 v[96:97], v[96:97], v[248:249] op_sel_hi:[1,0]\n"
    "v_pk_mul_f32 v[98:99], v[98:99], v[248:249] op_sel_hi:[1,0]\n"
    "v_cvt_pk_bf16_f32 v92, v88, v89\n"
    "v_cvt_pk_bf16_f32 v93, v90, v91\n"
    "ds_write_b64 v240, v[84:85] offset:2048\n"
    "v_accvgpr_read_b32 v112, a220\n"
    "v_accvgpr_read_b32 v113, a221\n"
    "v_accvgpr_read_b32 v114, a222\n"
    "v_accvgpr_read_b32 v115, a223\n"
    "v_pk_mul_f32 v[104:105], v[104:105], v[248:249] op_sel_hi:[1,0]\n"
    "v_pk_mul_f32 v[106:107], v[106:107], v[248:249] op_sel_hi:[1,0]\n"
    "v_cvt_pk_bf16_f32 v100, v96, v97\n"
    "v_cvt_pk_bf16_f32 v101, v98, v99\n"
    "ds_write_b64 v251, v[92:93] offset:2048\n"
    "v_accvgpr_read_b32 v120, a204\n"
    "v_accvgpr_read_b32 v121, a205\n"
    "v_accvgpr_read_b32 v122, a206\n"
    "v_accvgpr_read_b32 v123, a207\n"
    "v_pk_mul_f32 v[112:113], v[112:113], v[248:249] op_sel:[0,1]\n"
    "v_pk_mul_f32 v[114:115], v[114:115], v[248:249] op_sel:[0,1]\n"
    "v_cvt_pk_bf16_f32 v108, v104, v105\n"
    "v_cvt_pk_bf16_f32 v109, v106, v107\n"
    "ds_write_b64 v251, v[100:101] offset:4096\n"
    "v_accvgpr_read_b32 v64, a236\n"
    "v_accvgpr_read_b32 v65, a237\n"
    "v_accvgpr_read_b32 v66, a238\n"
    "v_accvgpr_read_b32 v67, a239\n"
    "v_pk_mul_f32 v[120:121], v[120:121], v[248:249] op_sel:[0,1]\n"
    "v_pk_mul_f32 v[122:123], v[122:123], v[248:249] op_sel:[0,1]\n"
    "v_cvt_pk_bf16_f32 v116, v112, v113\n"
    "v_cvt_pk_bf16_f32 v117, v114, v115\n"
    "ds_write_b64 v240, v[108:109] offset:4096\n"
    "v_accvgpr_read_b32 v72, a252\n"
    "v_accvgpr_read_b32 v73, a253\n"
    "v_accvgpr_read_b32 v74, a254\n"
    "v_accvgpr_read_b32 v75, a255\n"
    "v_pk_mul_f32 v[64:65], v[64:65], v[248:249] op_sel:[0,1]\n"
    "v_pk_mul_f32 v[66:67], v[66:67], v[248:249] op_sel:[0,1]\n"
    "v_cvt_pk_bf16_f32 v124, v120, v121\n"
    "v_cvt_pk_bf16_f32 v125, v122, v123\n"
    "ds_write_b64 v240, v[116:117] offset:6144\n"
    "v_accvgpr_read_b32 v80, a248\n"
    "v_accvgpr_read_b32 v81, a249\n"
    "v_accvgpr_read_b32 v82, a250\n"
    "v_accvgpr_read_b32 v83, a251\n"
    "v_pk_mul_f32 v[72:73], v[72:73], v[248:249] op_sel:[0,1]\n"
    "v_pk_mul_f32 v[74:75], v[74:75], v[248:249] op_sel:[0,1]\n"
    "v_cvt_pk_bf16_f32 v68, v64, v65\n"
    "v_cvt_pk_bf16_f32 v69, v66, v67\n"
    "ds_write_b64 v251, v[124:125] offset:6144\n"
    "v_accvgpr_read_b32 v88, a232\n"
    "v_accvgpr_read_b32 v89, a233\n"
    "v_accvgpr_read_b32 v90, a234\n"
    "v_accvgpr_read_b32 v91, a235\n"
    "v_pk_mul_f32 v[80:81], v[80:81], v[248:249] op_sel_hi:[1,0]\n"
    "v_pk_mul_f32 v[82:83], v[82:83], v[248:249] op_sel_hi:[1,0]\n"
    "v_cvt_pk_bf16_f32 v76, v72, v73\n"
    "v_cvt_pk_bf16_f32 v77, v74, v75\n"
    "ds_write_b64 v241, v[68:69] offset:6144\n"
    "v_accvgpr_read_b32 v96, a228\n"
    "v_accvgpr_read_b32 v97, a229\n"
    "v_accvgpr_read_b32 v98, a230\n"
    "v_accvgpr_read_b32 v99, a231\n"
    "v_pk_mul_f32 v[88:89], v[88:89], v[248:249] op_sel_hi:[1,0]\n"
    "v_pk_mul_f32 v[90:91], v[90:91], v[248:249] op_sel_hi:[1,0]\n"
    "v_cvt_pk_bf16_f32 v84, v80, v81\n"
    "v_cvt_pk_bf16_f32 v85, v82, v83\n"
    "ds_write_b64 v244, v[76:77] offset:6144\n"
    "v_accvgpr_read_b32 v104, a244\n"
    "v_accvgpr_read_b32 v105, a245\n"
    "v_accvgpr_read_b32 v106, a246\n"
    "v_accvgpr_read_b32 v107, a247\n"
    "v_pk_mul_f32 v[96:97], v[96:97], v[246:247] op_sel:[0,1]\n"
    "v_pk_mul_f32 v[98:99], v[98:99], v[246:247] op_sel:[0,1]\n"
    "v_cvt_pk_bf16_f32 v92, v88, v89\n"
    "v_cvt_pk_bf16_f32 v93, v90, v91\n"
    "ds_write_b64 v244, v[84:85] offset:4096\n"
    "v_accvgpr_read_b32 v112, a240\n"
    "v_accvgpr_read_b32 v113, a241\n"
    "v_accvgpr_read_b32 v114, a242\n"
    "v_accvgpr_read_b32 v115, a243\n"
    "v_pk_mul_f32 v[104:105], v[104:105], v[246:247] op_sel:[0,1]\n"
    "v_pk_mul_f32 v[106:107], v[106:107], v[246:247] op_sel:[0,1]\n"
    "v_cvt_pk_bf16_f32 v100, v96, v97\n"
    "v_cvt_pk_bf16_f32 v101, v98, v99\n"
    "ds_write_b64 v241, v[92:93] offset:4096\n"
    "v_accvgpr_read_b32 v120, a224\n"
    "v_accvgpr_read_b32 v121, a225\n"
    "v_accvgpr_read_b32 v122, a226\n"
    "v_accvgpr_read_b32 v123, a227\n"
    "v_pk_mul_f32 v[112:113], v[112:113], v[246:247] op_sel_hi:[1,0]\n"
    "v_pk_mul_f32 v[114:115], v[114:115], v[246:247] op_sel_hi:[1,0]\n"
    "v_cvt_pk_bf16_f32 v108, v104, v105\n"
    "v_cvt_pk_bf16_f32 v109, v106, v107\n"
    "ds_write_b64 v241, v[100:101] offset:2048\n"
    "v_pk_mul_f32 v[120:121], v[120:121], v[246:247] op_sel_hi:[1,0]\n"
    "v_pk_mul_f32 v[122:123], v[122:123], v[246:247] op_sel_hi:[1,0]\n"
    "v_cvt_pk_bf16_f32 v116, v112, v113\n"
    "v_cvt_pk_bf16_f32 v117, v114, v115\n"
    "ds_write_b64 v244, v[108:109] offset:2048\n"
    "v_cvt_pk_bf16_f32 v124, v120, v121\n"
    "v_cvt_pk_bf16_f32 v125, v122, v123\n"
    "ds_write_b64 v244, v[116:117] offset:0\n"
    "ds_write_b64 v241, v[124:125] offset:0\n"
    "ds_read_b128 v[32:35], v252 offset:0\n"
    "ds_read_b128 v[36:39], v252 offset:1024\n"
    "ds_read_b128 v[40:43], v252 offset:2048\n"
    "ds_read_b128 v[44:47], v252 offset:3072\n"
    "ds_read_b128 v[48:51], v252 offset:4096\n"
    "ds_read_b128 v[52:55], v252 offset:5120\n"
    "ds_read_b128 v[56:59], v252 offset:6144\n"
    "ds_read_b128 v[60:63], v252 offset:7168\n"
    "s_waitcnt lgkmcnt(7)\n"
    "buffer_store_dwordx4 v[32:35], v253, s[52:55], s67 offen offset:384 nt\n"
    "s_waitcnt lgkmcnt(6)\n"
    "buffer_store_dwordx4 v[36:39], v253, s[52:55], s48 offen offset:384 nt\n"
    "s_waitcnt lgkmcnt(5)\n"
    "buffer_store_dwordx4 v[40:43], v253, s[52:55], s49 offen offset:384 nt\n"
    "s_waitcnt lgkmcnt(4)\n"
    "buffer_store_dwordx4 v[44:47], v253, s[52:55], s50 offen offset:384 nt\n"
    "s_waitcnt lgkmcnt(3)\n"
    "buffer_store_dwordx4 v[48:51], v253, s[52:55], s51 offen offset:384 nt\n"
    "s_waitcnt lgkmcnt(2)\n"
    "buffer_store_dwordx4 v[52:55], v253, s[52:55], s36 offen offset:384 nt\n"
    "s_waitcnt lgkmcnt(1)\n"
    "buffer_store_dwordx4 v[56:59], v253, s[52:55], s37 offen offset:384 nt\n"
    "s_waitcnt lgkmcnt(0)\n"
    "buffer_store_dwordx4 v[60:63], v253, s[52:55], s38 offen offset:384 nt\n"
    "s_endpgm\n"
    "L_hotend_%=:\n"
    "L_redo_%=:\n"
    "s_waitcnt vmcnt(0) lgkmcnt(0)\n"
    "s_nop 15\n"
    "s_barrier\n"
    "s_load_dwordx8 s[92:99], %[ka], 0x0\n"
    "v_mbcnt_lo_u32_b32 v251, -1, 0\n"
    "v_mbcnt_hi_u32_b32 v251, -1, v251\n"
    "s_lshr_b32 s61, %[blk], 4\n"
    "v_lshrrev_b32 v254, 4, v251\n"
    "s_lshl_b32 s61, s61, 3\n"
    "v_lshrrev_b32 v252, 3, v251\n"
    "s_and_b32 s60, %[blk], 7\n"
    "v_xor_b32 v255, v251, v254\n"
    "s_or_b32 s61, s61, s60\n"
    "v_and_b32 v255, 7, v255\n"
    "s_bfe_u32 s60, %[blk], 0x10003\n"
    "v_lshlrev_b32 v255, 4, v255\n"
    "s_lshl1_add_u32 s60, s61, s60\n"
    "v_lshl_add_u32 v255, v252, 9, v255\n"
    "s_lshl_b32 s60, s60, 17\n"
    "v_xor_b32 v250, 64, v255\n"
    "s_lshl_b32 s61, s61, 19\n"
    "s_xor_b32 s36, %[w], 2\n"
    "s_lshl_b32 s36, s36, 14\n"
    "s_add_u32 s36, s36, 0x8000\n"
    "s_lshl_b32 s38, %[w], 15\n"
    "s_waitcnt lgkmcnt(0)\n"
    "s_add_u32 s48, s92, s60\n"
    "s_addc_u32 s49, s93, 0\n"
    "s_and_b32 s49, s49, 0xffff\n"
    "s_mov_b32 s50, 0x20000\n"
    "s_mov_b32 s51, 0x20000\n"
    "s_add_u32 m0, s36, 0x0\n"
    "s_add_u32 s64, s38, 0x0\n"
    "buffer_load_dwordx4 v255, s[48:51], s64 offen offset:0 nt lds\n"
    "s_add_u32 m0, s36, 0x400\n"
    "s_add_u32 s64, s38, 0x1000\n"
    "buffer_load_dwordx4 v250, s[48:51], s64 offen offset:0 nt lds\n"
    "s_add_u32 m0, s36, 0x800\n"
    "s_add_u32 s64, s38, 0x2000\n"
    "buffer_load_dwordx4 v255, s[48:51], s64 offen offset:0 nt lds\n"
    "s_add_u32 m0, s36, 0xc00\n"
    "s_add_u32 s64, s38, 0x3000\n"
    "buffer_load_dwordx4 v250, s[48:51], s64 offen offset:0 nt lds\n"
    "s_lshl_b32 s37, %[w], 14\n"
    "s_add_u32 s37, s37, 0x18000\n"
    "s_add_u32 s40, s94, s61\n"
    "s_addc_u32 s41, s95, 0\n"
    "s_and_b32 s41, s41, 0xffff\n"
    "s_mov_b32 s42, 0x80000\n"
    "s_mov_b32 s43, 0x20000\n"
    "s_lshl_b32 s58, %[w], 10\n"
    "s_lshl_b32 s59, %[w], 12\n"
    "s_lshl_b32 s65, %[w], 2\n"
    "v_add_u32 v244, s65, v254\n"
    "v_xor_b32 v244, v251, v244\n"
    "v_and_b32 v244, 7, v244\n"
    "v_lshlrev_b32 v244, 4, v244\n"
    "v_lshl_add_u32 v244, v252, 9, v244\n"
    "v_add_u32 v244, s59, v244\n"
    "s_mov_b32 s56, 0x0\n"
    "s_add_u32 m0, s58, 0x0\n"
    "s_nop 0\n"
    "buffer_load_dwordx4 v244, s[40:43], s56 offen offset:0 nt lds\n"
    "s_add_u32 m0, s58, 0xf80\n"
    "s_nop 0\n"
    "buffer_load_dwordx4 v244, s[40:43], s56 offen offset:128 nt lds\n"
    "s_add_u32 m0, s58, 0x1f00\n"
    "s_nop 0\n"
    "buffer_load_dwordx4 v244, s[40:43], s56 offen offset:256 nt lds\n"
    "s_add_u32 m0, s58, 0x2e80\n"
    "s_nop 0\n"
    "buffer_load_dwordx4 v244, s[40:43], s56 offen offset:384 nt lds\n"
    "s_add_u32 m0, s36, 0xf80\n"
    "s_add_u32 s64, s38, 0x0\n"
    "buffer_load_dwordx4 v255, s[48:51], s64 offen offset:128 nt lds\n"
    "s_add_u32 m0, s36, 0x1380\n"
    "s_add_u32 s64, s38, 0x1000\n"
    "buffer_load_dwordx4 v250, s[48:51], s64 offen offset:128 nt lds\n"
    "s_add_u32 m0, s36, 0x1780\n"
    "s_add_u32 s64, s38, 0x2000\n"
    "buffer_load_dwordx4 v255, s[48:51], s64 offen offset:128 nt lds\n"
    "s_add_u32 m0, s36, 0x1b80\n"
    "s_add_u32 s64, s38, 0x3000\n"
    "buffer_load_dwordx4 v250, s[48:51], s64 offen offset:128 nt lds\n"
    "s_add_u32 m0, s36, 0x1f00\n"
    "s_add_u32 s64, s38, 0x0\n"
    "buffer_load_dwordx4 v255, s[48:51], s64 offen offset:256 nt lds\n"
    "s_add_u32 m0, s36, 0x2300\n"
    "s_add_u32 s64, s38, 0x1000\n"
    "buffer_load_dwordx4 v250, s[48:51], s64 offen offset:256 nt lds\n"
    "s_add_u32 m0, s36, 0x2700\n"
    "s_add_u32 s64, s38, 0x2000\n"
    "buffer_load_dwordx4 v255, s[48:51], s64 offen offset:256 nt lds\n"
    "s_add_u32 m0, s36, 0x2b00\n"
    "s_add_u32 s64, s38, 0x3000\n"
    "buffer_load_dwordx4 v250, s[48:51], s64 offen offset:256 nt lds\n"
    "s_add_u32 m0, s36, 0x2e80\n"
    "s_add_u32 s64, s38, 0x0\n"
    "buffer_load_dwordx4 v255, s[48:51], s64 offen offset:384 nt lds\n"
    "s_add_u32 m0, s36, 0x3280\n"
    "s_add_u32 s64, s38, 0x1000\n"
    "buffer_load_dwordx4 v250, s[48:51], s64 offen offset:384 nt lds\n"
    "s_add_u32 m0, s36, 0x3680\n"
    "s_add_u32 s64, s38, 0x2000\n"
    "buffer_load_dwordx4 v255, s[48:51], s64 offen offset:384 nt lds\n"
    "s_add_u32 m0, s36, 0x3a80\n"
    "s_add_u32 s64, s38, 0x3000\n"
    "buffer_load_dwordx4 v250, s[48:51], s64 offen offset:384 nt lds\n"
    "s_add_u32 s44, s96, s61\n"
    "s_addc_u32 s45, s97, 0\n"
    "s_and_b32 s45, s45, 0xffff\n"
    "s_mov_b32 s46, 0x80000\n"
    "s_mov_b32 s47, 0x20000\n"
    "s_add_u32 s52, s98, s60\n"
    "s_addc_u32 s53, s99, 0\n"
    "s_and_b32 s53, s53, 0xffff\n"
    "s_mov_b32 s54, 0x20000\n"
    "s_mov_b32 s55, 0x20000\n"
    "v_lshrrev_b32 v254, 2, v251\n"
    "v_and_b32 v254, 7, v254\n"
    "v_lshlrev_b32 v245, 9, v254\n"
    "v_lshrrev_b32 v254, 1, v254\n"
    "v_and_b32 v254, 2, v254\n"
    "v_xor_b32 v254, v251, v254\n"
    "v_and_b32 v254, 3, v254\n"
    "v_lshl_add_u32 v245, v254, 4, v245\n"
    "v_lshrrev_b32 v254, 5, v251\n"
    "v_lshl_add_u32 v245, v254, 6, v245\n"
    "v_add_u32 v245, s59, v245\n"
    "s_add_u32 s59, s59, 0x10000\n"
    "s_mov_b32 s66, 0x3db8aa3b\n"
    "v_and_b32 v252, 15, v251\n"
    "v_lshrrev_b32 v253, 4, v251\n"
    "v_lshrrev_b32 v254, 1, v253\n"
    "v_lshlrev_b32 v242, 12, v254\n"
    "v_and_b32 v254, 1, v253\n"
    "v_mul_u32_u24 v254, 0x120, v254\n"
    "v_add_u32 v242, v254, v242\n"
    "v_lshrrev_b32 v254, 2, v252\n"
    "v_lshl_add_u32 v242, v254, 6, v242\n"
    "v_and_b32 v254, 3, v252\n"
    "v_lshl_add_u32 v242, v254, 3, v242\n"
    "v_add_u32 v242, 0x10000, v242\n"
    "v_xor_b32 v243, 32, v242\n"
    "v_lshrrev_b32 v254, 1, v252\n"
    "v_xor_b32 v254, v253, v254\n"
    "v_lshlrev_b32 v254, 4, v254\n"
    "v_lshl_add_u32 v196, v252, 7, v254\n"
    "v_xor_b32 v197, 64, v196\n"
    "v_mov_b32 v246, 0\n"
    "v_mov_b32 v247, 0\n"
    "v_mov_b32 v248, 0\n"
    "v_mov_b32 v249, 0\n"
    "v_mov_b32 v236, 0\n"
    "v_mov_b32 v237, 0\n"
    "v_mov_b32 v238, 0\n"
    "v_mov_b32 v239, 0\n"
    "s_nop 1\n"
    "v_mfma_f32_32x32x16_bf16 a[0:15], v[236:239], v[236:239], 0\n"
    "v_mfma_f32_32x32x16_bf16 a[16:31], v[236:239], v[236:239], 0\n"
    "v_mfma_f32_32x32x16_bf16 a[32:47], v[236:239], v[236:239], 0\n"
    "v_mfma_f32_32x32x16_bf16 a[48:63], v[236:239], v[236:239], 0\n"
    "v_mfma_f32_32x32x16_bf16 a[64:79], v[236:239], v[236:239], 0\n"
    "v_mfma_f32_32x32x16_bf16 a[80:95], v[236:239], v[236:239], 0\n"
    "v_mfma_f32_32x32x16_bf16 a[96:111], v[236:239], v[236:239], 0\n"
    "v_mfma_f32_32x32x16_bf16 a[112:127], v[236:239], v[236:239], 0\n"
    "v_mfma_f32_32x32x16_bf16 a[128:143], v[236:239], v[236:239], 0\n"
    "v_mfma_f32_32x32x16_bf16 a[144:159], v[236:239], v[236:239], 0\n"
    "v_mfma_f32_32x32x16_bf16 a[160:175], v[236:239], v[236:239], 0\n"
    "v_mfma_f32_32x32x16_bf16 a[176:191], v[236:239], v[236:239], 0\n"
    "v_mfma_f32_32x32x16_bf16 a[192:207], v[236:239], v[236:239], 0\n"
    "v_mfma_f32_32x32x16_bf16 a[208:223], v[236:239], v[236:239], 0\n"
    "v_mfma_f32_32x32x16_bf16 a[224:239], v[236:239], v[236:239], 0\n"
    "v_mfma_f32_32x32x16_bf16 a[240:255], v[236:239], v[236:239], 0\n"
    "s_mov_b32 s67, 0x3db8aa3b\n"
    "v_add_u32 v192, s36, v196\n"
    "v_add_u32 v193, s36, v197\n"
    "s_add_u32 m0, s37, 0x0\n"
    "s_add_u32 s64, s38, 0x4000\n"
    "buffer_load_dwordx4 v255, s[48:51], s64 offen offset:0 nt lds\n"
    "s_add_u32 m0, s37, 0x400\n"
    "s_add_u32 s64, s38, 0x5000\n"
    "buffer_load_dwordx4 v250, s[48:51], s64 offen offset:0 nt lds\n"
    "s_add_u32 m0, s37, 0x800\n"
    "s_add_u32 s64, s38, 0x6000\n"
    "buffer_load_dwordx4 v255, s[48:51], s64 offen offset:0 nt lds\n"
    "s_add_u32 m0, s37, 0xc00\n"
    "s_add_u32 s64, s38, 0x7000\n"
    "buffer_load_dwordx4 v250, s[48:51], s64 offen offset:0 nt lds\n"
    "s_waitcnt vmcnt(20)\n"
    "ds_read_b128 v[0:3], v192 offset:0\n"
    "ds_read_b128 v[32:35], v192 offset:2048\n"
    "ds_read_b128 v[4:7], v193 offset:0\n"
    "ds_read_b128 v[36:39], v193 offset:2048\n"
    "s_waitcnt lgkmcnt(3)\n"
    "v_lshlrev_b32 v160, 16, v0\n"
    "v_and_b32 v161, 0xffff0000, v0\n"
    "v_lshlrev_b32 v162, 16, v1\n"
    "v_and_b32 v163, 0xffff0000, v1\n"
    "v_lshlrev_b32 v164, 16, v2\n"
    "v_and_b32 v165, 0xffff0000, v2\n"
    "v_lshlrev_b32 v166, 16, v3\n"
    "v_and_b32 v167, 0xffff0000, v3\n"
    "v_pk_mul_f32 v[160:161], v[160:161], s[66:67]\n"
    "v_pk_mul_f32 v[162:163], v[162:163], s[66:67]\n"
    "v_pk_mul_f32 v[164:165], v[164:165], s[66:67]\n"
    "v_pk_mul_f32 v[166:167], v[166:167], s[66:67]\n"
    "v_cvt_pk_bf16_f32 v0, v160, v161\n"
    "v_cvt_pk_bf16_f32 v1, v162, v163\n"
    "v_cvt_pk_bf16_f32 v2, v164, v165\n"
    "v_cvt_pk_bf16_f32 v3, v166, v167\n"
    "s_waitcnt lgkmcnt(2)\n"
    "v_lshlrev_b32 v168, 16, v32\n"
    "v_and_b32 v169, 0xffff0000, v32\n"
    "v_lshlrev_b32 v170, 16, v33\n"
    "v_and_b32 v171, 0xffff0000, v33\n"
    "v_lshlrev_b32 v172, 16, v34\n"
    "v_and_b32 v173, 0xffff0000, v34\n"
    "v_lshlrev_b32 v174, 16, v35\n"
    "v_and_b32 v175, 0xffff0000, v35\n"
    "v_pk_mul_f32 v[168:169], v[168:169], s[66:67]\n"
    "v_pk_mul_f32 v[170:171], v[170:171], s[66:67]\n"
    "v_pk_mul_f32 v[172:173], v[172:173], s[66:67]\n"
    "v_pk_mul_f32 v[174:175], v[174:175], s[66:67]\n"
    "v_cvt_pk_bf16_f32 v32, v168, v169\n"
    "v_cvt_pk_bf16_f32 v33, v170, v171\n"
    "v_cvt_pk_bf16_f32 v34, v172, v173\n"
    "v_cvt_pk_bf16_f32 v35, v174, v175\n"
    "s_waitcnt lgkmcnt(1)\n"
    "v_lshlrev_b32 v160, 16, v4\n"
    "v_and_b32 v161, 0xffff0000, v4\n"
    "v_lshlrev_b32 v162, 16, v5\n"
    "v_and_b32 v163, 0xffff0000, v5\n"
    "v_lshlrev_b32 v164, 16, v6\n"
    "v_and_b32 v165, 0xffff0000, v6\n"
    "v_lshlrev_b32 v166, 16, v7\n"
    "v_and_b32 v167, 0xffff0000, v7\n"
    "v_pk_mul_f32 v[160:161], v[160:161], s[66:67]\n"
    "v_pk_mul_f32 v[162:163], v[162:163], s[66:67]\n"
    "v_pk_mul_f32 v[164:165], v[164:165], s[66:67]\n"
    "v_pk_mul_f32 v[166:167], v[166:167], s[66:67]\n"
    "v_cvt_pk_bf16_f32 v4, v160, v161\n"
    "v_cvt_pk_bf16_f32 v5, v162, v163\n"
    "v_cvt_pk_bf16_f32 v6, v164, v165\n"
    "v_cvt_pk_bf16_f32 v7, v166, v167\n"
    "s_waitcnt lgkmcnt(0)\n"
    "v_lshlrev_b32 v168, 16, v36\n"
    "v_and_b32 v169, 0xffff0000, v36\n"
    "v_lshlrev_b32 v170, 16, v37\n"
    "v_and_b32 v171, 0xffff0000, v37\n"
    "v_lshlrev_b32 v172, 16, v38\n"
    "v_and_b32 v173, 0xffff0000, v38\n"
    "v_lshlrev_b32 v174, 16, v39\n"
    "v_and_b32 v175, 0xffff0000, v39\n"
    "v_pk_mul_f32 v[168:169], v[168:169], s[66:67]\n"
    "v_pk_mul_f32 v[170:171], v[170:171], s[66:67]\n"
    "v_pk_mul_f32 v[172:173], v[172:173], s[66:67]\n"
    "v_pk_mul_f32 v[174:175], v[174:175], s[66:67]\n"
    "v_cvt_pk_bf16_f32 v36, v168, v169\n"
    "v_cvt_pk_bf16_f32 v37, v170, v171\n"
    "v_cvt_pk_bf16_f32 v38, v172, v173\n"
    "v_cvt_pk_bf16_f32 v39, v174, v175\n"
    "s_add_u32 m0, s37, 0xf80\n"
    "s_add_u32 s64, s38, 0x4000\n"
    "buffer_load_dwordx4 v255, s[48:51], s64 offen offset:128 nt lds\n"
    "s_add_u32 m0, s37, 0x1380\n"
    "s_add_u32 s64, s38, 0x5000\n"
    "buffer_load_dwordx4 v250, s[48:51], s64 offen offset:128 nt lds\n"
    "s_add_u32 m0, s37, 0x1780\n"
    "s_add_u32 s64, s38, 0x6000\n"
    "buffer_load_dwordx4 v255, s[48:51], s64 offen offset:128 nt lds\n"
    "s_add_u32 m0, s37, 0x1b80\n"
    "s_add_u32 s64, s38, 0x7000\n"
    "buffer_load_dwordx4 v250, s[48:51], s64 offen offset:128 nt lds\n"
    "s_waitcnt vmcnt(16)\n"
    "ds_read_b128 v[8:11], v192 offset:4096\n"
    "ds_read_b128 v[40:43], v192 offset:6144\n"
    "ds_read_b128 v[12:15], v193 offset:4096\n"
    "ds_read_b128 v[44:47], v193 offset:6144\n"
    "s_waitcnt lgkmcnt(3)\n"
    "v_lshlrev_b32 v160, 16, v8\n"
    "v_and_b32 v161, 0xffff0000, v8\n"
    "v_lshlrev_b32 v162, 16, v9\n"
    "v_and_b32 v163, 0xffff0000, v9\n"
    "v_lshlrev_b32 v164, 16, v10\n"
    "v_and_b32 v165, 0xffff0000, v10\n"
    "v_lshlrev_b32 v166, 16, v11\n"
    "v_and_b32 v167, 0xffff0000, v11\n"
    "v_pk_mul_f32 v[160:161], v[160:161], s[66:67]\n"
    "v_pk_mul_f32 v[162:163], v[162:163], s[66:67]\n"
    "v_pk_mul_f32 v[164:165], v[164:165], s[66:67]\n"
    "v_pk_mul_f32 v[166:167], v[166:167], s[66:67]\n"
    "v_cvt_pk_bf16_f32 v8, v160, v161\n"
    "v_cvt_pk_bf16_f32 v9, v162, v163\n"
    "v_cvt_pk_bf16_f32 v10, v164, v165\n"
    "v_cvt_pk_bf16_f32 v11, v166, v167\n"
    "s_waitcnt lgkmcnt(2)\n"
    "v_lshlrev_b32 v168, 16, v40\n"
    "v_and_b32 v169, 0xffff0000, v40\n"
    "v_lshlrev_b32 v170, 16, v41\n"
    "v_and_b32 v171, 0xffff0000, v41\n"
    "v_lshlrev_b32 v172, 16, v42\n"
    "v_and_b32 v173, 0xffff0000, v42\n"
    "v_lshlrev_b32 v174, 16, v43\n"
    "v_and_b32 v175, 0xffff0000, v43\n"
    "v_pk_mul_f32 v[168:169], v[168:169], s[66:67]\n"
    "v_pk_mul_f32 v[170:171], v[170:171], s[66:67]\n"
    "v_pk_mul_f32 v[172:173], v[172:173], s[66:67]\n"
    "v_pk_mul_f32 v[174:175], v[174:175], s[66:67]\n"
    "v_cvt_pk_bf16_f32 v40, v168, v169\n"
    "v_cvt_pk_bf16_f32 v41, v170, v171\n"
    "v_cvt_pk_bf16_f32 v42, v172, v173\n"
    "v_cvt_pk_bf16_f32 v43, v174, v175\n"
    "s_waitcnt lgkmcnt(1)\n"
    "v_lshlrev_b32 v160, 16, v12\n"
    "v_and_b32 v161, 0xffff0000, v12\n"
    "v_lshlrev_b32 v162, 16, v13\n"
    "v_and_b32 v163, 0xffff0000, v13\n"
    "v_lshlrev_b32 v164, 16, v14\n"
    "v_and_b32 v165, 0xffff0000, v14\n"
    "v_lshlrev_b32 v166, 16, v15\n"
    "v_and_b32 v167, 0xffff0000, v15\n"
    "v_pk_mul_f32 v[160:161], v[160:161], s[66:67]\n"
    "v_pk_mul_f32 v[162:163], v[162:163], s[66:67]\n"
    "v_pk_mul_f32 v[164:165], v[164:165], s[66:67]\n"
    "v_pk_mul_f32 v[166:167], v[166:167], s[66:67]\n"
    "v_cvt_pk_bf16_f32 v12, v160, v161\n"
    "v_cvt_pk_bf16_f32 v13, v162, v163\n"
    "v_cvt_pk_bf16_f32 v14, v164, v165\n"
    "v_cvt_pk_bf16_f32 v15, v166, v167\n"
    "s_waitcnt lgkmcnt(0)\n"
    "v_lshlrev_b32 v168, 16, v44\n"
    "v_and_b32 v169, 0xffff0000, v44\n"
    "v_lshlrev_b32 v170, 16, v45\n"
    "v_and_b32 v171, 0xffff0000, v45\n"
    "v_lshlrev_b32 v172, 16, v46\n"
    "v_and_b32 v173, 0xffff0000, v46\n"
    "v_lshlrev_b32 v174, 16, v47\n"
    "v_and_b32 v175, 0xffff0000, v47\n"
    "v_pk_mul_f32 v[168:169], v[168:169], s[66:67]\n"
    "v_pk_mul_f32 v[170:171], v[170:171], s[66:67]\n"
    "v_pk_mul_f32 v[172:173], v[172:173], s[66:67]\n"
    "v_pk_mul_f32 v[174:175], v[174:175], s[66:67]\n"
    "v_cvt_pk_bf16_f32 v44, v168, v169\n"
    "v_cvt_pk_bf16_f32 v45, v170, v171\n"
    "v_cvt_pk_bf16_f32 v46, v172, v173\n"
    "v_cvt_pk_bf16_f32 v47, v174, v175\n"
    "s_add_u32 m0, s37, 0x1f00\n"
    "s_add_u32 s64, s38, 0x4000\n"
    "buffer_load_dwordx4 v255, s[48:51], s64 offen offset:256 nt lds\n"
    "s_add_u32 m0, s37, 0x2300\n"
    "s_add_u32 s64, s38, 0x5000\n"
    "buffer_load_dwordx4 v250, s[48:51], s64 offen offset:256 nt lds\n"
    "s_add_u32 m0, s37, 0x2700\n"
    "s_add_u32 s64, s38, 0x6000\n"
    "buffer_load_dwordx4 v255, s[48:51], s64 offen offset:256 nt lds\n"
    "s_add_u32 m0, s37, 0x2b00\n"
    "s_add_u32 s64, s38, 0x7000\n"
    "buffer_load_dwordx4 v250, s[48:51], s64 offen offset:256 nt lds\n"
    "s_waitcnt vmcnt(16)\n"
    "ds_read_b128 v[16:19], v192 offset:8192\n"
    "ds_read_b128 v[48:51], v192 offset:10240\n"
    "ds_read_b128 v[20:23], v193 offset:8192\n"
    "ds_read_b128 v[52:55], v193 offset:10240\n"
    "s_waitcnt lgkmcnt(3)\n"
    "v_lshlrev_b32 v160, 16, v16\n"
    "v_and_b32 v161, 0xffff0000, v16\n"
    "v_lshlrev_b32 v162, 16, v17\n"
    "v_and_b32 v163, 0xffff0000, v17\n"
    "v_lshlrev_b32 v164, 16, v18\n"
    "v_and_b32 v165, 0xffff0000, v18\n"
    "v_lshlrev_b32 v166, 16, v19\n"
    "v_and_b32 v167, 0xffff0000, v19\n"
    "v_pk_mul_f32 v[160:161], v[160:161], s[66:67]\n"
    "v_pk_mul_f32 v[162:163], v[162:163], s[66:67]\n"
    "v_pk_mul_f32 v[164:165], v[164:165], s[66:67]\n"
    "v_pk_mul_f32 v[166:167], v[166:167], s[66:67]\n"
    "v_cvt_pk_bf16_f32 v16, v160, v161\n"
    "v_cvt_pk_bf16_f32 v17, v162, v163\n"
    "v_cvt_pk_bf16_f32 v18, v164, v165\n"
    "v_cvt_pk_bf16_f32 v19, v166, v167\n"
    "s_waitcnt lgkmcnt(2)\n"
    "v_lshlrev_b32 v168, 16, v48\n"
    "v_and_b32 v169, 0xffff0000, v48\n"
    "v_lshlrev_b32 v170, 16, v49\n"
    "v_and_b32 v171, 0xffff0000, v49\n"
    "v_lshlrev_b32 v172, 16, v50\n"
    "v_and_b32 v173, 0xffff0000, v50\n"
    "v_lshlrev_b32 v174, 16, v51\n"
    "v_and_b32 v175, 0xffff0000, v51\n"
    "v_pk_mul_f32 v[168:169], v[168:169], s[66:67]\n"
    "v_pk_mul_f32 v[170:171], v[170:171], s[66:67]\n"
    "v_pk_mul_f32 v[172:173], v[172:173], s[66:67]\n"
    "v_pk_mul_f32 v[174:175], v[174:175], s[66:67]\n"
    "v_cvt_pk_bf16_f32 v48, v168, v169\n"
    "v_cvt_pk_bf16_f32 v49, v170, v171\n"
    "v_cvt_pk_bf16_f32 v50, v172, v173\n"
    "v_cvt_pk_bf16_f32 v51, v174, v175\n"
    "s_waitcnt lgkmcnt(1)\n"
    "v_lshlrev_b32 v160, 16, v20\n"
    "v_and_b32 v161, 0xffff0000, v20\n"
    "v_lshlrev_b32 v162, 16, v21\n"
    "v_and_b32 v163, 0xffff0000, v21\n"
    "v_lshlrev_b32 v164, 16, v22\n"
    "v_and_b32 v165, 0xffff0000, v22\n"
    "v_lshlrev_b32 v166, 16, v23\n"
    "v_and_b32 v167, 0xffff0000, v23\n"
    "v_pk_mul_f32 v[160:161], v[160:161], s[66:67]\n"
    "v_pk_mul_f32 v[162:163], v[162:163], s[66:67]\n"
    "v_pk_mul_f32 v[164:165], v[164:165], s[66:67]\n"
    "v_pk_mul_f32 v[166:167], v[166:167], s[66:67]\n"
    "v_cvt_pk_bf16_f32 v20, v160, v161\n"
    "v_cvt_pk_bf16_f32 v21, v162, v163\n"
    "v_cvt_pk_bf16_f32 v22, v164, v165\n"
    "v_cvt_pk_bf16_f32 v23, v166, v167\n"
    "s_waitcnt lgkmcnt(0)\n"
    "v_lshlrev_b32 v168, 16, v52\n"
    "v_and_b32 v169, 0xffff0000, v52\n"
    "v_lshlrev_b32 v170, 16, v53\n"
    "v_and_b32 v171, 0xffff0000, v53\n"
    "v_lshlrev_b32 v172, 16, v54\n"
    "v_and_b32 v173, 0xffff0000, v54\n"
    "v_lshlrev_b32 v174, 16, v55\n"
    "v_and_b32 v175, 0xffff0000, v55\n"
    "v_pk_mul_f32 v[168:169], v[168:169], s[66:67]\n"
    "v_pk_mul_f32 v[170:171], v[170:171], s[66:67]\n"
    "v_pk_mul_f32 v[172:173], v[172:173], s[66:67]\n"
    "v_pk_mul_f32 v[174:175], v[174:175], s[66:67]\n"
    "v_cvt_pk_bf16_f32 v52, v168, v169\n"
    "v_cvt_pk_bf16_f32 v53, v170, v171\n"
    "v_cvt_pk_bf16_f32 v54, v172, v173\n"
    "v_cvt_pk_bf16_f32 v55, v174, v175\n"
    "s_add_u32 m0, s37, 0x2e80\n"
    "s_add_u32 s64, s38, 0x4000\n"
    "buffer_load_dwordx4 v255, s[48:51], s64 offen offset:384 nt lds\n"
    "s_add_u32 m0, s37, 0x3280\n"
    "s_add_u32 s64, s38, 0x5000\n"
    "buffer_load_dwordx4 v250, s[48:51], s64 offen offset:384 nt lds\n"
    "s_add_u32 m0, s37, 0x3680\n"
    "s_add_u32 s64, s38, 0x6000\n"
    "buffer_load_dwordx4 v255, s[48:51], s64 offen offset:384 nt lds\n"
    "s_add_u32 m0, s37, 0x3a80\n"
    "s_add_u32 s64, s38, 0x7000\n"
    "buffer_load_dwordx4 v250, s[48:51], s64 offen offset:384 nt lds\n"
    "s_waitcnt vmcnt(16)\n"
    "ds_read_b128 v[24:27], v192 offset:12288\n"
    "ds_read_b128 v[56:59], v192 offset:14336\n"
    "ds_read_b128 v[28:31], v193 offset:12288\n"
    "ds_read_b128 v[60:63], v193 offset:14336\n"
    "s_waitcnt lgkmcnt(3)\n"
    "v_lshlrev_b32 v160, 16, v24\n"
    "v_and_b32 v161, 0xffff0000, v24\n"
    "v_lshlrev_b32 v162, 16, v25\n"
    "v_and_b32 v163, 0xffff0000, v25\n"
    "v_lshlrev_b32 v164, 16, v26\n"
    "v_and_b32 v165, 0xffff0000, v26\n"
    "v_lshlrev_b32 v166, 16, v27\n"
    "v_and_b32 v167, 0xffff0000, v27\n"
    "v_pk_mul_f32 v[160:161], v[160:161], s[66:67]\n"
    "v_pk_mul_f32 v[162:163], v[162:163], s[66:67]\n"
    "v_pk_mul_f32 v[164:165], v[164:165], s[66:67]\n"
    "v_pk_mul_f32 v[166:167], v[166:167], s[66:67]\n"
    "v_cvt_pk_bf16_f32 v24, v160, v161\n"
    "v_cvt_pk_bf16_f32 v25, v162, v163\n"
    "v_cvt_pk_bf16_f32 v26, v164, v165\n"
    "v_cvt_pk_bf16_f32 v27, v166, v167\n"
    "s_waitcnt lgkmcnt(2)\n"
    "v_lshlrev_b32 v168, 16, v56\n"
    "v_and_b32 v169, 0xffff0000, v56\n"
    "v_lshlrev_b32 v170, 16, v57\n"
    "v_and_b32 v171, 0xffff0000, v57\n"
    "v_lshlrev_b32 v172, 16, v58\n"
    "v_and_b32 v173, 0xffff0000, v58\n"
    "v_lshlrev_b32 v174, 16, v59\n"
    "v_and_b32 v175, 0xffff0000, v59\n"
    "v_pk_mul_f32 v[168:169], v[168:169], s[66:67]\n"
    "v_pk_mul_f32 v[170:171], v[170:171], s[66:67]\n"
    "v_pk_mul_f32 v[172:173], v[172:173], s[66:67]\n"
    "v_pk_mul_f32 v[174:175], v[174:175], s[66:67]\n"
    "v_cvt_pk_bf16_f32 v56, v168, v169\n"
    "v_cvt_pk_bf16_f32 v57, v170, v171\n"
    "v_cvt_pk_bf16_f32 v58, v172, v173\n"
    "v_cvt_pk_bf16_f32 v59, v174, v175\n"
    "s_waitcnt lgkmcnt(1)\n"
    "v_lshlrev_b32 v160, 16, v28\n"
    "v_and_b32 v161, 0xffff0000, v28\n"
    "v_lshlrev_b32 v162, 16, v29\n"
    "v_and_b32 v163, 0xffff0000, v29\n"
    "v_lshlrev_b32 v164, 16, v30\n"
    "v_and_b32 v165, 0xffff0000, v30\n"
    "v_lshlrev_b32 v166, 16, v31\n"
    "v_and_b32 v167, 0xffff0000, v31\n"
    "v_pk_mul_f32 v[160:161], v[160:161], s[66:67]\n"
    "v_pk_mul_f32 v[162:163], v[162:163], s[66:67]\n"
    "v_pk_mul_f32 v[164:165], v[164:165], s[66:67]\n"
    "v_pk_mul_f32 v[166:167], v[166:167], s[66:67]\n"
    "v_cvt_pk_bf16_f32 v28, v160, v161\n"
    "v_cvt_pk_bf16_f32 v29, v162, v163\n"
    "v_cvt_pk_bf16_f32 v30, v164, v165\n"
    "v_cvt_pk_bf16_f32 v31, v166, v167\n"
    "s_waitcnt lgkmcnt(0)\n"
    "v_lshlrev_b32 v168, 16, v60\n"
    "v_and_b32 v169, 0xffff0000, v60\n"
    "v_lshlrev_b32 v170, 16, v61\n"
    "v_and_b32 v171, 0xffff0000, v61\n"
    "v_lshlrev_b32 v172, 16, v62\n"
    "v_and_b32 v173, 0xffff0000, v62\n"
    "v_lshlrev_b32 v174, 16, v63\n"
    "v_and_b32 v175, 0xffff0000, v63\n"
    "v_pk_mul_f32 v[168:169], v[168:169], s[66:67]\n"
    "v_pk_mul_f32 v[170:171], v[170:171], s[66:67]\n"
    "v_pk_mul_f32 v[172:173], v[172:173], s[66:67]\n"
    "v_pk_mul_f32 v[174:175], v[174:175], s[66:67]\n"
    "v_cvt_pk_bf16_f32 v60, v168, v169\n"
    "v_cvt_pk_bf16_f32 v61, v170, v171\n"
    "v_cvt_pk_bf16_f32 v62, v172, v173\n"
    "v_cvt_pk_bf16_f32 v63, v174, v175\n"
    "v_mov_b32 v240, v196\n"
    "v_mov_b32 v241, v197\n"
    "v_add_u32 v192, s37, v196\n"
    "v_add_u32 v193, s37, v197\n"
    "s_waitcnt vmcnt(16)\n"
    "v_mbcnt_lo_u32_b32 v254, -1, 0\n"
    "v_mbcnt_hi_u32_b32 v254, -1, v254\n"
    "v_and_b32 v255, 15, v254\n"
    "v_lshrrev_b32 v250, 4, v254\n"
    "s_lshl_b32 s65, %[w], 13\n"
    "s_add_u32 s65, s65, 0x20000\n"
    "v_lshrrev_b32 v251, 1, v250\n"
    "v_xor_b32 v251, v255, v251\n"
    "v_and_b32 v251, 7, v251\n"
    "v_lshlrev_b32 v251, 4, v251\n"
    "v_lshl_add_u32 v251, v255, 7, v251\n"
    "v_and_b32 v250, 1, v250\n"
    "v_lshl_add_u32 v251, v250, 3, v251\n"
    "v_add_u32 v251, s65, v251\n"
    "v_lshrrev_b32 v250, 3, v254\n"
    "v_xor_b32 v255, v254, v250\n"
    "v_and_b32 v255, 7, v255\n"
    "v_lshlrev_b32 v255, 4, v255\n"
    "v_lshl_add_u32 v255, v250, 7, v255\n"
    "v_add_u32 v252, s65, v255\n"
    "v_and_b32 v255, 7, v254\n"
    "v_lshlrev_b32 v255, 4, v255\n"
    "v_lshl_add_u32 v253, v250, 9, v255\n"
    "s_lshl_b32 s67, %[w], 15\n"
    "s_add_u32 s48, s67, 0x1000\n"
    "s_add_u32 s49, s67, 0x2000\n"
    "s_add_u32 s50, s67, 0x3000\n"
    "s_add_u32 s51, s67, 0x4000\n"
    "s_add_u32 s36, s67, 0x5000\n"
    "s_add_u32 s37, s67, 0x6000\n"
    "s_add_u32 s38, s67, 0x7000\n"
    "s_barrier\n"
    "ds_read_b128 v[208:211], v240 offset:0\n"
    "ds_read_b128 v[212:215], v240 offset:2048\n"
    "ds_read_b128 v[216:219], v241 offset:0\n"
    "ds_read_b128 v[220:223], v241 offset:2048\n"
    "ds_read_b128 v[224:227], v240 offset:4096\n"
    "ds_read_b128 v[228:231], v240 offset:6144\n"
    "ds_read_b128 v[232:235], v241 offset:4096\n"
    "ds_read_b128 v[236:239], v241 offset:6144\n"
    "s_waitcnt lgkmcnt(4)\n"
    "v_mfma_f32_16x16x32_bf16 v[128:131], v[208:211], v[0:3], 0\n"
    "v_mfma_f32_16x16x32_bf16 v[136:139], v[208:211], v[32:35], 0\n"
    "v_mfma_f32_16x16x32_bf16 v[132:135], v[212:215], v[0:3], 0\n"
    "v_mfma_f32_16x16x32_bf16 v[140:143], v[212:215], v[32:35], 0\n"
    "v_mfma_f32_16x16x32_bf16 v[128:131], v[216:219], v[4:7], v[128:131]\n"
    "v_mfma_f32_16x16x32_bf16 v[136:139], v[216:219], v[36:39], v[136:139]\n"
    "v_mfma_f32_16x16x32_bf16 v[132:135], v[220:223], v[4:7], v[132:135]\n"
    "v_mfma_f32_16x16x32_bf16 v[140:143], v[220:223], v[36:39], v[140:143]\n"
    "ds_read_b128 v[208:211], v240 offset:8192\n"
    "ds_read_b128 v[212:215], v240 offset:10240\n"
    "ds_read_b128 v[216:219], v241 offset:8192\n"
    "ds_read_b128 v[220:223], v241 offset:10240\n"
    "s_waitcnt lgkmcnt(4)\n"
    "v_mfma_f32_16x16x32_bf16 v[128:131], v[224:227], v[8:11], v[128:131]\n"
    "v_mfma_f32_16x16x32_bf16 v[136:139], v[224:227], v[40:43], v[136:139]\n"
    "v_mfma_f32_16x16x32_bf16 v[132:135], v[228:231], v[8:11], v[132:135]\n"
    "v_mfma_f32_16x16x32_bf16 v[140:143], v[228:231], v[40:43], v[140:143]\n"
    "v_mfma_f32_16x16x32_bf16 v[128:131], v[232:235], v[12:15], v[128:131]\n"
    "v_mfma_f32_16x16x32_bf16 v[136:139], v[232:235], v[44:47], v[136:139]\n"
    "v_mfma_f32_16x16x32_bf16 v[132:135], v[236:239], v[12:15], v[132:135]\n"
    "v_mfma_f32_16x16x32_bf16 v[140:143], v[236:239], v[44:47], v[140:143]\n"
    "ds_read_b128 v[224:227], v240 offset:12288\n"
    "ds_read_b128 v[228:231], v240 offset:14336\n"
    "ds_read_b128 v[232:235], v241 offset:12288\n"
    "ds_read_b128 v[236:239], v241 offset:14336\n"
    "s_waitcnt lgkmcnt(4)\n"
    "v_mfma_f32_16x16x32_bf16 v[128:131], v[208:211], v[16:19], v[128:131]\n"
    "v_mfma_f32_16x16x32_bf16 v[136:139], v[208:211], v[48:51], v[136:139]\n"
    "v_mfma_f32_16x16x32_bf16 v[132:135], v[212:215], v[16:19], v[132:135]\n"
    "v_mfma_f32_16x16x32_bf16 v[140:143], v[212:215], v[48:51], v[140:143]\n"
    "v_mfma_f32_16x16x32_bf16 v[128:131], v[216:219], v[20:23], v[128:131]\n"
    "v_mfma_f32_16x16x32_bf16 v[136:139], v[216:219], v[52:55], v[136:139]\n"
    "v_mfma_f32_16x16x32_bf16 v[132:135], v[220:223], v[20:23], v[132:135]\n"
    "v_mfma_f32_16x16x32_bf16 v[140:143], v[220:223], v[52:55], v[140:143]\n"
    "s_waitcnt lgkmcnt(0)\n"
    "v_mfma_f32_16x16x32_bf16 v[128:131], v[224:227], v[24:27], v[128:131]\n"
    "v_mfma_f32_16x16x32_bf16 v[136:139], v[224:227], v[56:59], v[136:139]\n"
    "v_mfma_f32_16x16x32_bf16 v[132:135], v[228:231], v[24:27], v[132:135]\n"
    "v_mfma_f32_16x16x32_bf16 v[140:143], v[228:231], v[56:59], v[140:143]\n"
    "v_mfma_f32_16x16x32_bf16 v[128:131], v[232:235], v[28:31], v[128:131]\n"
    "v_mfma_f32_16x16x32_bf16 v[136:139], v[232:235], v[60:63], v[136:139]\n"
    "v_mfma_f32_16x16x32_bf16 v[132:135], v[236:239], v[28:31], v[132:135]\n"
    "v_mfma_f32_16x16x32_bf16 v[140:143], v[236:239], v[60:63], v[140:143]\n"
    "s_nop 4\n"
    "v_max3_f32 v176, v128, v129, v130\n"
    "v_max3_f32 v184, v136, v137, v138\n"
    "v_max3_f32 v176, v176, v131, v132\n"
    "v_max3_f32 v184, v184, v139, v140\n"
    "v_max3_f32 v176, v176, v133, v134\n"
    "v_max3_f32 v184, v184, v141, v142\n"
    "v_max_f32 v176, v176, v135\n"
    "v_max_f32 v184, v184, v143\n"
    "v_mov_b32 v177, v176\n"
    "v_mov_b32 v185, v184\n"
    "s_nop 0\n"
    "v_permlane32_swap_b32 v176, v177\n"
    "v_permlane32_swap_b32 v184, v185\n"
    "v_max_f32 v176, v176, v177\n"
    "v_max_f32 v184, v184, v185\n"
    "v_mov_b32 v177, v176\n"
    "v_mov_b32 v185, v184\n"
    "s_nop 0\n"
    "v_permlane16_swap_b32 v176, v177\n"
    "v_permlane16_swap_b32 v184, v185\n"
    "v_max_f32 v176, v176, v177\n"
    "v_max_f32 v184, v184, v185\n"
    "v_sub_f32 v128, v128, v176\n"
    "v_sub_f32 v136, v136, v184\n"
    "v_sub_f32 v129, v129, v176\n"
    "v_sub_f32 v137, v137, v184\n"
    "v_sub_f32 v130, v130, v176\n"
    "v_sub_f32 v138, v138, v184\n"
    "v_sub_f32 v131, v131, v176\n"
    "v_sub_f32 v139, v139, v184\n"
    "v_sub_f32 v132, v132, v176\n"
    "v_sub_f32 v140, v140, v184\n"
    "v_sub_f32 v133, v133, v176\n"
    "v_sub_f32 v141, v141, v184\n"
    "v_sub_f32 v134, v134, v176\n"
    "v_sub_f32 v142, v142, v184\n"
    "v_sub_f32 v135, v135, v176\n"
    "v_sub_f32 v143, v143, v184\n"
    "v_exp_f32 v128, v128\n"
    "s_nop 0\n"
    "v_add_f32 v246, v128, v246\n"
    "v_exp_f32 v129, v129\n"
    "s_nop 0\n"
    "v_add_f32 v246, v129, v246\n"
    "v_cvt_pk_bf16_f32 v128, v128, v129\n"
    "v_exp_f32 v130, v130\n"
    "s_nop 0\n"
    "v_add_f32 v246, v130, v246\n"
    "v_exp_f32 v131, v131\n"
    "s_nop 0\n"
    "v_add_f32 v246, v131, v246\n"
    "v_cvt_pk_bf16_f32 v129, v130, v131\n"
    "v_exp_f32 v132, v132\n"
    "v_exp_f32 v133, v133\n"
    "v_add_f32 v246, v132, v246\n"
    "v_add_f32 v246, v133, v246\n"
    "v_cvt_pk_bf16_f32 v130, v132, v133\n"
    "v_exp_f32 v134, v134\n"
    "s_nop 0\n"
    "v_add_f32 v246, v134, v246\n"
    "v_exp_f32 v135, v135\n"
    "s_nop 0\n"
    "v_add_f32 v246, v135, v246\n"
    "v_cvt_pk_bf16_f32 v131, v134, v135\n"
    "v_exp_f32 v136, v136\n"
    "s_nop 0\n"
    "v_add_f32 v247, v136, v247\n"
    "v_exp_f32 v137, v137\n"
    "v_exp_f32 v138, v138\n"
    "v_add_f32 v247, v137, v247\n"
    "v_cvt_pk_bf16_f32 v136, v136, v137\n"
    "v_exp_f32 v139, v139\n"
    "v_add_f32 v247, v138, v247\n"
    "v_add_f32 v247, v139, v247\n"
    "v_cvt_pk_bf16_f32 v137, v138, v139\n"
    "v_exp_f32 v140, v140\n"
    "s_nop 0\n"
    "v_add_f32 v247, v140, v247\n"
    "ds_read_b128 v[208:211], v240 offset:0\n"
    "ds_read_b128 v[212:215], v240 offset:2048\n"
    "ds_read_b128 v[216:219], v241 offset:0\n"
    "ds_read_b128 v[220:223], v241 offset:2048\n"
    "s_waitcnt vmcnt(12)\n"
    "ds_read_b128 v[64:67], v192 offset:0\n"
    "ds_read_b128 v[96:99], v192 offset:2048\n"
    "ds_read_b128 v[68:71], v193 offset:0\n"
    "ds_read_b128 v[100:103], v193 offset:2048\n"
    "s_mov_b32 s56, 0x4000\n"
    "s_add_u32 m0, s58, 0x4000\n"
    "s_nop 0\n"
    "buffer_load_dwordx4 v244, s[40:43], s56 offen offset:0 nt lds\n"
    "s_add_u32 m0, s58, 0x4f80\n"
    "s_nop 0\n"
    "buffer_load_dwordx4 v244, s[40:43], s56 offen offset:128 nt lds\n"
    "s_add_u32 m0, s58, 0x5f00\n"
    "s_nop 0\n"
    "buffer_load_dwordx4 v244, s[40:43], s56 offen offset:256 nt lds\n"
    "s_add_u32 m0, s58, 0x6e80\n"
    "s_nop 0\n"
    "buffer_load_dwordx4 v244, s[40:43], s56 offen offset:384 nt lds\n"
    "s_waitcnt lgkmcnt(3)\n"
    "v_lshlrev_b32 v160, 16, v64\n"
    "v_and_b32 v161, 0xffff0000, v64\n"
    "v_lshlrev_b32 v162, 16, v65\n"
    "v_and_b32 v163, 0xffff0000, v65\n"
    "v_lshlrev_b32 v164, 16, v66\n"
    "v_and_b32 v165, 0xffff0000, v66\n"
    "v_lshlrev_b32 v166, 16, v67\n"
    "v_and_b32 v167, 0xffff0000, v67\n"
    "v_mul_f32 v160, s66, v160\n"
    "v_mul_f32 v161, s66, v161\n"
    "v_mul_f32 v162, s66, v162\n"
    "v_mul_f32 v163, s66, v163\n"
    "v_mul_f32 v164, s66, v164\n"
    "v_mul_f32 v165, s66, v165\n"
    "v_mul_f32 v166, s66, v166\n"
    "v_mul_f32 v167, s66, v167\n"
    "v_cvt_pk_bf16_f32 v64, v160, v161\n"
    "v_cvt_pk_bf16_f32 v65, v162, v163\n"
    "v_cvt_pk_bf16_f32 v66, v164, v165\n"
    "v_cvt_pk_bf16_f32 v67, v166, v167\n"
    "s_waitcnt lgkmcnt(2)\n"
    "v_lshlrev_b32 v168, 16, v96\n"
    "v_and_b32 v169, 0xffff0000, v96\n"
    "v_lshlrev_b32 v170, 16, v97\n"
    "v_and_b32 v171, 0xffff0000, v97\n"
    "v_lshlrev_b32 v172, 16, v98\n"
    "v_and_b32 v173, 0xffff0000, v98\n"
    "v_lshlrev_b32 v174, 16, v99\n"
    "v_and_b32 v175, 0xffff0000, v99\n"
    "v_mul_f32 v168, s66, v168\n"
    "v_mul_f32 v169, s66, v169\n"
    "v_mul_f32 v170, s66, v170\n"
    "v_mul_f32 v171, s66, v171\n"
    "v_mul_f32 v172, s66, v172\n"
    "v_mul_f32 v173, s66, v173\n"
    "v_mul_f32 v174, s66, v174\n"
    "v_mul_f32 v175, s66, v175\n"
    "v_cvt_pk_bf16_f32 v96, v168, v169\n"
    "v_cvt_pk_bf16_f32 v97, v170, v171\n"
    "v_cvt_pk_bf16_f32 v98, v172, v173\n"
    "v_cvt_pk_bf16_f32 v99, v174, v175\n"
    "s_waitcnt lgkmcnt(1)\n"
    "v_lshlrev_b32 v160, 16, v68\n"
    "v_and_b32 v161, 0xffff0000, v68\n"
    "v_lshlrev_b32 v162, 16, v69\n"
    "v_and_b32 v163, 0xffff0000, v69\n"
    "v_lshlrev_b32 v164, 16, v70\n"
    "v_and_b32 v165, 0xffff0000, v70\n"
    "v_lshlrev_b32 v166, 16, v71\n"
    "v_and_b32 v167, 0xffff0000, v71\n"
    "v_mul_f32 v160, s66, v160\n"
    "v_mul_f32 v161, s66, v161\n"
    "v_mul_f32 v162, s66, v162\n"
    "v_mul_f32 v163, s66, v163\n"
    "v_mul_f32 v164, s66, v164\n"
    "v_mul_f32 v165, s66, v165\n"
    "v_mul_f32 v166, s66, v166\n"
    "v_mul_f32 v167, s66, v167\n"
    "v_cvt_pk_bf16_f32 v68, v160, v161\n"
    "v_cvt_pk_bf16_f32 v69, v162, v163\n"
    "v_cvt_pk_bf16_f32 v70, v164, v165\n"
    "v_cvt_pk_bf16_f32 v71, v166, v167\n"
    "s_waitcnt lgkmcnt(0)\n"
    "v_lshlrev_b32 v168, 16, v100\n"
    "v_and_b32 v169, 0xffff0000, v100\n"
    "v_lshlrev_b32 v170, 16, v101\n"
    "v_and_b32 v171, 0xffff0000, v101\n"
    "v_lshlrev_b32 v172, 16, v102\n"
    "v_and_b32 v173, 0xffff0000, v102\n"
    "v_lshlrev_b32 v174, 16, v103\n"
    "v_and_b32 v175, 0xffff0000, v103\n"
    "v_mul_f32 v168, s66, v168\n"
    "v_mul_f32 v169, s66, v169\n"
    "v_mul_f32 v170, s66, v170\n"
    "v_mul_f32 v171, s66, v171\n"
    "v_mul_f32 v172, s66, v172\n"
    "v_mul_f32 v173, s66, v173\n"
    "v_mul_f32 v174, s66, v174\n"
    "v_mul_f32 v175, s66, v175\n"
    "v_cvt_pk_bf16_f32 v100, v168, v169\n"
    "v_cvt_pk_bf16_f32 v101, v170, v171\n"
    "v_cvt_pk_bf16_f32 v102, v172, v173\n"
    "v_cvt_pk_bf16_f32 v103, v174, v175\n"
    "ds_read_b128 v[224:227], v240 offset:4096\n"
    "ds_read_b128 v[228:231], v240 offset:6144\n"
    "ds_read_b128 v[232:235], v241 offset:4096\n"
    "ds_read_b128 v[236:239], v241 offset:6144\n"
    "s_waitcnt vmcnt(12)\n"
    "ds_read_b128 v[72:75], v192 offset:4096\n"
    "ds_read_b128 v[104:107], v192 offset:6144\n"
    "ds_read_b128 v[76:79], v193 offset:4096\n"
    "ds_read_b128 v[108:111], v193 offset:6144\n"
    "s_mov_b32 s56, 0x0\n"
    "s_add_u32 m0, s59, 0x0\n"
    "s_nop 0\n"
    "buffer_load_dwordx4 v245, s[44:47], s56 offen offset:0 nt lds\n"
    "s_add_u32 m0, s59, 0x380\n"
    "s_nop 0\n"
    "buffer_load_dwordx4 v245, s[44:47], s56 offen offset:128 nt lds\n"
    "s_add_u32 m0, s59, 0x700\n"
    "s_nop 0\n"
    "buffer_load_dwordx4 v245, s[44:47], s56 offen offset:256 nt lds\n"
    "s_add_u32 m0, s59, 0xa80\n"
    "s_nop 0\n"
    "buffer_load_dwordx4 v245, s[44:47], s56 offen offset:384 nt lds\n"
    "s_waitcnt lgkmcnt(3)\n"
    "v_lshlrev_b32 v160, 16, v72\n"
    "v_and_b32 v161, 0xffff0000, v72\n"
    "v_mfma_f32_16x16x32_bf16 v[144:147], v[208:211], v[64:67], 0\n"
    "v_lshlrev_b32 v162, 16, v73\n"
    "v_and_b32 v163, 0xffff0000, v73\n"
    "v_lshlrev_b32 v164, 16, v74\n"
    "v_and_b32 v165, 0xffff0000, v74\n"
    "v_lshlrev_b32 v166, 16, v75\n"
    "v_and_b32 v167, 0xffff0000, v75\n"
    "v_mul_f32 v160, s66, v160\n"
    "v_mul_f32 v161, s66, v161\n"
    "v_mul_f32 v162, s66, v162\n"
    "v_mul_f32 v163, s66, v163\n"
    "v_mfma_f32_16x16x32_bf16 v[152:155], v[208:211], v[96:99], 0\n"
    "v_mul_f32 v164, s66, v164\n"
    "v_mul_f32 v165, s66, v165\n"
    "v_mul_f32 v166, s66, v166\n"
    "v_mul_f32 v167, s66, v167\n"
    "v_cvt_pk_bf16_f32 v72, v160, v161\n"
    "v_cvt_pk_bf16_f32 v73, v162, v163\n"
    "v_cvt_pk_bf16_f32 v74, v164, v165\n"
    "v_cvt_pk_bf16_f32 v75, v166, v167\n"
    "s_waitcnt lgkmcnt(2)\n"
    "v_lshlrev_b32 v168, 16, v104\n"
    "v_mfma_f32_16x16x32_bf16 v[148:151], v[212:215], v[64:67], 0\n"
    "v_and_b32 v169, 0xffff0000, v104\n"
    "v_lshlrev_b32 v170, 16, v105\n"
    "v_and_b32 v171, 0xffff0000, v105\n"
    "v_lshlrev_b32 v172, 16, v106\n"
    "v_and_b32 v173, 0xffff0000, v106\n"
    "v_lshlrev_b32 v174, 16, v107\n"
    "v_and_b32 v175, 0xffff0000, v107\n"
    "v_mul_f32 v168, s66, v168\n"
    "v_mul_f32 v169, s66, v169\n"
    "v_mul_f32 v170, s66, v170\n"
    "v_mfma_f32_16x16x32_bf16 v[156:159], v[212:215], v[96:99], 0\n"
    "v_mul_f32 v171, s66, v171\n"
    "v_mul_f32 v172, s66, v172\n"
    "v_mul_f32 v173, s66, v173\n"
    "v_mul_f32 v174, s66, v174\n"
    "v_mul_f32 v175, s66, v175\n"
    "v_cvt_pk_bf16_f32 v104, v168, v169\n"
    "v_cvt_pk_bf16_f32 v105, v170, v171\n"
    "v_cvt_pk_bf16_f32 v106, v172, v173\n"
    "v_cvt_pk_bf16_f32 v107, v174, v175\n"
    "s_waitcnt lgkmcnt(1)\n"
    "v_mfma_f32_16x16x32_bf16 v[144:147], v[216:219], v[68:71], v[144:147]\n"
    "v_lshlrev_b32 v160, 16, v76\n"
    "v_and_b32 v161, 0xffff0000, v76\n"
    "v_lshlrev_b32 v162, 16, v77\n"
    "v_and_b32 v163, 0xffff0000, v77\n"
    "v_lshlrev_b32 v164, 16, v78\n"
    "v_and_b32 v165, 0xffff0000, v78\n"
    "v_lshlrev_b32 v166, 16, v79\n"
    "v_and_b32 v167, 0xffff0000, v79\n"
    "v_mul_f32 v160, s66, v160\n"
    "v_mul_f32 v161, s66, v161\n"
    "v_mfma_f32_16x16x32_bf16 v[152:155], v[216:219], v[100:103], v[152:155]\n"
    "v_mul_f32 v162, s66, v162\n"
    "v_mul_f32 v163, s66, v163\n"
    "v_mul_f32 v164, s66, v164\n"
    "v_mul_f32 v165, s66, v165\n"
    "v_mul_f32 v166, s66, v166\n"
    "v_mul_f32 v167, s66, v167\n"
    "v_cvt_pk_bf16_f32 v76, v160, v161\n"
    "v_cvt_pk_bf16_f32 v77, v162, v163\n"
    "v_cvt_pk_bf16_f32 v78, v164, v165\n"
    "v_cvt_pk_bf16_f32 v79, v166, v167\n"
    "v_mfma_f32_16x16x32_bf16 v[148:151], v[220:223], v[68:71], v[148:151]\n"
    "s_waitcnt lgkmcnt(0)\n"
    "v_lshlrev_b32 v168, 16, v108\n"
    "v_and_b32 v169, 0xffff0000, v108\n"
    "v_lshlrev_b32 v170, 16, v109\n"
    "v_and_b32 v171, 0xffff0000, v109\n"
    "v_lshlrev_b32 v172, 16, v110\n"
    "v_and_b32 v173, 0xffff0000, v110\n"
    "v_lshlrev_b32 v174, 16, v111\n"
    "v_and_b32 v175, 0xffff0000, v111\n"
    "v_mul_f32 v168, s66, v168\n"
    "v_mfma_f32_16x16x32_bf16 v[156:159], v[220:223], v[100:103], v[156:159]\n"
    "v_mul_f32 v169, s66, v169\n"
    "v_mul_f32 v170, s66, v170\n"
    "v_mul_f32 v171, s66, v171\n"
    "v_mul_f32 v172, s66, v172\n"
    "v_mul_f32 v173, s66, v173\n"
    "v_mul_f32 v174, s66, v174\n"
    "v_mul_f32 v175, s66, v175\n"
    "v_cvt_pk_bf16_f32 v108, v168, v169\n"
    "v_cvt_pk_bf16_f32 v109, v170, v171\n"
    "v_cvt_pk_bf16_f32 v110, v172, v173\n"
    "v_cvt_pk_bf16_f32 v111, v174, v175\n"
    "ds_read_b128 v[208:211], v240 offset:8192\n"
    "ds_read_b128 v[212:215], v240 offset:10240\n"
    "ds_read_b128 v[216:219], v241 offset:8192\n"
    "ds_read_b128 v[220:223], v241 offset:10240\n"
    "s_waitcnt vmcnt(12)\n"
    "ds_read_b128 v[80:83], v192 offset:8192\n"
    "ds_read_b128 v[112:115], v192 offset:10240\n"
    "ds_read_b128 v[84:87], v193 offset:8192\n"
    "ds_read_b128 v[116:119], v193 offset:10240\n"
    "s_mov_b32 s56, 0x8000\n"
    "s_add_u32 m0, s58, 0x8000\n"
    "s_nop 0\n"
    "buffer_load_dwordx4 v244, s[40:43], s56 offen offset:0 nt lds\n"
    "s_add_u32 m0, s58, 0x8f80\n"
    "s_nop 0\n"
    "buffer_load_dwordx4 v244, s[40:43], s56 offen offset:128 nt lds\n"
    "s_add_u32 m0, s58, 0x9f00\n"
    "s_nop 0\n"
    "buffer_load_dwordx4 v244, s[40:43], s56 offen offset:256 nt lds\n"
    "s_add_u32 m0, s58, 0xae80\n"
    "s_nop 0\n"
    "buffer_load_dwordx4 v244, s[40:43], s56 offen offset:384 nt lds\n"
    "s_waitcnt lgkmcnt(3)\n"
    "v_lshlrev_b32 v160, 16, v80\n"
    "v_and_b32 v161, 0xffff0000, v80\n"
    "v_mfma_f32_16x16x32_bf16 v[144:147], v[224:227], v[72:75], v[144:147]\n"
    "v_lshlrev_b32 v162, 16, v81\n"
    "v_and_b32 v163, 0xffff0000, v81\n"
    "v_lshlrev_b32 v164, 16, v82\n"
    "v_and_b32 v165, 0xffff0000, v82\n"
    "v_lshlrev_b32 v166, 16, v83\n"
    "v_and_b32 v167, 0xffff0000, v83\n"
    "v_mul_f32 v160, s66, v160\n"
    "v_mul_f32 v161, s66, v161\n"
    "v_mul_f32 v162, s66, v162\n"
    "v_mul_f32 v163, s66, v163\n"
    "v_mfma_f32_16x16x32_bf16 v[152:155], v[224:227], v[104:107], v[152:155]\n"
    "v_mul_f32 v164, s66, v164\n"
    "v_mul_f32 v165, s66, v165\n"
    "v_mul_f32 v166, s66, v166\n"
    "v_mul_f32 v167, s66, v167\n"
    "v_cvt_pk_bf16_f32 v80, v160, v161\n"
    "v_cvt_pk_bf16_f32 v81, v162, v163\n"
    "v_cvt_pk_bf16_f32 v82, v164, v165\n"
    "v_cvt_pk_bf16_f32 v83, v166, v167\n"
    "s_waitcnt lgkmcnt(2)\n"
    "v_lshlrev_b32 v168, 16, v112\n"
    "v_mfma_f32_16x16x32_bf16 v[148:151], v[228:231], v[72:75], v[148:151]\n"
    "v_and_b32 v169, 0xffff0000, v112\n"
    "v_lshlrev_b32 v170, 16, v113\n"
    "v_and_b32 v171, 0xffff0000, v113\n"
    "v_lshlrev_b32 v172, 16, v114\n"
    "v_and_b32 v173, 0xffff0000, v114\n"
    "v_lshlrev_b32 v174, 16, v115\n"
    "v_and_b32 v175, 0xffff0000, v115\n"
    "v_mul_f32 v168, s66, v168\n"
    "v_mul_f32 v169, s66, v169\n"
    "v_mul_f32 v170, s66, v170\n"
    "v_mfma_f32_16x16x32_bf16 v[156:159], v[228:231], v[104:107], v[156:159]\n"
    "v_mul_f32 v171, s66, v171\n"
    "v_mul_f32 v172, s66, v172\n"
    "v_mul_f32 v173, s66, v173\n"
    "v_mul_f32 v174, s66, v174\n"
    "v_mul_f32 v175, s66, v175\n"
    "v_cvt_pk_bf16_f32 v112, v168, v169\n"
    "v_cvt_pk_bf16_f32 v113, v170, v171\n"
    "v_cvt_pk_bf16_f32 v114, v172, v173\n"
    "v_cvt_pk_bf16_f32 v115, v174, v175\n"
    "s_waitcnt lgkmcnt(1)\n"
    "v_mfma_f32_16x16x32_bf16 v[144:147], v[232:235], v[76:79], v[144:147]\n"
    "v_lshlrev_b32 v160, 16, v84\n"
    "v_and_b32 v161, 0xffff0000, v84\n"
    "v_lshlrev_b32 v162, 16, v85\n"
    "v_and_b32 v163, 0xffff0000, v85\n"
    "v_lshlrev_b32 v164, 16, v86\n"
    "v_and_b32 v165, 0xffff0000, v86\n"
    "v_lshlrev_b32 v166, 16, v87\n"
    "v_and_b32 v167, 0xffff0000, v87\n"
    "v_mul_f32 v160, s66, v160\n"
    "v_mul_f32 v161, s66, v161\n"
    "v_mfma_f32_16x16x32_bf16 v[152:155], v[232:235], v[108:111], v[152:155]\n"
    "v_mul_f32 v162, s66, v162\n"
    "v_mul_f32 v163, s66, v163\n"
    "v_mul_f32 v164, s66, v164\n"
    "v_mul_f32 v165, s66, v165\n"
    "v_mul_f32 v166, s66, v166\n"
    "v_mul_f32 v167, s66, v167\n"
    "v_cvt_pk_bf16_f32 v84, v160, v161\n"
    "v_cvt_pk_bf16_f32 v85, v162, v163\n"
    "v_cvt_pk_bf16_f32 v86, v164, v165\n"
    "v_cvt_pk_bf16_f32 v87, v166, v167\n"
    "v_mfma_f32_16x16x32_bf16 v[148:151], v[236:239], v[76:79], v[148:151]\n"
    "s_waitcnt lgkmcnt(0)\n"
    "v_lshlrev_b32 v168, 16, v116\n"
    "v_and_b32 v169, 0xffff0000, v116\n"
    "v_lshlrev_b32 v170, 16, v117\n"
    "v_and_b32 v171, 0xffff0000, v117\n"
    "v_lshlrev_b32 v172, 16, v118\n"
    "v_and_b32 v173, 0xffff0000, v118\n"
    "v_lshlrev_b32 v174, 16, v119\n"
    "v_and_b32 v175, 0xffff0000, v119\n"
    "v_mul_f32 v168, s66, v168\n"
    "v_mfma_f32_16x16x32_bf16 v[156:159], v[236:239], v[108:111], v[156:159]\n"
    "v_mul_f32 v169, s66, v169\n"
    "v_mul_f32 v170, s66, v170\n"
    "v_mul_f32 v171, s66, v171\n"
    "v_mul_f32 v172, s66, v172\n"
    "v_mul_f32 v173, s66, v173\n"
    "v_mul_f32 v174, s66, v174\n"
    "v_mul_f32 v175, s66, v175\n"
    "v_cvt_pk_bf16_f32 v116, v168, v169\n"
    "v_cvt_pk_bf16_f32 v117, v170, v171\n"
    "v_cvt_pk_bf16_f32 v118, v172, v173\n"
    "v_cvt_pk_bf16_f32 v119, v174, v175\n"
    "ds_read_b128 v[224:227], v240 offset:12288\n"
    "ds_read_b128 v[228:231], v240 offset:14336\n"
    "ds_read_b128 v[232:235], v241 offset:12288\n"
    "ds_read_b128 v[236:239], v241 offset:14336\n"
    "s_waitcnt vmcnt(12)\n"
    "ds_read_b128 v[88:91], v192 offset:12288\n"
    "ds_read_b128 v[120:123], v192 offset:14336\n"
    "ds_read_b128 v[92:95], v193 offset:12288\n"
    "ds_read_b128 v[124:127], v193 offset:14336\n"
    "s_waitcnt lgkmcnt(3)\n"
    "v_lshlrev_b32 v160, 16, v88\n"
    "v_and_b32 v161, 0xffff0000, v88\n"
    "v_mfma_f32_16x16x32_bf16 v[144:147], v[208:211], v[80:83], v[144:147]\n"
    "v_lshlrev_b32 v162, 16, v89\n"
    "v_and_b32 v163, 0xffff0000, v89\n"
    "v_lshlrev_b32 v164, 16, v90\n"
    "v_and_b32 v165, 0xffff0000, v90\n"
    "v_lshlrev_b32 v166, 16, v91\n"
    "v_and_b32 v167, 0xffff0000, v91\n"
    "v_mul_f32 v160, s66, v160\n"
    "v_mul_f32 v161, s66, v161\n"
    "v_mul_f32 v162, s66, v162\n"
    "v_mul_f32 v163, s66, v163\n"
    "v_mfma_f32_16x16x32_bf16 v[152:155], v[208:211], v[112:115], v[152:155]\n"
    "v_mul_f32 v164, s66, v164\n"
    "v_mul_f32 v165, s66, v165\n"
    "v_mul_f32 v166, s66, v166\n"
    "v_mul_f32 v167, s66, v167\n"
    "v_cvt_pk_bf16_f32 v88, v160, v161\n"
    "v_cvt_pk_bf16_f32 v89, v162, v163\n"
    "v_cvt_pk_bf16_f32 v90, v164, v165\n"
    "v_cvt_pk_bf16_f32 v91, v166, v167\n"
    "s_waitcnt lgkmcnt(2)\n"
    "v_lshlrev_b32 v168, 16, v120\n"
    "v_mfma_f32_16x16x32_bf16 v[148:151], v[212:215], v[80:83], v[148:151]\n"
    "v_and_b32 v169, 0xffff0000, v120\n"
    "v_lshlrev_b32 v170, 16, v121\n"
    "v_and_b32 v171, 0xffff0000, v121\n"
    "v_lshlrev_b32 v172, 16, v122\n"
    "v_and_b32 v173, 0xffff0000, v122\n"
    "v_lshlrev_b32 v174, 16, v123\n"
    "v_and_b32 v175, 0xffff0000, v123\n"
    "v_mul_f32 v168, s66, v168\n"
    "v_mul_f32 v169, s66, v169\n"
    "v_mul_f32 v170, s66, v170\n"
    "v_mfma_f32_16x16x32_bf16 v[156:159], v[212:215], v[112:115], v[156:159]\n"
    "v_mul_f32 v171, s66, v171\n"
    "v_mul_f32 v172, s66, v172\n"
    "v_mul_f32 v173, s66, v173\n"
    "v_mul_f32 v174, s66, v174\n"
    "v_mul_f32 v175, s66, v175\n"
    "v_cvt_pk_bf16_f32 v120, v168, v169\n"
    "v_cvt_pk_bf16_f32 v121, v170, v171\n"
    "v_cvt_pk_bf16_f32 v122, v172, v173\n"
    "v_cvt_pk_bf16_f32 v123, v174, v175\n"
    "s_waitcnt lgkmcnt(1)\n"
    "v_mfma_f32_16x16x32_bf16 v[144:147], v[216:219], v[84:87], v[144:147]\n"
    "v_lshlrev_b32 v160, 16, v92\n"
    "v_and_b32 v161, 0xffff0000, v92\n"
    "v_lshlrev_b32 v162, 16, v93\n"
    "v_and_b32 v163, 0xffff0000, v93\n"
    "v_lshlrev_b32 v164, 16, v94\n"
    "v_and_b32 v165, 0xffff0000, v94\n"
    "v_lshlrev_b32 v166, 16, v95\n"
    "v_and_b32 v167, 0xffff0000, v95\n"
    "v_mul_f32 v160, s66, v160\n"
    "v_mul_f32 v161, s66, v161\n"
    "v_mfma_f32_16x16x32_bf16 v[152:155], v[216:219], v[116:119], v[152:155]\n"
    "v_mul_f32 v162, s66, v162\n"
    "v_mul_f32 v163, s66, v163\n"
    "v_mul_f32 v164, s66, v164\n"
    "v_mul_f32 v165, s66, v165\n"
    "v_mul_f32 v166, s66, v166\n"
    "v_mul_f32 v167, s66, v167\n"
    "v_cvt_pk_bf16_f32 v92, v160, v161\n"
    "v_cvt_pk_bf16_f32 v93, v162, v163\n"
    "v_cvt_pk_bf16_f32 v94, v164, v165\n"
    "v_cvt_pk_bf16_f32 v95, v166, v167\n"
    "v_mfma_f32_16x16x32_bf16 v[148:151], v[220:223], v[84:87], v[148:151]\n"
    "s_waitcnt lgkmcnt(0)\n"
    "v_lshlrev_b32 v168, 16, v124\n"
    "v_and_b32 v169, 0xffff0000, v124\n"
    "v_lshlrev_b32 v170, 16, v125\n"
    "v_and_b32 v171, 0xffff0000, v125\n"
    "v_lshlrev_b32 v172, 16, v126\n"
    "v_and_b32 v173, 0xffff0000, v126\n"
    "v_lshlrev_b32 v174, 16, v127\n"
    "v_and_b32 v175, 0xffff0000, v127\n"
    "v_mul_f32 v168, s66, v168\n"
    "v_mfma_f32_16x16x32_bf16 v[156:159], v[220:223], v[116:119], v[156:159]\n"
    "v_mul_f32 v169, s66, v169\n"
    "v_mul_f32 v170, s66, v170\n"
    "v_mul_f32 v171, s66, v171\n"
    "v_mul_f32 v172, s66, v172\n"
    "v_mul_f32 v173, s66, v173\n"
    "v_mul_f32 v174, s66, v174\n"
    "v_mul_f32 v175, s66, v175\n"
    "v_cvt_pk_bf16_f32 v124, v168, v169\n"
    "v_cvt_pk_bf16_f32 v125, v170, v171\n"
    "v_cvt_pk_bf16_f32 v126, v172, v173\n"
    "v_cvt_pk_bf16_f32 v127, v174, v175\n"
    "v_mfma_f32_16x16x32_bf16 v[144:147], v[224:227], v[88:91], v[144:147]\n"
    "v_mfma_f32_16x16x32_bf16 v[152:155], v[224:227], v[120:123], v[152:155]\n"
    "v_mfma_f32_16x16x32_bf16 v[148:151], v[228:231], v[88:91], v[148:151]\n"
    "v_mfma_f32_16x16x32_bf16 v[156:159], v[228:231], v[120:123], v[156:159]\n"
    "v_mfma_f32_16x16x32_bf16 v[144:147], v[232:235], v[92:95], v[144:147]\n"
    "v_mfma_f32_16x16x32_bf16 v[152:155], v[232:235], v[124:127], v[152:155]\n"
    "v_mfma_f32_16x16x32_bf16 v[148:151], v[236:239], v[92:95], v[148:151]\n"
    "v_mfma_f32_16x16x32_bf16 v[156:159], v[236:239], v[124:127], v[156:159]\n"
    "s_nop 4\n"
    "v_max3_f32 v180, v144, v145, v146\n"
    "v_max3_f32 v188, v152, v153, v154\n"
    "v_max3_f32 v180, v180, v147, v148\n"
    "v_max3_f32 v188, v188, v155, v156\n"
    "v_max3_f32 v180, v180, v149, v150\n"
    "v_max3_f32 v188, v188, v157, v158\n"
    "v_max_f32 v180, v180, v151\n"
    "v_max_f32 v188, v188, v159\n"
    "v_mov_b32 v181, v180\n"
    "v_mov_b32 v189, v188\n"
    "s_nop 0\n"
    "v_permlane32_swap_b32 v180, v181\n"
    "v_permlane32_swap_b32 v188, v189\n"
    "v_max_f32 v180, v180, v181\n"
    "v_max_f32 v188, v188, v189\n"
    "v_mov_b32 v181, v180\n"
    "v_mov_b32 v189, v188\n"
    "s_nop 0\n"
    "v_permlane16_swap_b32 v180, v181\n"
    "v_permlane16_swap_b32 v188, v189\n"
    "v_max_f32 v180, v180, v181\n"
    "v_max_f32 v188, v188, v189\n"
    "v_sub_f32 v144, v144, v180\n"
    "v_sub_f32 v152, v152, v188\n"
    "v_sub_f32 v145, v145, v180\n"
    "v_sub_f32 v153, v153, v188\n"
    "v_sub_f32 v146, v146, v180\n"
    "v_sub_f32 v154, v154, v188\n"
    "v_sub_f32 v147, v147, v180\n"
    "v_sub_f32 v155, v155, v188\n"
    "v_sub_f32 v148, v148, v180\n"
    "v_sub_f32 v156, v156, v188\n"
    "v_sub_f32 v149, v149, v180\n"
    "v_sub_f32 v157, v157, v188\n"
    "v_sub_f32 v150, v150, v180\n"
    "v_sub_f32 v158, v158, v188\n"
    "v_sub_f32 v151, v151, v180\n"
    "v_sub_f32 v159, v159, v188\n"
    "v_sub_f32 v192, 0, v176\n"
    "v_sub_f32 v193, 0, v176\n"
    "v_sub_f32 v194, 0, v176\n"
    "v_sub_f32 v195, 0, v176\n"
    "v_sub_f32 v196, 0, v184\n"
    "v_sub_f32 v197, 0, v184\n"
    "v_sub_f32 v198, 0, v184\n"
    "v_sub_f32 v199, 0, v184\n"
    "v_sub_f32 v200, 0, v180\n"
    "v_sub_f32 v201, 0, v180\n"
    "v_sub_f32 v202, 0, v180\n"
    "v_sub_f32 v203, 0, v180\n"
    "v_sub_f32 v204, 0, v188\n"
    "v_sub_f32 v205, 0, v188\n"
    "v_sub_f32 v206, 0, v188\n"
    "v_sub_f32 v207, 0, v188\n"
    "s_mov_b32 s56, 0x4000\n"
    "s_add_u32 m0, s59, 0x4000\n"
    "s_nop 0\n"
    "buffer_load_dwordx4 v245, s[44:47], s56 offen offset:0 nt lds\n"
    "s_add_u32 m0, s59, 0x4380\n"
    "s_nop 0\n"
    "buffer_load_dwordx4 v245, s[44:47], s56 offen offset:128 nt lds\n"
    "s_add_u32 m0, s59, 0x4700\n"
    "s_nop 0\n"
    "buffer_load_dwordx4 v245, s[44:47], s56 offen offset:256 nt lds\n"
    "s_add_u32 m0, s59, 0x4a80\n"
    "s_nop 0\n"
    "buffer_load_dwordx4 v245, s[44:47], s56 offen offset:384 nt lds\n"
    "s_mov_b32 s56, 0xc000\n"
    "s_add_u32 m0, s58, 0xc000\n"
    "s_nop 0\n"
    "buffer_load_dwordx4 v244, s[40:43], s56 offen offset:0 nt lds\n"
    "s_add_u32 m0, s58, 0xcf80\n"
    "s_nop 0\n"
    "buffer_load_dwordx4 v244, s[40:43], s56 offen offset:128 nt lds\n"
    "s_add_u32 m0, s58, 0xdf00\n"
    "s_nop 0\n"
    "buffer_load_dwordx4 v244, s[40:43], s56 offen offset:256 nt lds\n"
    "s_add_u32 m0, s58, 0xee80\n"
    "s_nop 0\n"
    "buffer_load_dwordx4 v244, s[40:43], s56 offen offset:384 nt lds\n"
    "v_add_u32 v244, 0x8000, v244\n"
    "s_mov_b32 s56, 0x8000\n"
    "s_waitcnt vmcnt(12)\n"
    "s_barrier\n"
    "ds_read_b128 v[208:211], v240 offset:16384\n"
    "ds_read_b128 v[212:215], v240 offset:18432\n"
    "s_add_u32 m0, s59, 0x8000\n"
    "s_nop 0\n"
    "buffer_load_dwordx4 v245, s[44:47], s56 offen offset:0 nt lds\n"
    "ds_read_b128 v[216:219], v241 offset:16384\n"
    "ds_read_b128 v[220:223], v241 offset:18432\n"
    "s_add_u32 m0, s59, 0x8380\n"
    "ds_read_b128 v[224:227], v240 offset:20480\n"
    "buffer_load_dwordx4 v245, s[44:47], s56 offen offset:128 nt lds\n"
    "ds_read_b128 v[228:231], v240 offset:22528\n"
    "s_waitcnt lgkmcnt(4)\n"
    "s_mov_b32 s60, 7\n"
    ".p2align 6\n"
    "LS_loop_%=:\n"
    "v_mfma_f32_16x16x32_bf16 v[160:163], v[208:211], v[0:3], v[192:195]\n"
    "v_exp_f32 v141, v141\n"
    "v_mfma_f32_16x16x32_bf16 v[164:167], v[212:215], v[0:3], v[192:195]\n"
    "v_exp_f32 v142, v142\n"
    "v_mfma_f32_16x16x32_bf16 v[172:175], v[212:215], v[32:35], v[196:199]\n"
    "s_add_u32 m0, s59, 0x8700\n"
    "v_add_f32 v247, v141, v247\n"
    "v_mfma_f32_16x16x32_bf16 v[168:171], v[208:211], v[32:35], v[196:199]\n"
    "ds_read_b128 v[232:235], v241 offset:20480\n"
    "buffer_load_dwordx4 v245, s[44:47], s56 offen offset:256 nt lds\n"
    "v_mfma_f32_16x16x32_bf16 v[176:179], v[208:211], v[64:67], v[200:203]\n"
    "v_cvt_pk_bf16_f32 v138, v140, v141\n"
    "v_add_f32 v247, v142, v247\n"
    "v_mfma_f32_16x16x32_bf16 v[180:183], v[212:215], v[64:67], v[200:203]\n"
    "v_exp_f32 v143, v143\n"
    "v_mfma_f32_16x16x32_bf16 v[188:191], v[212:215], v[96:99], v[204:207]\n"
    "v_add_f32 v247, v143, v247\n"
    "v_cvt_pk_bf16_f32 v139, v142, v143\n"
    "v_mfma_f32_16x16x32_bf16 v[184:187], v[208:211], v[96:99], v[204:207]\n"
    "ds_read_b128 v[236:239], v241 offset:22528\n"
    "s_waitcnt lgkmcnt(4)\n"
    "v_mfma_f32_16x16x32_bf16 v[184:187], v[216:219], v[100:103], v[184:187]\n"
    "v_exp_f32 v144, v144\n"
    "v_mfma_f32_16x16x32_bf16 v[188:191], v[220:223], v[100:103], v[188:191]\n"
    "v_add_f32 v248, v144, v248\n"
    "v_mfma_f32_16x16x32_bf16 v[180:183], v[220:223], v[68:71], v[180:183]\n"
    "s_add_u32 m0, s59, 0x8a80\n"
    "v_mfma_f32_16x16x32_bf16 v[176:179], v[216:219], v[68:71], v[176:179]\n"
    "ds_read_b128 v[208:211], v240 offset:24576\n"
    "buffer_load_dwordx4 v245, s[44:47], s56 offen offset:384 nt lds\n"
    "v_mfma_f32_16x16x32_bf16 v[168:171], v[216:219], v[36:39], v[168:171]\n"
    "v_exp_f32 v145, v145\n"
    "v_mfma_f32_16x16x32_bf16 v[172:175], v[220:223], v[36:39], v[172:175]\n"
    "v_add_f32 v248, v145, v248\n"
    "v_cvt_pk_bf16_f32 v144, v144, v145\n"
    "v_mfma_f32_16x16x32_bf16 v[164:167], v[220:223], v[4:7], v[164:167]\n"
    "v_exp_f32 v146, v146\n"
    "v_mfma_f32_16x16x32_bf16 v[160:163], v[216:219], v[4:7], v[160:163]\n"
    "ds_read_b128 v[212:215], v240 offset:26624\n"
    "s_waitcnt lgkmcnt(4)\n"
    "v_mfma_f32_16x16x32_bf16 v[160:163], v[224:227], v[8:11], v[160:163]\n"
    "v_exp_f32 v147, v147\n"
    "v_mfma_f32_16x16x32_bf16 v[164:167], v[228:231], v[8:11], v[164:167]\n"
    "v_add_f32 v248, v146, v248\n"
    "v_add_f32 v248, v147, v248\n"
    "v_mfma_f32_16x16x32_bf16 v[172:175], v[228:231], v[40:43], v[172:175]\n"
    "s_add_u32 m0, s58, 0x0\n"
    "v_cvt_pk_bf16_f32 v145, v146, v147\n"
    "v_mfma_f32_16x16x32_bf16 v[168:171], v[224:227], v[40:43], v[168:171]\n"
    "ds_read_b128 v[216:219], v241 offset:24576\n"
    "buffer_load_dwordx4 v244, s[40:43], s56 offen offset:0 nt lds\n"
    "v_mfma_f32_16x16x32_bf16 v[176:179], v[224:227], v[72:75], v[176:179]\n"
    "v_exp_f32 v148, v148\n"
    "v_mfma_f32_16x16x32_bf16 v[180:183], v[228:231], v[72:75], v[180:183]\n"
    "v_add_f32 v248, v148, v248\n"
    "v_mfma_f32_16x16x32_bf16 v[188:191], v[228:231], v[104:107], v[188:191]\n"
    "v_exp_f32 v149, v149\n"
    "v_mfma_f32_16x16x32_bf16 v[184:187], v[224:227], v[104:107], v[184:187]\n"
    "ds_read_b128 v[220:223], v241 offset:26624\n"
    "s_waitcnt lgkmcnt(4)\n"
    "v_mfma_f32_16x16x32_bf16 v[184:187], v[232:235], v[108:111], v[184:187]\n"
    "v_add_f32 v248, v149, v248\n"
    "v_cvt_pk_bf16_f32 v146, v148, v149\n"
    "v_mfma_f32_16x16x32_bf16 v[188:191], v[236:239], v[108:111], v[188:191]\n"
    "v_exp_f32 v150, v150\n"
    "v_mfma_f32_16x16x32_bf16 v[180:183], v[236:239], v[76:79], v[180:183]\n"
    "s_add_u32 m0, s58, 0xf80\n"
    "v_add_f32 v248, v150, v248\n"
    "v_mfma_f32_16x16x32_bf16 v[176:179], v[232:235], v[76:79], v[176:179]\n"
    "ds_read_b128 v[224:227], v240 offset:28672\n"
    "buffer_load_dwordx4 v244, s[40:43], s56 offen offset:128 nt lds\n"
    "v_mfma_f32_16x16x32_bf16 v[168:171], v[232:235], v[44:47], v[168:171]\n"
    "v_exp_f32 v151, v151\n"
    "v_mfma_f32_16x16x32_bf16 v[172:175], v[236:239], v[44:47], v[172:175]\n"
    "v_add_f32 v248, v151, v248\n"
    "v_cvt_pk_bf16_f32 v147, v150, v151\n"
    "v_mfma_f32_16x16x32_bf16 v[164:167], v[236:239], v[12:15], v[164:167]\n"
    "v_exp_f32 v152, v152\n"
    "v_mfma_f32_16x16x32_bf16 v[160:163], v[232:235], v[12:15], v[160:163]\n"
    "ds_read_b128 v[228:231], v240 offset:30720\n"
    "s_waitcnt lgkmcnt(4)\n"
    "v_mfma_f32_16x16x32_bf16 v[160:163], v[208:211], v[16:19], v[160:163]\n"
    "v_add_f32 v249, v152, v249\n"
    "v_mfma_f32_16x16x32_bf16 v[164:167], v[212:215], v[16:19], v[164:167]\n"
    "v_exp_f32 v153, v153\n"
    "v_mfma_f32_16x16x32_bf16 v[172:175], v[212:215], v[48:51], v[172:175]\n"
    "s_add_u32 m0, s58, 0x1f00\n"
    "v_add_f32 v249, v153, v249\n"
    "v_mfma_f32_16x16x32_bf16 v[168:171], v[208:211], v[48:51], v[168:171]\n"
    "ds_read_b128 v[232:235], v241 offset:28672\n"
    "buffer_load_dwordx4 v244, s[40:43], s56 offen offset:256 nt lds\n"
    "v_mfma_f32_16x16x32_bf16 v[176:179], v[208:211], v[80:83], v[176:179]\n"
    "v_cvt_pk_bf16_f32 v152, v152, v153\n"
    "v_mfma_f32_16x16x32_bf16 v[180:183], v[212:215], v[80:83], v[180:183]\n"
    "v_exp_f32 v154, v154\n"
    "v_mfma_f32_16x16x32_bf16 v[188:191], v[212:215], v[112:115], v[188:191]\n"
    "v_add_f32 v249, v154, v249\n"
    "v_mfma_f32_16x16x32_bf16 v[184:187], v[208:211], v[112:115], v[184:187]\n"
    "ds_read_b128 v[236:239], v241 offset:30720\n"
    "s_waitcnt lgkmcnt(4)\n"
    "v_mfma_f32_16x16x32_bf16 v[184:187], v[216:219], v[116:119], v[184:187]\n"
    "v_exp_f32 v155, v155\n"
    "v_mfma_f32_16x16x32_bf16 v[188:191], v[220:223], v[116:119], v[188:191]\n"
    "v_exp_f32 v156, v156\n"
    "v_mfma_f32_16x16x32_bf16 v[180:183], v[220:223], v[84:87], v[180:183]\n"
    "ds_read_b64_tr_b16 v[208:209], v242 offset:0\n"
    "s_add_u32 m0, s58, 0x2e80\n"
    "v_mfma_f32_16x16x32_bf16 v[176:179], v[216:219], v[84:87], v[176:179]\n"
    "ds_read_b64_tr_b16 v[210:211], v242 offset:8192\n"
    "buffer_load_dwordx4 v244, s[40:43], s56 offen offset:384 nt lds\n"
    "v_mfma_f32_16x16x32_bf16 v[168:171], v[216:219], v[52:55], v[168:171]\n"
    "v_add_f32 v249, v155, v249\n"
    "v_cvt_pk_bf16_f32 v153, v154, v155\n"
    "v_mfma_f32_16x16x32_bf16 v[172:175], v[220:223], v[52:55], v[172:175]\n"
    "v_exp_f32 v157, v157\n"
    "v_mfma_f32_16x16x32_bf16 v[164:167], v[220:223], v[20:23], v[164:167]\n"
    "ds_read_b64_tr_b16 v[212:213], v243 offset:0\n"
    "v_add_f32 v249, v156, v249\n"
    "v_mfma_f32_16x16x32_bf16 v[160:163], v[216:219], v[20:23], v[160:163]\n"
    "ds_read_b64_tr_b16 v[214:215], v243 offset:8192\n"
    "s_waitcnt lgkmcnt(6)\n"
    "v_mfma_f32_16x16x32_bf16 v[160:163], v[224:227], v[24:27], v[160:163]\n"
    "v_add_f32 v249, v157, v249\n"
    "v_cvt_pk_bf16_f32 v154, v156, v157\n"
    "v_mfma_f32_16x16x32_bf16 v[164:167], v[228:231], v[24:27], v[164:167]\n"
    "v_exp_f32 v158, v158\n"
    "v_mfma_f32_16x16x32_bf16 v[172:175], v[228:231], v[56:59], v[172:175]\n"
    "ds_read_b64_tr_b16 v[216:217], v242 offset:512\n"
    "s_add_u32 s56, s56, 0x4000\n"
    "v_mfma_f32_16x16x32_bf16 v[168:171], v[224:227], v[56:59], v[168:171]\n"
    "ds_read_b64_tr_b16 v[218:219], v242 offset:8704\n"
    "v_add_f32 v249, v158, v249\n"
    "v_mfma_f32_16x16x32_bf16 v[176:179], v[224:227], v[88:91], v[176:179]\n"
    "v_mfma_f32_16x16x32_bf16 v[180:183], v[228:231], v[88:91], v[180:183]\n"
    "v_exp_f32 v159, v159\n"
    "v_mfma_f32_16x16x32_bf16 v[188:191], v[228:231], v[120:123], v[188:191]\n"
    "ds_read_b64_tr_b16 v[220:221], v243 offset:512\n"
    "v_add_f32 v249, v159, v249\n"
    "v_mfma_f32_16x16x32_bf16 v[184:187], v[224:227], v[120:123], v[184:187]\n"
    "ds_read_b64_tr_b16 v[222:223], v243 offset:8704\n"
    "s_waitcnt lgkmcnt(8)\n"
    "v_mfma_f32_16x16x32_bf16 v[184:187], v[232:235], v[124:127], v[184:187]\n"
    "v_cvt_pk_bf16_f32 v155, v158, v159\n"
    "v_mfma_f32_16x16x32_bf16 v[188:191], v[236:239], v[124:127], v[188:191]\n"
    "v_mfma_f32_16x16x32_bf16 v[180:183], v[236:239], v[92:95], v[180:183]\n"
    "ds_read_b64_tr_b16 v[224:225], v242 offset:1024\n"
    "v_mfma_f32_16x16x32_bf16 v[176:179], v[232:235], v[92:95], v[176:179]\n"
    "ds_read_b64_tr_b16 v[226:227], v242 offset:9216\n"
    "v_mfma_f32_16x16x32_bf16 v[168:171], v[232:235], v[60:63], v[168:171]\n"
    "v_mfma_f32_16x16x32_bf16 v[172:175], v[236:239], v[60:63], v[172:175]\n"
    "v_mfma_f32_16x16x32_bf16 v[164:167], v[236:239], v[28:31], v[164:167]\n"
    "ds_read_b64_tr_b16 v[228:229], v243 offset:1024\n"
    "v_mfma_f32_16x16x32_bf16 v[160:163], v[232:235], v[28:31], v[160:163]\n"
    "ds_read_b64_tr_b16 v[230:231], v243 offset:9216\n"
    "s_waitcnt lgkmcnt(8)\n"
    "v_mfma_f32_16x16x32_bf16 a[0:3], v[208:211], v[128:131], a[0:3]\n"
    "v_mfma_f32_16x16x32_bf16 a[16:19], v[212:215], v[128:131], a[16:19]\n"
    "v_mfma_f32_16x16x32_bf16 a[20:23], v[212:215], v[136:139], a[20:23]\n"
    "ds_read_b64_tr_b16 v[232:233], v242 offset:1536\n"
    "v_mfma_f32_16x16x32_bf16 a[4:7], v[208:211], v[136:139], a[4:7]\n"
    "ds_read_b64_tr_b16 v[234:235], v242 offset:9728\n"
    "v_mfma_f32_16x16x32_bf16 a[8:11], v[208:211], v[144:147], a[8:11]\n"
    "v_mfma_f32_16x16x32_bf16 a[24:27], v[212:215], v[144:147], a[24:27]\n"
    "v_max3_f32 v250, v160, v161, v162\n"
    "v_max3_f32 v250, v250, v163, v168\n"
    "v_mfma_f32_16x16x32_bf16 a[28:31], v[212:215], v[152:155], a[28:31]\n"
    "ds_read_b64_tr_b16 v[236:237], v243 offset:1536\n"
    "v_max3_f32 v250, v250, v169, v170\n"
    "v_mfma_f32_16x16x32_bf16 a[12:15], v[208:211], v[152:155], a[12:15]\n"
    "ds_read_b64_tr_b16 v[238:239], v243 offset:9728\n"
    "s_waitcnt lgkmcnt(8)\n"
    "v_mfma_f32_16x16x32_bf16 a[44:47], v[216:219], v[152:155], a[44:47]\n"
    "v_max3_f32 v250, v250, v171, v176\n"
    "v_max3_f32 v250, v250, v177, v178\n"
    "v_mfma_f32_16x16x32_bf16 a[60:63], v[220:223], v[152:155], a[60:63]\n"
    "v_max3_f32 v250, v250, v179, v184\n"
    "v_max3_f32 v250, v250, v185, v186\n"
    "v_mfma_f32_16x16x32_bf16 a[56:59], v[220:223], v[144:147], a[56:59]\n"
    "ds_read_b64_tr_b16 v[208:209], v242 offset:2048\n"
    "v_max3_f32 v250, v250, v187, v164\n"
    "v_mfma_f32_16x16x32_bf16 a[40:43], v[216:219], v[144:147], a[40:43]\n"
    "ds_read_b64_tr_b16 v[210:211], v242 offset:10240\n"
    "v_max3_f32 v250, v250, v165, v166\n"
    "v_mfma_f32_16x16x32_bf16 a[36:39], v[216:219], v[136:139], a[36:39]\n"
    "v_max3_f32 v250, v250, v167, v172\n"
    "v_max3_f32 v250, v250, v173, v174\n"
    "v_mfma_f32_16x16x32_bf16 a[52:55], v[220:223], v[136:139], a[52:55]\n"
    "v_max3_f32 v250, v250, v175, v180\n"
    "v_max3_f32 v250, v250, v181, v182\n"
    "v_mfma_f32_16x16x32_bf16 a[48:51], v[220:223], v[128:131], a[48:51]\n"
    "ds_read_b64_tr_b16 v[212:213], v243 offset:2048\n"
    "v_max3_f32 v250, v250, v183, v188\n"
    "v_mfma_f32_16x16x32_bf16 a[32:35], v[216:219], v[128:131], a[32:35]\n"
    "ds_read_b64_tr_b16 v[214:215], v243 offset:10240\n"
    "s_waitcnt lgkmcnt(8)\n"
    "v_mfma_f32_16x16x32_bf16 a[64:67], v[224:227], v[128:131], a[64:67]\n"
    "v_max3_f32 v250, v250, v189, v190\n"
    "v_max_f32 v250, v250, v191\n"
    "v_mfma_f32_16x16x32_bf16 a[80:83], v[228:231], v[128:131], a[80:83]\n"
    "v_cmp_lt_f32 vcc, 0x41000000, v250\n"
    "v_mfma_f32_16x16x32_bf16 a[84:87], v[228:231], v[136:139], a[84:87]\n"
    "ds_read_b64_tr_b16 v[216:217], v242 offset:2560\n"
    "v_mfma_f32_16x16x32_bf16 a[68:71], v[224:227], v[136:139], a[68:71]\n"
    "ds_read_b64_tr_b16 v[218:219], v242 offset:10752\n"
    "v_mfma_f32_16x16x32_bf16 a[72:75], v[224:227], v[144:147], a[72:75]\n"
    "s_cbranch_vccnz LS_st1_0_%=\n"
    "LS_st1_0_back_%=:\n"
    "v_mfma_f32_16x16x32_bf16 a[88:91], v[228:231], v[144:147], a[88:91]\n"
    "v_exp_f32 v160, v160\n"
    "v_mfma_f32_16x16x32_bf16 a[92:95], v[228:231], v[152:155], a[92:95]\n"
    "ds_read_b64_tr_b16 v[220:221], v243 offset:2560\n"
    "v_add_f32 v246, v160, v246\n"
    "v_mfma_f32_16x16x32_bf16 a[76:79], v[224:227], v[152:155], a[76:79]\n"
    "ds_read_b64_tr_b16 v[222:223], v243 offset:10752\n"
    "s_waitcnt lgkmcnt(8)\n"
    "v_mfma_f32_16x16x32_bf16 a[108:111], v[232:235], v[152:155], a[108:111]\n"
    "v_exp_f32 v161, v161\n"
    "v_mfma_f32_16x16x32_bf16 a[124:127], v[236:239], v[152:155], a[124:127]\n"
    "v_add_f32 v246, v161, v246\n"
    "v_cvt_pk_bf16_f32 v160, v160, v161\n"
    "v_mfma_f32_16x16x32_bf16 a[120:123], v[236:239], v[144:147], a[120:123]\n"
    "ds_read_b64_tr_b16 v[224:225], v242 offset:3072\n"
    "v_mfma_f32_16x16x32_bf16 a[104:107], v[232:235], v[144:147], a[104:107]\n"
    "ds_read_b64_tr_b16 v[226:227], v242 offset:11264\n"
    "v_mfma_f32_16x16x32_bf16 a[100:103], v[232:235], v[136:139], a[100:103]\n"
    "v_exp_f32 v162, v162\n"
    "v_mfma_f32_16x16x32_bf16 a[116:119], v[236:239], v[136:139], a[116:119]\n"
    "v_add_f32 v246, v162, v246\n"
    "v_mfma_f32_16x16x32_bf16 a[112:115], v[236:239], v[128:131], a[112:115]\n"
    "ds_read_b64_tr_b16 v[228:229], v243 offset:3072\n"
    "v_mfma_f32_16x16x32_bf16 a[96:99], v[232:235], v[128:131], a[96:99]\n"
    "ds_read_b64_tr_b16 v[230:231], v243 offset:11264\n"
    "s_waitcnt lgkmcnt(8)\n"
    "v_mfma_f32_16x16x32_bf16 a[128:131], v[208:211], v[128:131], a[128:131]\n"
    "v_exp_f32 v163, v163\n"
    "v_mfma_f32_16x16x32_bf16 a[144:147], v[212:215], v[128:131], a[144:147]\n"
    "v_add_f32 v246, v163, v246\n"
    "v_cvt_pk_bf16_f32 v161, v162, v163\n"
    "v_mfma_f32_16x16x32_bf16 a[148:151], v[212:215], v[136:139], a[148:151]\n"
    "ds_read_b64_tr_b16 v[232:233], v242 offset:3584\n"
    "v_mfma_f32_16x16x32_bf16 a[132:135], v[208:211], v[136:139], a[132:135]\n"
    "ds_read_b64_tr_b16 v[234:235], v242 offset:11776\n"
    "v_mfma_f32_16x16x32_bf16 a[136:139], v[208:211], v[144:147], a[136:139]\n"
    "v_exp_f32 v164, v164\n"
    "v_mfma_f32_16x16x32_bf16 a[152:155], v[212:215], v[144:147], a[152:155]\n"
    "v_exp_f32 v165, v165\n"
    "v_mfma_f32_16x16x32_bf16 a[156:159], v[212:215], v[152:155], a[156:159]\n"
    "ds_read_b64_tr_b16 v[236:237], v243 offset:3584\n"
    "v_add_f32 v246, v164, v246\n"
    "v_mfma_f32_16x16x32_bf16 a[140:143], v[208:211], v[152:155], a[140:143]\n"
    "ds_read_b64_tr_b16 v[238:239], v243 offset:11776\n"
    "s_waitcnt lgkmcnt(8)\n"
    "v_mfma_f32_16x16x32_bf16 a[172:175], v[216:219], v[152:155], a[172:175]\n"
    "v_add_f32 v246, v165, v246\n"
    "v_cvt_pk_bf16_f32 v162, v164, v165\n"
    "v_mfma_f32_16x16x32_bf16 a[188:191], v[220:223], v[152:155], a[188:191]\n"
    "v_exp_f32 v166, v166\n"
    "v_mfma_f32_16x16x32_bf16 a[184:187], v[220:223], v[144:147], a[184:187]\n"
    "s_waitcnt vmcnt(12)\n"
    "s_barrier\n"
    "v_mfma_f32_16x16x32_bf16 a[168:171], v[216:219], v[144:147], a[168:171]\n"
    "ds_read_b128 v[208:211], v240 offset:32768\n"
    "v_add_f32 v246, v166, v246\n"
    "v_mfma_f32_16x16x32_bf16 a[164:167], v[216:219], v[136:139], a[164:167]\n"
    "v_mfma_f32_16x16x32_bf16 a[180:183], v[220:223], v[136:139], a[180:183]\n"
    "v_exp_f32 v167, v167\n"
    "v_mfma_f32_16x16x32_bf16 a[176:179], v[220:223], v[128:131], a[176:179]\n"
    "v_add_f32 v246, v167, v246\n"
    "v_cvt_pk_bf16_f32 v163, v166, v167\n"
    "v_mfma_f32_16x16x32_bf16 a[160:163], v[216:219], v[128:131], a[160:163]\n"
    "ds_read_b128 v[212:215], v240 offset:34816\n"
    "s_waitcnt lgkmcnt(6)\n"
    "v_mfma_f32_16x16x32_bf16 a[192:195], v[224:227], v[128:131], a[192:195]\n"
    "s_add_u32 m0, s59, 0xc000\n"
    "v_mfma_f32_16x16x32_bf16 a[208:211], v[228:231], v[128:131], a[208:211]\n"
    "buffer_load_dwordx4 v245, s[44:47], s56 offen offset:0 nt lds\n"
    "v_mfma_f32_16x16x32_bf16 a[212:215], v[228:231], v[136:139], a[212:215]\n"
    "v_exp_f32 v168, v168\n"
    "v_mfma_f32_16x16x32_bf16 a[196:199], v[224:227], v[136:139], a[196:199]\n"
    "ds_read_b128 v[216:219], v241 offset:32768\n"
    "v_add_f32 v247, v168, v247\n"
    "v_mfma_f32_16x16x32_bf16 a[200:203], v[224:227], v[144:147], a[200:203]\n"
    "v_mfma_f32_16x16x32_bf16 a[216:219], v[228:231], v[144:147], a[216:219]\n"
    "v_exp_f32 v169, v169\n"
    "v_mfma_f32_16x16x32_bf16 a[220:223], v[228:231], v[152:155], a[220:223]\n"
    "v_exp_f32 v170, v170\n"
    "v_mfma_f32_16x16x32_bf16 a[204:207], v[224:227], v[152:155], a[204:207]\n"
    "ds_read_b128 v[220:223], v241 offset:34816\n"
    "s_waitcnt lgkmcnt(4)\n"
    "v_mfma_f32_16x16x32_bf16 a[236:239], v[232:235], v[152:155], a[236:239]\n"
    "v_add_f32 v247, v169, v247\n"
    "v_cvt_pk_bf16_f32 v168, v168, v169\n"
    "v_mfma_f32_16x16x32_bf16 a[252:255], v[236:239], v[152:155], a[252:255]\n"
    "v_exp_f32 v171, v171\n"
    "v_mfma_f32_16x16x32_bf16 a[248:251], v[236:239], v[144:147], a[248:251]\n"
    "s_add_u32 m0, s59, 0xc380\n"
    "v_add_f32 v247, v170, v247\n"
    "v_mfma_f32_16x16x32_bf16 a[232:235], v[232:235], v[144:147], a[232:235]\n"
    "ds_read_b128 v[224:227], v240 offset:36864\n"
    "buffer_load_dwordx4 v245, s[44:47], s56 offen offset:128 nt lds\n"
    "v_mfma_f32_16x16x32_bf16 a[228:231], v[232:235], v[136:139], a[228:231]\n"
    "v_add_f32 v247, v171, v247\n"
    "v_cvt_pk_bf16_f32 v169, v170, v171\n"
    "v_mfma_f32_16x16x32_bf16 a[244:247], v[236:239], v[136:139], a[244:247]\n"
    "v_exp_f32 v172, v172\n"
    "v_mfma_f32_16x16x32_bf16 a[240:243], v[236:239], v[128:131], a[240:243]\n"
    "v_add_f32 v247, v172, v247\n"
    "v_mfma_f32_16x16x32_bf16 a[224:227], v[232:235], v[128:131], a[224:227]\n"
    "s_cbranch_vccnz LS_st2_1_%=\n"
    "LS_st2_1_back_%=:\n"
    "ds_read_b128 v[228:231], v240 offset:38912\n"
    "s_waitcnt lgkmcnt(4)\n"
    "v_mfma_f32_16x16x32_bf16 v[128:131], v[208:211], v[0:3], v[192:195]\n"
    "v_exp_f32 v173, v173\n"
    "v_mfma_f32_16x16x32_bf16 v[132:135], v[212:215], v[0:3], v[192:195]\n"
    "v_exp_f32 v174, v174\n"
    "v_mfma_f32_16x16x32_bf16 v[140:143], v[212:215], v[32:35], v[196:199]\n"
    "s_add_u32 m0, s59, 0xc700\n"
    "v_add_f32 v247, v173, v247\n"
    "v_mfma_f32_16x16x32_bf16 v[136:139], v[208:211], v[32:35], v[196:199]\n"
    "ds_read_b128 v[232:235], v241 offset:36864\n"
    "buffer_load_dwordx4 v245, s[44:47], s56 offen offset:256 nt lds\n"
    "v_mfma_f32_16x16x32_bf16 v[144:147], v[208:211], v[64:67], v[200:203]\n"
    "v_cvt_pk_bf16_f32 v170, v172, v173\n"
    "v_add_f32 v247, v174, v247\n"
    "v_mfma_f32_16x16x32_bf16 v[148:151], v[212:215], v[64:67], v[200:203]\n"
    "v_exp_f32 v175, v175\n"
    "v_mfma_f32_16x16x32_bf16 v[156:159], v[212:215], v[96:99], v[204:207]\n"
    "v_add_f32 v247, v175, v247\n"
    "v_cvt_pk_bf16_f32 v171, v174, v175\n"
    "v_mfma_f32_16x16x32_bf16 v[152:155], v[208:211], v[96:99], v[204:207]\n"
    "ds_read_b128 v[236:239], v241 offset:38912\n"
    "s_waitcnt lgkmcnt(4)\n"
    "v_mfma_f32_16x16x32_bf16 v[152:155], v[216:219], v[100:103], v[152:155]\n"
    "v_exp_f32 v176, v176\n"
    "v_mfma_f32_16x16x32_bf16 v[156:159], v[220:223], v[100:103], v[156:159]\n"
    "v_add_f32 v248, v176, v248\n"
    "v_mfma_f32_16x16x32_bf16 v[148:151], v[220:223], v[68:71], v[148:151]\n"
    "s_add_u32 m0, s59, 0xca80\n"
    "v_mfma_f32_16x16x32_bf16 v[144:147], v[216:219], v[68:71], v[144:147]\n"
    "ds_read_b128 v[208:211], v240 offset:40960\n"
    "buffer_load_dwordx4 v245, s[44:47], s56 offen offset:384 nt lds\n"
    "v_mfma_f32_16x16x32_bf16 v[136:139], v[216:219], v[36:39], v[136:139]\n"
    "v_exp_f32 v177, v177\n"
    "v_mfma_f32_16x16x32_bf16 v[140:143], v[220:223], v[36:39], v[140:143]\n"
    "v_add_f32 v248, v177, v248\n"
    "v_cvt_pk_bf16_f32 v176, v176, v177\n"
    "v_mfma_f32_16x16x32_bf16 v[132:135], v[220:223], v[4:7], v[132:135]\n"
    "v_exp_f32 v178, v178\n"
    "v_mfma_f32_16x16x32_bf16 v[128:131], v[216:219], v[4:7], v[128:131]\n"
    "ds_read_b128 v[212:215], v240 offset:43008\n"
    "s_waitcnt lgkmcnt(4)\n"
    "v_mfma_f32_16x16x32_bf16 v[128:131], v[224:227], v[8:11], v[128:131]\n"
    "v_exp_f32 v179, v179\n"
    "v_mfma_f32_16x16x32_bf16 v[132:135], v[228:231], v[8:11], v[132:135]\n"
    "v_add_f32 v248, v178, v248\n"
    "v_add_f32 v248, v179, v248\n"
    "v_mfma_f32_16x16x32_bf16 v[140:143], v[228:231], v[40:43], v[140:143]\n"
    "s_add_u32 m0, s58, 0x4000\n"
    "v_cvt_pk_bf16_f32 v177, v178, v179\n"
    "v_mfma_f32_16x16x32_bf16 v[136:139], v[224:227], v[40:43], v[136:139]\n"
    "ds_read_b128 v[216:219], v241 offset:40960\n"
    "buffer_load_dwordx4 v244, s[40:43], s56 offen offset:0 nt lds\n"
    "v_mfma_f32_16x16x32_bf16 v[144:147], v[224:227], v[72:75], v[144:147]\n"
    "v_exp_f32 v180, v180\n"
    "v_mfma_f32_16x16x32_bf16 v[148:151], v[228:231], v[72:75], v[148:151]\n"
    "v_add_f32 v248, v180, v248\n"
    "v_mfma_f32_16x16x32_bf16 v[156:159], v[228:231], v[104:107], v[156:159]\n"
    "v_exp_f32 v181, v181\n"
    "v_mfma_f32_16x16x32_bf16 v[152:155], v[224:227], v[104:107], v[152:155]\n"
    "ds_read_b128 v[220:223], v241 offset:43008\n"
    "s_waitcnt lgkmcnt(4)\n"
    "v_mfma_f32_16x16x32_bf16 v[152:155], v[232:235], v[108:111], v[152:155]\n"
    "v_add_f32 v248, v181, v248\n"
    "v_cvt_pk_bf16_f32 v178, v180, v181\n"
    "v_mfma_f32_16x16x32_bf16 v[156:159], v[236:239], v[108:111], v[156:159]\n"
    "v_exp_f32 v182, v182\n"
    "v_mfma_f32_16x16x32_bf16 v[148:151], v[236:239], v[76:79], v[148:151]\n"
    "s_add_u32 m0, s58, 0x4f80\n"
    "v_add_f32 v248, v182, v248\n"
    "v_mfma_f32_16x16x32_bf16 v[144:147], v[232:235], v[76:79], v[144:147]\n"
    "ds_read_b128 v[224:227], v240 offset:45056\n"
    "buffer_load_dwordx4 v244, s[40:43], s56 offen offset:128 nt lds\n"
    "v_mfma_f32_16x16x32_bf16 v[136:139], v[232:235], v[44:47], v[136:139]\n"
    "v_exp_f32 v183, v183\n"
    "v_mfma_f32_16x16x32_bf16 v[140:143], v[236:239], v[44:47], v[140:143]\n"
    "v_add_f32 v248, v183, v248\n"
    "v_cvt_pk_bf16_f32 v179, v182, v183\n"
    "v_mfma_f32_16x16x32_bf16 v[132:135], v[236:239], v[12:15], v[132:135]\n"
    "v_exp_f32 v184, v184\n"
    "v_mfma_f32_16x16x32_bf16 v[128:131], v[232:235], v[12:15], v[128:131]\n"
    "ds_read_b128 v[228:231], v240 offset:47104\n"
    "s_waitcnt lgkmcnt(4)\n"
    "v_mfma_f32_16x16x32_bf16 v[128:131], v[208:211], v[16:19], v[128:131]\n"
    "v_add_f32 v249, v184, v249\n"
    "v_mfma_f32_16x16x32_bf16 v[132:135], v[212:215], v[16:19], v[132:135]\n"
    "v_exp_f32 v185, v185\n"
    "v_mfma_f32_16x16x32_bf16 v[140:143], v[212:215], v[48:51], v[140:143]\n"
    "s_add_u32 m0, s58, 0x5f00\n"
    "v_add_f32 v249, v185, v249\n"
    "v_mfma_f32_16x16x32_bf16 v[136:139], v[208:211], v[48:51], v[136:139]\n"
    "ds_read_b128 v[232:235], v241 offset:45056\n"
    "buffer_load_dwordx4 v244, s[40:43], s56 offen offset:256 nt lds\n"
    "v_mfma_f32_16x16x32_bf16 v[144:147], v[208:211], v[80:83], v[144:147]\n"
    "v_cvt_pk_bf16_f32 v184, v184, v185\n"
    "v_mfma_f32_16x16x32_bf16 v[148:151], v[212:215], v[80:83], v[148:151]\n"
    "v_exp_f32 v186, v186\n"
    "v_mfma_f32_16x16x32_bf16 v[156:159], v[212:215], v[112:115], v[156:159]\n"
    "v_add_f32 v249, v186, v249\n"
    "v_mfma_f32_16x16x32_bf16 v[152:155], v[208:211], v[112:115], v[152:155]\n"
    "ds_read_b128 v[236:239], v241 offset:47104\n"
    "s_waitcnt lgkmcnt(4)\n"
    "v_mfma_f32_16x16x32_bf16 v[152:155], v[216:219], v[116:119], v[152:155]\n"
    "v_exp_f32 v187, v187\n"
    "v_mfma_f32_16x16x32_bf16 v[156:159], v[220:223], v[116:119], v[156:159]\n"
    "v_exp_f32 v188, v188\n"
    "v_mfma_f32_16x16x32_bf16 v[148:151], v[220:223], v[84:87], v[148:151]\n"
    "ds_read_b64_tr_b16 v[208:209], v242 offset:16384\n"
    "s_add_u32 m0, s58, 0x6e80\n"
    "v_mfma_f32_16x16x32_bf16 v[144:147], v[216:219], v[84:87], v[144:147]\n"
    "ds_read_b64_tr_b16 v[210:211], v242 offset:24576\n"
    "buffer_load_dwordx4 v244, s[40:43], s56 offen offset:384 nt lds\n"
    "v_mfma_f32_16x16x32_bf16 v[136:139], v[216:219], v[52:55], v[136:139]\n"
    "v_add_f32 v249, v187, v249\n"
    "v_cvt_pk_bf16_f32 v185, v186, v187\n"
    "v_mfma_f32_16x16x32_bf16 v[140:143], v[220:223], v[52:55], v[140:143]\n"
    "v_exp_f32 v189, v189\n"
    "v_mfma_f32_16x16x32_bf16 v[132:135], v[220:223], v[20:23], v[132:135]\n"
    "ds_read_b64_tr_b16 v[212:213], v243 offset:16384\n"
    "v_add_f32 v249, v188, v249\n"
    "v_mfma_f32_16x16x32_bf16 v[128:131], v[216:219], v[20:23], v[128:131]\n"
    "ds_read_b64_tr_b16 v[214:215], v243 offset:24576\n"
    "s_waitcnt lgkmcnt(6)\n"
    "v_mfma_f32_16x16x32_bf16 v[128:131], v[224:227], v[24:27], v[128:131]\n"
    "v_add_f32 v249, v189, v249\n"
    "v_cvt_pk_bf16_f32 v186, v188, v189\n"
    "v_mfma_f32_16x16x32_bf16 v[132:135], v[228:231], v[24:27], v[132:135]\n"
    "v_exp_f32 v190, v190\n"
    "v_mfma_f32_16x16x32_bf16 v[140:143], v[228:231], v[56:59], v[140:143]\n"
    "ds_read_b64_tr_b16 v[216:217], v242 offset:16896\n"
    "s_add_u32 s56, s56, 0x4000\n"
    "v_mfma_f32_16x16x32_bf16 v[136:139], v[224:227], v[56:59], v[136:139]\n"
    "ds_read_b64_tr_b16 v[218:219], v242 offset:25088\n"
    "v_add_f32 v249, v190, v249\n"
    "v_mfma_f32_16x16x32_bf16 v[144:147], v[224:227], v[88:91], v[144:147]\n"
    "v_mfma_f32_16x16x32_bf16 v[148:151], v[228:231], v[88:91], v[148:151]\n"
    "v_exp_f32 v191, v191\n"
    "v_mfma_f32_16x16x32_bf16 v[156:159], v[228:231], v[120:123], v[156:159]\n"
    "ds_read_b64_tr_b16 v[220:221], v243 offset:16896\n"
    "v_add_f32 v249, v191, v249\n"
    "v_mfma_f32_16x16x32_bf16 v[152:155], v[224:227], v[120:123], v[152:155]\n"
    "ds_read_b64_tr_b16 v[222:223], v243 offset:25088\n"
    "s_waitcnt lgkmcnt(8)\n"
    "v_mfma_f32_16x16x32_bf16 v[152:155], v[232:235], v[124:127], v[152:155]\n"
    "v_cvt_pk_bf16_f32 v187, v190, v191\n"
    "v_mfma_f32_16x16x32_bf16 v[156:159], v[236:239], v[124:127], v[156:159]\n"
    "v_mfma_f32_16x16x32_bf16 v[148:151], v[236:239], v[92:95], v[148:151]\n"
    "ds_read_b64_tr_b16 v[224:225], v242 offset:17408\n"
    "v_mfma_f32_16x16x32_bf16 v[144:147], v[232:235], v[92:95], v[144:147]\n"
    "ds_read_b64_tr_b16 v[226:227], v242 offset:25600\n"
    "v_mfma_f32_16x16x32_bf16 v[136:139], v[232:235], v[60:63], v[136:139]\n"
    "v_mfma_f32_16x16x32_bf16 v[140:143], v[236:239], v[60:63], v[140:143]\n"
    "v_mfma_f32_16x16x32_bf16 v[132:135], v[236:239], v[28:31], v[132:135]\n"
    "ds_read_b64_tr_b16 v[228:229], v243 offset:17408\n"
    "v_mfma_f32_16x16x32_bf16 v[128:131], v[232:235], v[28:31], v[128:131]\n"
    "ds_read_b64_tr_b16 v[230:231], v243 offset:25600\n"
    "s_waitcnt lgkmcnt(8)\n"
    "v_mfma_f32_16x16x32_bf16 a[0:3], v[208:211], v[160:163], a[0:3]\n"
    "v_mfma_f32_16x16x32_bf16 a[16:19], v[212:215], v[160:163], a[16:19]\n"
    "v_mfma_f32_16x16x32_bf16 a[20:23], v[212:215], v[168:171], a[20:23]\n"
    "ds_read_b64_tr_b16 v[232:233], v242 offset:17920\n"
    "v_mfma_f32_16x16x32_bf16 a[4:7], v[208:211], v[168:171], a[4:7]\n"
    "ds_read_b64_tr_b16 v[234:235], v242 offset:26112\n"
    "v_mfma_f32_16x16x32_bf16 a[8:11], v[208:211], v[176:179], a[8:11]\n"
    "v_mfma_f32_16x16x32_bf16 a[24:27], v[212:215], v[176:179], a[24:27]\n"
    "v_max3_f32 v250, v128, v129, v130\n"
    "v_max3_f32 v250, v250, v131, v136\n"
    "v_mfma_f32_16x16x32_bf16 a[28:31], v[212:215], v[184:187], a[28:31]\n"
    "ds_read_b64_tr_b16 v[236:237], v243 offset:17920\n"
    "v_max3_f32 v250, v250, v137, v138\n"
    "v_mfma_f32_16x16x32_bf16 a[12:15], v[208:211], v[184:187], a[12:15]\n"
    "ds_read_b64_tr_b16 v[238:239], v243 offset:26112\n"
    "s_waitcnt lgkmcnt(8)\n"
    "v_mfma_f32_16x16x32_bf16 a[44:47], v[216:219], v[184:187], a[44:47]\n"
    "v_max3_f32 v250, v250, v139, v144\n"
    "v_max3_f32 v250, v250, v145, v146\n"
    "v_mfma_f32_16x16x32_bf16 a[60:63], v[220:223], v[184:187], a[60:63]\n"
    "v_max3_f32 v250, v250, v147, v152\n"
    "v_max3_f32 v250, v250, v153, v154\n"
    "v_mfma_f32_16x16x32_bf16 a[56:59], v[220:223], v[176:179], a[56:59]\n"
    "ds_read_b64_tr_b16 v[208:209], v242 offset:18432\n"
    "v_max3_f32 v250, v250, v155, v132\n"
    "v_mfma_f32_16x16x32_bf16 a[40:43], v[216:219], v[176:179], a[40:43]\n"
    "ds_read_b64_tr_b16 v[210:211], v242 offset:26624\n"
    "v_max3_f32 v250, v250, v133, v134\n"
    "v_mfma_f32_16x16x32_bf16 a[36:39], v[216:219], v[168:171], a[36:39]\n"
    "v_max3_f32 v250, v250, v135, v140\n"
    "v_max3_f32 v250, v250, v141, v142\n"
    "v_mfma_f32_16x16x32_bf16 a[52:55], v[220:223], v[168:171], a[52:55]\n"
    "v_max3_f32 v250, v250, v143, v148\n"
    "v_max3_f32 v250, v250, v149, v150\n"
    "v_mfma_f32_16x16x32_bf16 a[48:51], v[220:223], v[160:163], a[48:51]\n"
    "ds_read_b64_tr_b16 v[212:213], v243 offset:18432\n"
    "v_max3_f32 v250, v250, v151, v156\n"
    "v_mfma_f32_16x16x32_bf16 a[32:35], v[216:219], v[160:163], a[32:35]\n"
    "ds_read_b64_tr_b16 v[214:215], v243 offset:26624\n"
    "s_waitcnt lgkmcnt(8)\n"
    "v_mfma_f32_16x16x32_bf16 a[64:67], v[224:227], v[160:163], a[64:67]\n"
    "v_max3_f32 v250, v250, v157, v158\n"
    "v_max_f32 v250, v250, v159\n"
    "v_mfma_f32_16x16x32_bf16 a[80:83], v[228:231], v[160:163], a[80:83]\n"
    "v_cmp_lt_f32 vcc, 0x41000000, v250\n"
    "v_mfma_f32_16x16x32_bf16 a[84:87], v[228:231], v[168:171], a[84:87]\n"
    "ds_read_b64_tr_b16 v[216:217], v242 offset:18944\n"
    "v_mfma_f32_16x16x32_bf16 a[68:71], v[224:227], v[168:171], a[68:71]\n"
    "ds_read_b64_tr_b16 v[218:219], v242 offset:27136\n"
    "v_mfma_f32_16x16x32_bf16 a[72:75], v[224:227], v[176:179], a[72:75]\n"
    "s_cbranch_vccnz LS_st1_2_%=\n"
    "LS_st1_2_back_%=:\n"
    "v_mfma_f32_16x16x32_bf16 a[88:91], v[228:231], v[176:179], a[88:91]\n"
    "v_exp_f32 v128, v128\n"
    "v_mfma_f32_16x16x32_bf16 a[92:95], v[228:231], v[184:187], a[92:95]\n"
    "ds_read_b64_tr_b16 v[220:221], v243 offset:18944\n"
    "v_add_f32 v246, v128, v246\n"
    "v_mfma_f32_16x16x32_bf16 a[76:79], v[224:227], v[184:187], a[76:79]\n"
    "ds_read_b64_tr_b16 v[222:223], v243 offset:27136\n"
    "s_waitcnt lgkmcnt(8)\n"
    "v_mfma_f32_16x16x32_bf16 a[108:111], v[232:235], v[184:187], a[108:111]\n"
    "v_exp_f32 v129, v129\n"
    "v_mfma_f32_16x16x32_bf16 a[124:127], v[236:239], v[184:187], a[124:127]\n"
    "v_add_f32 v246, v129, v246\n"
    "v_cvt_pk_bf16_f32 v128, v128, v129\n"
    "v_mfma_f32_16x16x32_bf16 a[120:123], v[236:239], v[176:179], a[120:123]\n"
    "ds_read_b64_tr_b16 v[224:225], v242 offset:19456\n"
    "v_mfma_f32_16x16x32_bf16 a[104:107], v[232:235], v[176:179], a[104:107]\n"
    "ds_read_b64_tr_b16 v[226:227], v242 offset:27648\n"
    "v_mfma_f32_16x16x32_bf16 a[100:103], v[232:235], v[168:171], a[100:103]\n"
    "v_exp_f32 v130, v130\n"
    "v_mfma_f32_16x16x32_bf16 a[116:119], v[236:239], v[168:171], a[116:119]\n"
    "v_add_f32 v246, v130, v246\n"
    "v_mfma_f32_16x16x32_bf16 a[112:115], v[236:239], v[160:163], a[112:115]\n"
    "ds_read_b64_tr_b16 v[228:229], v243 offset:19456\n"
    "v_mfma_f32_16x16x32_bf16 a[96:99], v[232:235], v[160:163], a[96:99]\n"
    "ds_read_b64_tr_b16 v[230:231], v243 offset:27648\n"
    "s_waitcnt lgkmcnt(8)\n"
    "v_mfma_f32_16x16x32_bf16 a[128:131], v[208:211], v[160:163], a[128:131]\n"
    "v_exp_f32 v131, v131\n"
    "v_mfma_f32_16x16x32_bf16 a[144:147], v[212:215], v[160:163], a[144:147]\n"
    "v_add_f32 v246, v131, v246\n"
    "v_cvt_pk_bf16_f32 v129, v130, v131\n"
    "v_mfma_f32_16x16x32_bf16 a[148:151], v[212:215], v[168:171], a[148:151]\n"
    "ds_read_b64_tr_b16 v[232:233], v242 offset:19968\n"
    "v_mfma_f32_16x16x32_bf16 a[132:135], v[208:211], v[168:171], a[132:135]\n"
    "ds_read_b64_tr_b16 v[234:235], v242 offset:28160\n"
    "v_mfma_f32_16x16x32_bf16 a[136:139], v[208:211], v[176:179], a[136:139]\n"
    "v_exp_f32 v132, v132\n"
    "v_mfma_f32_16x16x32_bf16 a[152:155], v[212:215], v[176:179], a[152:155]\n"
    "v_exp_f32 v133, v133\n"
    "v_mfma_f32_16x16x32_bf16 a[156:159], v[212:215], v[184:187], a[156:159]\n"
    "ds_read_b64_tr_b16 v[236:237], v243 offset:19968\n"
    "v_add_f32 v246, v132, v246\n"
    "v_mfma_f32_16x16x32_bf16 a[140:143], v[208:211], v[184:187], a[140:143]\n"
    "ds_read_b64_tr_b16 v[238:239], v243 offset:28160\n"
    "s_waitcnt lgkmcnt(8)\n"
    "v_mfma_f32_16x16x32_bf16 a[172:175], v[216:219], v[184:187], a[172:175]\n"
    "v_add_f32 v246, v133, v246\n"
    "v_cvt_pk_bf16_f32 v130, v132, v133\n"
    "v_mfma_f32_16x16x32_bf16 a[188:191], v[220:223], v[184:187], a[188:191]\n"
    "v_exp_f32 v134, v134\n"
    "v_mfma_f32_16x16x32_bf16 a[184:187], v[220:223], v[176:179], a[184:187]\n"
    "s_waitcnt vmcnt(12)\n"
    "s_barrier\n"
    "v_mfma_f32_16x16x32_bf16 a[168:171], v[216:219], v[176:179], a[168:171]\n"
    "ds_read_b128 v[208:211], v240 offset:49152\n"
    "v_add_f32 v246, v134, v246\n"
    "v_mfma_f32_16x16x32_bf16 a[164:167], v[216:219], v[168:171], a[164:167]\n"
    "v_mfma_f32_16x16x32_bf16 a[180:183], v[220:223], v[168:171], a[180:183]\n"
    "v_exp_f32 v135, v135\n"
    "v_mfma_f32_16x16x32_bf16 a[176:179], v[220:223], v[160:163], a[176:179]\n"
    "v_add_f32 v246, v135, v246\n"
    "v_cvt_pk_bf16_f32 v131, v134, v135\n"
    "v_mfma_f32_16x16x32_bf16 a[160:163], v[216:219], v[160:163], a[160:163]\n"
    "ds_read_b128 v[212:215], v240 offset:51200\n"
    "s_waitcnt lgkmcnt(6)\n"
    "v_mfma_f32_16x16x32_bf16 a[192:195], v[224:227], v[160:163], a[192:195]\n"
    "s_add_u32 m0, s59, 0x0\n"
    "v_mfma_f32_16x16x32_bf16 a[208:211], v[228:231], v[160:163], a[208:211]\n"
    "buffer_load_dwordx4 v245, s[44:47], s56 offen offset:0 nt lds\n"
    "v_mfma_f32_16x16x32_bf16 a[212:215], v[228:231], v[168:171], a[212:215]\n"
    "v_exp_f32 v136, v136\n"
    "v_mfma_f32_16x16x32_bf16 a[196:199], v[224:227], v[168:171], a[196:199]\n"
    "ds_read_b128 v[216:219], v241 offset:49152\n"
    "v_add_f32 v247, v136, v247\n"
    "v_mfma_f32_16x16x32_bf16 a[200:203], v[224:227], v[176:179], a[200:203]\n"
    "v_mfma_f32_16x16x32_bf16 a[216:219], v[228:231], v[176:179], a[216:219]\n"
    "v_exp_f32 v137, v137\n"
    "v_mfma_f32_16x16x32_bf16 a[220:223], v[228:231], v[184:187], a[220:223]\n"
    "v_exp_f32 v138, v138\n"
    "v_mfma_f32_16x16x32_bf16 a[204:207], v[224:227], v[184:187], a[204:207]\n"
    "ds_read_b128 v[220:223], v241 offset:51200\n"
    "s_waitcnt lgkmcnt(4)\n"
    "v_mfma_f32_16x16x32_bf16 a[236:239], v[232:235], v[184:187], a[236:239]\n"
    "v_add_f32 v247, v137, v247\n"
    "v_cvt_pk_bf16_f32 v136, v136, v137\n"
    "v_mfma_f32_16x16x32_bf16 a[252:255], v[236:239], v[184:187], a[252:255]\n"
    "v_exp_f32 v139, v139\n"
    "v_mfma_f32_16x16x32_bf16 a[248:251], v[236:239], v[176:179], a[248:251]\n"
    "s_add_u32 m0, s59, 0x380\n"
    "v_add_f32 v247, v138, v247\n"
    "v_mfma_f32_16x16x32_bf16 a[232:235], v[232:235], v[176:179], a[232:235]\n"
    "ds_read_b128 v[224:227], v240 offset:53248\n"
    "buffer_load_dwordx4 v245, s[44:47], s56 offen offset:128 nt lds\n"
    "v_mfma_f32_16x16x32_bf16 a[228:231], v[232:235], v[168:171], a[228:231]\n"
    "v_add_f32 v247, v139, v247\n"
    "v_cvt_pk_bf16_f32 v137, v138, v139\n"
    "v_mfma_f32_16x16x32_bf16 a[244:247], v[236:239], v[168:171], a[244:247]\n"
    "v_exp_f32 v140, v140\n"
    "v_mfma_f32_16x16x32_bf16 a[240:243], v[236:239], v[160:163], a[240:243]\n"
    "v_add_f32 v247, v140, v247\n"
    "v_mfma_f32_16x16x32_bf16 a[224:227], v[232:235], v[160:163], a[224:227]\n"
    "s_cbranch_vccnz LS_st2_3_%=\n"
    "LS_st2_3_back_%=:\n"
    "ds_read_b128 v[228:231], v240 offset:55296\n"
    "s_waitcnt lgkmcnt(4)\n"
    "v_mfma_f32_16x16x32_bf16 v[160:163], v[208:211], v[0:3], v[192:195]\n"
    "v_exp_f32 v141, v141\n"
    "v_mfma_f32_16x16x32_bf16 v[164:167], v[212:215], v[0:3], v[192:195]\n"
    "v_exp_f32 v142, v142\n"
    "v_mfma_f32_16x16x32_bf16 v[172:175], v[212:215], v[32:35], v[196:199]\n"
    "s_add_u32 m0, s59, 0x700\n"
    "v_add_f32 v247, v141, v247\n"
    "v_mfma_f32_16x16x32_bf16 v[168:171], v[208:211], v[32:35], v[196:199]\n"
    "ds_read_b128 v[232:235], v241 offset:53248\n"
    "buffer_load_dwordx4 v245, s[44:47], s56 offen offset:256 nt lds\n"
    "v_mfma_f32_16x16x32_bf16 v[176:179], v[208:211], v[64:67], v[200:203]\n"
    "v_cvt_pk_bf16_f32 v138, v140, v141\n"
    "v_add_f32 v247, v142, v247\n"
    "v_mfma_f32_16x16x32_bf16 v[180:183], v[212:215], v[64:67], v[200:203]\n"
    "v_exp_f32 v143, v143\n"
    "v_mfma_f32_16x16x32_bf16 v[188:191], v[212:215], v[96:99], v[204:207]\n"
    "v_add_f32 v247, v143, v247\n"
    "v_cvt_pk_bf16_f32 v139, v142, v143\n"
    "v_mfma_f32_16x16x32_bf16 v[184:187], v[208:211], v[96:99], v[204:207]\n"
    "ds_read_b128 v[236:239], v241 offset:55296\n"
    "s_waitcnt lgkmcnt(4)\n"
    "v_mfma_f32_16x16x32_bf16 v[184:187], v[216:219], v[100:103], v[184:187]\n"
    "v_exp_f32 v144, v144\n"
    "v_mfma_f32_16x16x32_bf16 v[188:191], v[220:223], v[100:103], v[188:191]\n"
    "v_add_f32 v248, v144, v248\n"
    "v_mfma_f32_16x16x32_bf16 v[180:183], v[220:223], v[68:71], v[180:183]\n"
    "s_add_u32 m0, s59, 0xa80\n"
    "v_mfma_f32_16x16x32_bf16 v[176:179], v[216:219], v[68:71], v[176:179]\n"
    "ds_read_b128 v[208:211], v240 offset:57344\n"
    "buffer_load_dwordx4 v245, s[44:47], s56 offen offset:384 nt lds\n"
    "v_mfma_f32_16x16x32_bf16 v[168:171], v[216:219], v[36:39], v[168:171]\n"
    "v_exp_f32 v145, v145\n"
    "v_mfma_f32_16x16x32_bf16 v[172:175], v[220:223], v[36:39], v[172:175]\n"
    "v_add_f32 v248, v145, v248\n"
    "v_cvt_pk_bf16_f32 v144, v144, v145\n"
    "v_mfma_f32_16x16x32_bf16 v[164:167], v[220:223], v[4:7], v[164:167]\n"
    "v_exp_f32 v146, v146\n"
    "v_mfma_f32_16x16x32_bf16 v[160:163], v[216:219], v[4:7], v[160:163]\n"
    "ds_read_b128 v[212:215], v240 offset:59392\n"
    "s_waitcnt lgkmcnt(4)\n"
    "v_mfma_f32_16x16x32_bf16 v[160:163], v[224:227], v[8:11], v[160:163]\n"
    "v_exp_f32 v147, v147\n"
    "v_mfma_f32_16x16x32_bf16 v[164:167], v[228:231], v[8:11], v[164:167]\n"
    "v_add_f32 v248, v146, v248\n"
    "v_add_f32 v248, v147, v248\n"
    "v_mfma_f32_16x16x32_bf16 v[172:175], v[228:231], v[40:43], v[172:175]\n"
    "s_add_u32 m0, s58, 0x8000\n"
    "v_cvt_pk_bf16_f32 v145, v146, v147\n"
    "v_mfma_f32_16x16x32_bf16 v[168:171], v[224:227], v[40:43], v[168:171]\n"
    "ds_read_b128 v[216:219], v241 offset:57344\n"
    "buffer_load_dwordx4 v244, s[40:43], s56 offen offset:0 nt lds\n"
    "v_mfma_f32_16x16x32_bf16 v[176:179], v[224:227], v[72:75], v[176:179]\n"
    "v_exp_f32 v148, v148\n"
    "v_mfma_f32_16x16x32_bf16 v[180:183], v[228:231], v[72:75], v[180:183]\n"
    "v_add_f32 v248, v148, v248\n"
    "v_mfma_f32_16x16x32_bf16 v[188:191], v[228:231], v[104:107], v[188:191]\n"
    "v_exp_f32 v149, v149\n"
    "v_mfma_f32_16x16x32_bf16 v[184:187], v[224:227], v[104:107], v[184:187]\n"
    "ds_read_b128 v[220:223], v241 offset:59392\n"
    "s_waitcnt lgkmcnt(4)\n"
    "v_mfma_f32_16x16x32_bf16 v[184:187], v[232:235], v[108:111], v[184:187]\n"
    "v_add_f32 v248, v149, v248\n"
    "v_cvt_pk_bf16_f32 v146, v148, v149\n"
    "v_mfma_f32_16x16x32_bf16 v[188:191], v[236:239], v[108:111], v[188:191]\n"
    "v_exp_f32 v150, v150\n"
    "v_mfma_f32_16x16x32_bf16 v[180:183], v[236:239], v[76:79], v[180:183]\n"
    "s_add_u32 m0, s58, 0x8f80\n"
    "v_add_f32 v248, v150, v248\n"
    "v_mfma_f32_16x16x32_bf16 v[176:179], v[232:235], v[76:79], v[176:179]\n"
    "ds_read_b128 v[224:227], v240 offset:61440\n"
    "buffer_load_dwordx4 v244, s[40:43], s56 offen offset:128 nt lds\n"
    "v_mfma_f32_16x16x32_bf16 v[168:171], v[232:235], v[44:47], v[168:171]\n"
    "v_exp_f32 v151, v151\n"
    "v_mfma_f32_16x16x32_bf16 v[172:175], v[236:239], v[44:47], v[172:175]\n"
    "v_add_f32 v248, v151, v248\n"
    "v_cvt_pk_bf16_f32 v147, v150, v151\n"
    "v_mfma_f32_16x16x32_bf16 v[164:167], v[236:239], v[12:15], v[164:167]\n"
    "v_exp_f32 v152, v152\n"
    "v_mfma_f32_16x16x32_bf16 v[160:163], v[232:235], v[12:15], v[160:163]\n"
    "ds_read_b128 v[228:231], v240 offset:63488\n"
    "s_waitcnt lgkmcnt(4)\n"
    "v_mfma_f32_16x16x32_bf16 v[160:163], v[208:211], v[16:19], v[160:163]\n"
    "v_add_f32 v249, v152, v249\n"
    "v_mfma_f32_16x16x32_bf16 v[164:167], v[212:215], v[16:19], v[164:167]\n"
    "v_exp_f32 v153, v153\n"
    "v_mfma_f32_16x16x32_bf16 v[172:175], v[212:215], v[48:51], v[172:175]\n"
    "s_add_u32 m0, s58, 0x9f00\n"
    "v_add_f32 v249, v153, v249\n"
    "v_mfma_f32_16x16x32_bf16 v[168:171], v[208:211], v[48:51], v[168:171]\n"
    "ds_read_b128 v[232:235], v241 offset:61440\n"
    "buffer_load_dwordx4 v244, s[40:43], s56 offen offset:256 nt lds\n"
    "v_mfma_f32_16x16x32_bf16 v[176:179], v[208:211], v[80:83], v[176:179]\n"
    "v_cvt_pk_bf16_f32 v152, v152, v153\n"
    "v_mfma_f32_16x16x32_bf16 v[180:183], v[212:215], v[80:83], v[180:183]\n"
    "v_exp_f32 v154, v154\n"
    "v_mfma_f32_16x16x32_bf16 v[188:191], v[212:215], v[112:115], v[188:191]\n"
    "v_add_f32 v249, v154, v249\n"
    "v_mfma_f32_16x16x32_bf16 v[184:187], v[208:211], v[112:115], v[184:187]\n"
    "ds_read_b128 v[236:239], v241 offset:63488\n"
    "s_waitcnt lgkmcnt(4)\n"
    "v_mfma_f32_16x16x32_bf16 v[184:187], v[216:219], v[116:119], v[184:187]\n"
    "v_exp_f32 v155, v155\n"
    "v_mfma_f32_16x16x32_bf16 v[188:191], v[220:223], v[116:119], v[188:191]\n"
    "v_exp_f32 v156, v156\n"
    "v_mfma_f32_16x16x32_bf16 v[180:183], v[220:223], v[84:87], v[180:183]\n"
    "ds_read_b64_tr_b16 v[208:209], v242 offset:32768\n"
    "s_add_u32 m0, s58, 0xae80\n"
    "v_mfma_f32_16x16x32_bf16 v[176:179], v[216:219], v[84:87], v[176:179]\n"
    "ds_read_b64_tr_b16 v[210:211], v242 offset:40960\n"
    "buffer_load_dwordx4 v244, s[40:43], s56 offen offset:384 nt lds\n"
    "v_mfma_f32_16x16x32_bf16 v[168:171], v[216:219], v[52:55], v[168:171]\n"
    "v_add_f32 v249, v155, v249\n"
    "v_cvt_pk_bf16_f32 v153, v154, v155\n"
    "v_mfma_f32_16x16x32_bf16 v[172:175], v[220:223], v[52:55], v[172:175]\n"
    "v_exp_f32 v157, v157\n"
    "v_mfma_f32_16x16x32_bf16 v[164:167], v[220:223], v[20:23], v[164:167]\n"
    "ds_read_b64_tr_b16 v[212:213], v243 offset:32768\n"
    "v_add_f32 v249, v156, v249\n"
    "v_mfma_f32_16x16x32_bf16 v[160:163], v[216:219], v[20:23], v[160:163]\n"
    "ds_read_b64_tr_b16 v[214:215], v243 offset:40960\n"
    "s_waitcnt lgkmcnt(6)\n"
    "v_mfma_f32_16x16x32_bf16 v[160:163], v[224:227], v[24:27], v[160:163]\n"
    "v_add_f32 v249, v157, v249\n"
    "v_cvt_pk_bf16_f32 v154, v156, v157\n"
    "v_mfma_f32_16x16x32_bf16 v[164:167], v[228:231], v[24:27], v[164:167]\n"
    "v_exp_f32 v158, v158\n"
    "v_mfma_f32_16x16x32_bf16 v[172:175], v[228:231], v[56:59], v[172:175]\n"
    "ds_read_b64_tr_b16 v[216:217], v242 offset:33280\n"
    "s_add_u32 s56, s56, 0x4000\n"
    "v_mfma_f32_16x16x32_bf16 v[168:171], v[224:227], v[56:59], v[168:171]\n"
    "ds_read_b64_tr_b16 v[218:219], v242 offset:41472\n"
    "v_add_f32 v249, v158, v249\n"
    "v_mfma_f32_16x16x32_bf16 v[176:179], v[224:227], v[88:91], v[176:179]\n"
    "v_mfma_f32_16x16x32_bf16 v[180:183], v[228:231], v[88:91], v[180:183]\n"
    "v_exp_f32 v159, v159\n"
    "v_mfma_f32_16x16x32_bf16 v[188:191], v[228:231], v[120:123], v[188:191]\n"
    "ds_read_b64_tr_b16 v[220:221], v243 offset:33280\n"
    "v_add_f32 v249, v159, v249\n"
    "v_mfma_f32_16x16x32_bf16 v[184:187], v[224:227], v[120:123], v[184:187]\n"
    "ds_read_b64_tr_b16 v[222:223], v243 offset:41472\n"
    "s_waitcnt lgkmcnt(8)\n"
    "v_mfma_f32_16x16x32_bf16 v[184:187], v[232:235], v[124:127], v[184:187]\n"
    "v_cvt_pk_bf16_f32 v155, v158, v159\n"
    "v_mfma_f32_16x16x32_bf16 v[188:191], v[236:239], v[124:127], v[188:191]\n"
    "v_mfma_f32_16x16x32_bf16 v[180:183], v[236:239], v[92:95], v[180:183]\n"
    "ds_read_b64_tr_b16 v[224:225], v242 offset:33792\n"
    "v_mfma_f32_16x16x32_bf16 v[176:179], v[232:235], v[92:95], v[176:179]\n"
    "ds_read_b64_tr_b16 v[226:227], v242 offset:41984\n"
    "v_mfma_f32_16x16x32_bf16 v[168:171], v[232:235], v[60:63], v[168:171]\n"
    "v_mfma_f32_16x16x32_bf16 v[172:175], v[236:239], v[60:63], v[172:175]\n"
    "v_mfma_f32_16x16x32_bf16 v[164:167], v[236:239], v[28:31], v[164:167]\n"
    "ds_read_b64_tr_b16 v[228:229], v243 offset:33792\n"
    "v_mfma_f32_16x16x32_bf16 v[160:163], v[232:235], v[28:31], v[160:163]\n"
    "ds_read_b64_tr_b16 v[230:231], v243 offset:41984\n"
    "s_waitcnt lgkmcnt(8)\n"
    "v_mfma_f32_16x16x32_bf16 a[0:3], v[208:211], v[128:131], a[0:3]\n"
    "v_mfma_f32_16x16x32_bf16 a[16:19], v[212:215], v[128:131], a[16:19]\n"
    "v_mfma_f32_16x16x32_bf16 a[20:23], v[212:215], v[136:139], a[20:23]\n"
    "ds_read_b64_tr_b16 v[232:233], v242 offset:34304\n"
    "v_mfma_f32_16x16x32_bf16 a[4:7], v[208:211], v[136:139], a[4:7]\n"
    "ds_read_b64_tr_b16 v[234:235], v242 offset:42496\n"
    "v_mfma_f32_16x16x32_bf16 a[8:11], v[208:211], v[144:147], a[8:11]\n"
    "v_mfma_f32_16x16x32_bf16 a[24:27], v[212:215], v[144:147], a[24:27]\n"
    "v_max3_f32 v250, v160, v161, v162\n"
    "v_max3_f32 v250, v250, v163, v168\n"
    "v_mfma_f32_16x16x32_bf16 a[28:31], v[212:215], v[152:155], a[28:31]\n"
    "ds_read_b64_tr_b16 v[236:237], v243 offset:34304\n"
    "v_max3_f32 v250, v250, v169, v170\n"
    "v_mfma_f32_16x16x32_bf16 a[12:15], v[208:211], v[152:155], a[12:15]\n"
    "ds_read_b64_tr_b16 v[238:239], v243 offset:42496\n"
    "s_waitcnt lgkmcnt(8)\n"
    "v_mfma_f32_16x16x32_bf16 a[44:47], v[216:219], v[152:155], a[44:47]\n"
    "v_max3_f32 v250, v250, v171, v176\n"
    "v_max3_f32 v250, v250, v177, v178\n"
    "v_mfma_f32_16x16x32_bf16 a[60:63], v[220:223], v[152:155], a[60:63]\n"
    "v_max3_f32 v250, v250, v179, v184\n"
    "v_max3_f32 v250, v250, v185, v186\n"
    "v_mfma_f32_16x16x32_bf16 a[56:59], v[220:223], v[144:147], a[56:59]\n"
    "ds_read_b64_tr_b16 v[208:209], v242 offset:34816\n"
    "v_max3_f32 v250, v250, v187, v164\n"
    "v_mfma_f32_16x16x32_bf16 a[40:43], v[216:219], v[144:147], a[40:43]\n"
    "ds_read_b64_tr_b16 v[210:211], v242 offset:43008\n"
    "v_max3_f32 v250, v250, v165, v166\n"
    "v_mfma_f32_16x16x32_bf16 a[36:39], v[216:219], v[136:139], a[36:39]\n"
    "v_max3_f32 v250, v250, v167, v172\n"
    "v_max3_f32 v250, v250, v173, v174\n"
    "v_mfma_f32_16x16x32_bf16 a[52:55], v[220:223], v[136:139], a[52:55]\n"
    "v_max3_f32 v250, v250, v175, v180\n"
    "v_max3_f32 v250, v250, v181, v182\n"
    "v_mfma_f32_16x16x32_bf16 a[48:51], v[220:223], v[128:131], a[48:51]\n"
    "ds_read_b64_tr_b16 v[212:213], v243 offset:34816\n"
    "v_max3_f32 v250, v250, v183, v188\n"
    "v_mfma_f32_16x16x32_bf16 a[32:35], v[216:219], v[128:131], a[32:35]\n"
    "ds_read_b64_tr_b16 v[214:215], v243 offset:43008\n"
    "s_waitcnt lgkmcnt(8)\n"
    "v_mfma_f32_16x16x32_bf16 a[64:67], v[224:227], v[128:131], a[64:67]\n"
    "v_max3_f32 v250, v250, v189, v190\n"
    "v_max_f32 v250, v250, v191\n"
    "v_mfma_f32_16x16x32_bf16 a[80:83], v[228:231], v[128:131], a[80:83]\n"
    "v_cmp_lt_f32 vcc, 0x41000000, v250\n"
    "v_mfma_f32_16x16x32_bf16 a[84:87], v[228:231], v[136:139], a[84:87]\n"
    "ds_read_b64_tr_b16 v[216:217], v242 offset:35328\n"
    "v_mfma_f32_16x16x32_bf16 a[68:71], v[224:227], v[136:139], a[68:71]\n"
    "ds_read_b64_tr_b16 v[218:219], v242 offset:43520\n"
    "v_mfma_f32_16x16x32_bf16 a[72:75], v[224:227], v[144:147], a[72:75]\n"
    "s_cbranch_vccnz LS_st1_4_%=\n"
    "LS_st1_4_back_%=:\n"
    "v_mfma_f32_16x16x32_bf16 a[88:91], v[228:231], v[144:147], a[88:91]\n"
    "v_exp_f32 v160, v160\n"
    "v_mfma_f32_16x16x32_bf16 a[92:95], v[228:231], v[152:155], a[92:95]\n"
    "ds_read_b64_tr_b16 v[220:221], v243 offset:35328\n"
    "v_add_f32 v246, v160, v246\n"
    "v_mfma_f32_16x16x32_bf16 a[76:79], v[224:227], v[152:155], a[76:79]\n"
    "ds_read_b64_tr_b16 v[222:223], v243 offset:43520\n"
    "s_waitcnt lgkmcnt(8)\n"
    "v_mfma_f32_16x16x32_bf16 a[108:111], v[232:235], v[152:155], a[108:111]\n"
    "v_exp_f32 v161, v161\n"
    "v_mfma_f32_16x16x32_bf16 a[124:127], v[236:239], v[152:155], a[124:127]\n"
    "v_add_f32 v246, v161, v246\n"
    "v_cvt_pk_bf16_f32 v160, v160, v161\n"
    "v_mfma_f32_16x16x32_bf16 a[120:123], v[236:239], v[144:147], a[120:123]\n"
    "ds_read_b64_tr_b16 v[224:225], v242 offset:35840\n"
    "v_mfma_f32_16x16x32_bf16 a[104:107], v[232:235], v[144:147], a[104:107]\n"
    "ds_read_b64_tr_b16 v[226:227], v242 offset:44032\n"
    "v_mfma_f32_16x16x32_bf16 a[100:103], v[232:235], v[136:139], a[100:103]\n"
    "v_exp_f32 v162, v162\n"
    "v_mfma_f32_16x16x32_bf16 a[116:119], v[236:239], v[136:139], a[116:119]\n"
    "v_add_f32 v246, v162, v246\n"
    "v_mfma_f32_16x16x32_bf16 a[112:115], v[236:239], v[128:131], a[112:115]\n"
    "ds_read_b64_tr_b16 v[228:229], v243 offset:35840\n"
    "v_mfma_f32_16x16x32_bf16 a[96:99], v[232:235], v[128:131], a[96:99]\n"
    "ds_read_b64_tr_b16 v[230:231], v243 offset:44032\n"
    "s_waitcnt lgkmcnt(8)\n"
    "v_mfma_f32_16x16x32_bf16 a[128:131], v[208:211], v[128:131], a[128:131]\n"
    "v_exp_f32 v163, v163\n"
    "v_mfma_f32_16x16x32_bf16 a[144:147], v[212:215], v[128:131], a[144:147]\n"
    "v_add_f32 v246, v163, v246\n"
    "v_cvt_pk_bf16_f32 v161, v162, v163\n"
    "v_mfma_f32_16x16x32_bf16 a[148:151], v[212:215], v[136:139], a[148:151]\n"
    "ds_read_b64_tr_b16 v[232:233], v242 offset:36352\n"
    "v_mfma_f32_16x16x32_bf16 a[132:135], v[208:211], v[136:139], a[132:135]\n"
    "ds_read_b64_tr_b16 v[234:235], v242 offset:44544\n"
    "v_mfma_f32_16x16x32_bf16 a[136:139], v[208:211], v[144:147], a[136:139]\n"
    "v_exp_f32 v164, v164\n"
    "v_mfma_f32_16x16x32_bf16 a[152:155], v[212:215], v[144:147], a[152:155]\n"
    "v_exp_f32 v165, v165\n"
    "v_mfma_f32_16x16x32_bf16 a[156:159], v[212:215], v[152:155], a[156:159]\n"
    "ds_read_b64_tr_b16 v[236:237], v243 offset:36352\n"
    "v_add_f32 v246, v164, v246\n"
    "v_mfma_f32_16x16x32_bf16 a[140:143], v[208:211], v[152:155], a[140:143]\n"
    "ds_read_b64_tr_b16 v[238:239], v243 offset:44544\n"
    "s_waitcnt lgkmcnt(8)\n"
    "v_mfma_f32_16x16x32_bf16 a[172:175], v[216:219], v[152:155], a[172:175]\n"
    "v_add_f32 v246, v165, v246\n"
    "v_cvt_pk_bf16_f32 v162, v164, v165\n"
    "v_mfma_f32_16x16x32_bf16 a[188:191], v[220:223], v[152:155], a[188:191]\n"
    "v_exp_f32 v166, v166\n"
    "v_mfma_f32_16x16x32_bf16 a[184:187], v[220:223], v[144:147], a[184:187]\n"
    "s_waitcnt vmcnt(12)\n"
    "s_barrier\n"
    "v_mfma_f32_16x16x32_bf16 a[168:171], v[216:219], v[144:147], a[168:171]\n"
    "ds_read_b128 v[208:211], v240 offset:0\n"
    "v_add_f32 v246, v166, v246\n"
    "v_mfma_f32_16x16x32_bf16 a[164:167], v[216:219], v[136:139], a[164:167]\n"
    "v_mfma_f32_16x16x32_bf16 a[180:183], v[220:223], v[136:139], a[180:183]\n"
    "v_exp_f32 v167, v167\n"
    "v_mfma_f32_16x16x32_bf16 a[176:179], v[220:223], v[128:131], a[176:179]\n"
    "v_add_f32 v246, v167, v246\n"
    "v_cvt_pk_bf16_f32 v163, v166, v167\n"
    "v_mfma_f32_16x16x32_bf16 a[160:163], v[216:219], v[128:131], a[160:163]\n"
    "ds_read_b128 v[212:215], v240 offset:2048\n"
    "s_waitcnt lgkmcnt(6)\n"
    "v_mfma_f32_16x16x32_bf16 a[192:195], v[224:227], v[128:131], a[192:195]\n"
    "s_add_u32 m0, s59, 0x4000\n"
    "v_mfma_f32_16x16x32_bf16 a[208:211], v[228:231], v[128:131], a[208:211]\n"
    "buffer_load_dwordx4 v245, s[44:47], s56 offen offset:0 nt lds\n"
    "v_mfma_f32_16x16x32_bf16 a[212:215], v[228:231], v[136:139], a[212:215]\n"
    "v_exp_f32 v168, v168\n"
    "v_mfma_f32_16x16x32_bf16 a[196:199], v[224:227], v[136:139], a[196:199]\n"
    "ds_read_b128 v[216:219], v241 offset:0\n"
    "v_add_f32 v247, v168, v247\n"
    "v_mfma_f32_16x16x32_bf16 a[200:203], v[224:227], v[144:147], a[200:203]\n"
    "v_mfma_f32_16x16x32_bf16 a[216:219], v[228:231], v[144:147], a[216:219]\n"
    "v_exp_f32 v169, v169\n"
    "v_mfma_f32_16x16x32_bf16 a[220:223], v[228:231], v[152:155], a[220:223]\n"
    "v_exp_f32 v170, v170\n"
    "v_mfma_f32_16x16x32_bf16 a[204:207], v[224:227], v[152:155], a[204:207]\n"
    "ds_read_b128 v[220:223], v241 offset:2048\n"
    "s_waitcnt lgkmcnt(4)\n"
    "v_mfma_f32_16x16x32_bf16 a[236:239], v[232:235], v[152:155], a[236:239]\n"
    "v_add_f32 v247, v169, v247\n"
    "v_cvt_pk_bf16_f32 v168, v168, v169\n"
    "v_mfma_f32_16x16x32_bf16 a[252:255], v[236:239], v[152:155], a[252:255]\n"
    "v_exp_f32 v171, v171\n"
    "v_mfma_f32_16x16x32_bf16 a[248:251], v[236:239], v[144:147], a[248:251]\n"
    "s_add_u32 m0, s59, 0x4380\n"
    "v_add_f32 v247, v170, v247\n"
    "v_mfma_f32_16x16x32_bf16 a[232:235], v[232:235], v[144:147], a[232:235]\n"
    "ds_read_b128 v[224:227], v240 offset:4096\n"
    "buffer_load_dwordx4 v245, s[44:47], s56 offen offset:128 nt lds\n"
    "v_mfma_f32_16x16x32_bf16 a[228:231], v[232:235], v[136:139], a[228:231]\n"
    "v_add_f32 v247, v171, v247\n"
    "v_cvt_pk_bf16_f32 v169, v170, v171\n"
    "v_mfma_f32_16x16x32_bf16 a[244:247], v[236:239], v[136:139], a[244:247]\n"
    "v_exp_f32 v172, v172\n"
    "v_mfma_f32_16x16x32_bf16 a[240:243], v[236:239], v[128:131], a[240:243]\n"
    "v_add_f32 v247, v172, v247\n"
    "v_mfma_f32_16x16x32_bf16 a[224:227], v[232:235], v[128:131], a[224:227]\n"
    "s_cbranch_vccnz LS_st2_5_%=\n"
    "LS_st2_5_back_%=:\n"
    "ds_read_b128 v[228:231], v240 offset:6144\n"
    "s_waitcnt lgkmcnt(4)\n"
    "s_sub_u32 s60, s60, 1\n"
    "s_cbranch_scc1 LS_tail_%=\n"
    "v_mfma_f32_16x16x32_bf16 v[128:131], v[208:211], v[0:3], v[192:195]\n"
    "v_exp_f32 v173, v173\n"
    "v_mfma_f32_16x16x32_bf16 v[132:135], v[212:215], v[0:3], v[192:195]\n"
    "v_exp_f32 v174, v174\n"
    "v_mfma_f32_16x16x32_bf16 v[140:143], v[212:215], v[32:35], v[196:199]\n"
    "s_add_u32 m0, s59, 0x4700\n"
    "v_add_f32 v247, v173, v247\n"
    "v_mfma_f32_16x16x32_bf16 v[136:139], v[208:211], v[32:35], v[196:199]\n"
    "ds_read_b128 v[232:235], v241 offset:4096\n"
    "buffer_load_dwordx4 v245, s[44:47], s56 offen offset:256 nt lds\n"
    "v_mfma_f32_16x16x32_bf16 v[144:147], v[208:211], v[64:67], v[200:203]\n"
    "v_cvt_pk_bf16_f32 v170, v172, v173\n"
    "v_add_f32 v247, v174, v247\n"
    "v_mfma_f32_16x16x32_bf16 v[148:151], v[212:215], v[64:67], v[200:203]\n"
    "v_exp_f32 v175, v175\n"
    "v_mfma_f32_16x16x32_bf16 v[156:159], v[212:215], v[96:99], v[204:207]\n"
    "v_add_f32 v247, v175, v247\n"
    "v_cvt_pk_bf16_f32 v171, v174, v175\n"
    "v_mfma_f32_16x16x32_bf16 v[152:155], v[208:211], v[96:99], v[204:207]\n"
    "ds_read_b128 v[236:239], v241 offset:6144\n"
    "s_waitcnt lgkmcnt(4)\n"
    "v_mfma_f32_16x16x32_bf16 v[152:155], v[216:219], v[100:103], v[152:155]\n"
    "v_exp_f32 v176, v176\n"
    "v_mfma_f32_16x16x32_bf16 v[156:159], v[220:223], v[100:103], v[156:159]\n"
    "v_add_f32 v248, v176, v248\n"
    "v_mfma_f32_16x16x32_bf16 v[148:151], v[220:223], v[68:71], v[148:151]\n"
    "s_add_u32 m0, s59, 0x4a80\n"
    "v_mfma_f32_16x16x32_bf16 v[144:147], v[216:219], v[68:71], v[144:147]\n"
    "ds_read_b128 v[208:211], v240 offset:8192\n"
    "buffer_load_dwordx4 v245, s[44:47], s56 offen offset:384 nt lds\n"
    "v_mfma_f32_16x16x32_bf16 v[136:139], v[216:219], v[36:39], v[136:139]\n"
    "v_exp_f32 v177, v177\n"
    "v_mfma_f32_16x16x32_bf16 v[140:143], v[220:223], v[36:39], v[140:143]\n"
    "v_add_f32 v248, v177, v248\n"
    "v_cvt_pk_bf16_f32 v176, v176, v177\n"
    "v_mfma_f32_16x16x32_bf16 v[132:135], v[220:223], v[4:7], v[132:135]\n"
    "v_exp_f32 v178, v178\n"
    "v_mfma_f32_16x16x32_bf16 v[128:131], v[216:219], v[4:7], v[128:131]\n"
    "ds_read_b128 v[212:215], v240 offset:10240\n"
    "s_waitcnt lgkmcnt(4)\n"
    "v_mfma_f32_16x16x32_bf16 v[128:131], v[224:227], v[8:11], v[128:131]\n"
    "v_exp_f32 v179, v179\n"
    "v_mfma_f32_16x16x32_bf16 v[132:135], v[228:231], v[8:11], v[132:135]\n"
    "v_add_f32 v248, v178, v248\n"
    "v_add_f32 v248, v179, v248\n"
    "v_mfma_f32_16x16x32_bf16 v[140:143], v[228:231], v[40:43], v[140:143]\n"
    "s_add_u32 m0, s58, 0xc000\n"
    "v_cvt_pk_bf16_f32 v177, v178, v179\n"
    "v_mfma_f32_16x16x32_bf16 v[136:139], v[224:227], v[40:43], v[136:139]\n"
    "ds_read_b128 v[216:219], v241 offset:8192\n"
    "buffer_load_dwordx4 v244, s[40:43], s56 offen offset:0 nt lds\n"
    "v_mfma_f32_16x16x32_bf16 v[144:147], v[224:227], v[72:75], v[144:147]\n"
    "v_exp_f32 v180, v180\n"
    "v_mfma_f32_16x16x32_bf16 v[148:151], v[228:231], v[72:75], v[148:151]\n"
    "v_add_f32 v248, v180, v248\n"
    "v_mfma_f32_16x16x32_bf16 v[156:159], v[228:231], v[104:107], v[156:159]\n"
    "v_exp_f32 v181, v181\n"
    "v_mfma_f32_16x16x32_bf16 v[152:155], v[224:227], v[104:107], v[152:155]\n"
    "ds_read_b128 v[220:223], v241 offset:10240\n"
    "s_waitcnt lgkmcnt(4)\n"
    "v_mfma_f32_16x16x32_bf16 v[152:155], v[232:235], v[108:111], v[152:155]\n"
    "v_add_f32 v248, v181, v248\n"
    "v_cvt_pk_bf16_f32 v178, v180, v181\n"
    "v_mfma_f32_16x16x32_bf16 v[156:159], v[236:239], v[108:111], v[156:159]\n"
    "v_exp_f32 v182, v182\n"
    "v_mfma_f32_16x16x32_bf16 v[148:151], v[236:239], v[76:79], v[148:151]\n"
    "s_add_u32 m0, s58, 0xcf80\n"
    "v_add_f32 v248, v182, v248\n"
    "v_mfma_f32_16x16x32_bf16 v[144:147], v[232:235], v[76:79], v[144:147]\n"
    "ds_read_b128 v[224:227], v240 offset:12288\n"
    "buffer_load_dwordx4 v244, s[40:43], s56 offen offset:128 nt lds\n"
    "v_mfma_f32_16x16x32_bf16 v[136:139], v[232:235], v[44:47], v[136:139]\n"
    "v_exp_f32 v183, v183\n"
    "v_mfma_f32_16x16x32_bf16 v[140:143], v[236:239], v[44:47], v[140:143]\n"
    "v_add_f32 v248, v183, v248\n"
    "v_cvt_pk_bf16_f32 v179, v182, v183\n"
    "v_mfma_f32_16x16x32_bf16 v[132:135], v[236:239], v[12:15], v[132:135]\n"
    "v_exp_f32 v184, v184\n"
    "v_mfma_f32_16x16x32_bf16 v[128:131], v[232:235], v[12:15], v[128:131]\n"
    "ds_read_b128 v[228:231], v240 offset:14336\n"
    "s_waitcnt lgkmcnt(4)\n"
    "v_mfma_f32_16x16x32_bf16 v[128:131], v[208:211], v[16:19], v[128:131]\n"
    "v_add_f32 v249, v184, v249\n"
    "v_mfma_f32_16x16x32_bf16 v[132:135], v[212:215], v[16:19], v[132:135]\n"
    "v_exp_f32 v185, v185\n"
    "v_mfma_f32_16x16x32_bf16 v[140:143], v[212:215], v[48:51], v[140:143]\n"
    "s_add_u32 m0, s58, 0xdf00\n"
    "v_add_f32 v249, v185, v249\n"
    "v_mfma_f32_16x16x32_bf16 v[136:139], v[208:211], v[48:51], v[136:139]\n"
    "ds_read_b128 v[232:235], v241 offset:12288\n"
    "buffer_load_dwordx4 v244, s[40:43], s56 offen offset:256 nt lds\n"
    "v_mfma_f32_16x16x32_bf16 v[144:147], v[208:211], v[80:83], v[144:147]\n"
    "v_cvt_pk_bf16_f32 v184, v184, v185\n"
    "v_mfma_f32_16x16x32_bf16 v[148:151], v[212:215], v[80:83], v[148:151]\n"
    "v_exp_f32 v186, v186\n"
    "v_mfma_f32_16x16x32_bf16 v[156:159], v[212:215], v[112:115], v[156:159]\n"
    "v_add_f32 v249, v186, v249\n"
    "v_mfma_f32_16x16x32_bf16 v[152:155], v[208:211], v[112:115], v[152:155]\n"
    "ds_read_b128 v[236:239], v241 offset:14336\n"
    "s_waitcnt lgkmcnt(4)\n"
    "v_mfma_f32_16x16x32_bf16 v[152:155], v[216:219], v[116:119], v[152:155]\n"
    "v_exp_f32 v187, v187\n"
    "v_mfma_f32_16x16x32_bf16 v[156:159], v[220:223], v[116:119], v[156:159]\n"
    "v_exp_f32 v188, v188\n"
    "v_mfma_f32_16x16x32_bf16 v[148:151], v[220:223], v[84:87], v[148:151]\n"
    "ds_read_b64_tr_b16 v[208:209], v242 offset:49152\n"
    "s_add_u32 m0, s58, 0xee80\n"
    "v_mfma_f32_16x16x32_bf16 v[144:147], v[216:219], v[84:87], v[144:147]\n"
    "ds_read_b64_tr_b16 v[210:211], v242 offset:57344\n"
    "buffer_load_dwordx4 v244, s[40:43], s56 offen offset:384 nt lds\n"
    "v_mfma_f32_16x16x32_bf16 v[136:139], v[216:219], v[52:55], v[136:139]\n"
    "v_add_f32 v249, v187, v249\n"
    "v_cvt_pk_bf16_f32 v185, v186, v187\n"
    "v_mfma_f32_16x16x32_bf16 v[140:143], v[220:223], v[52:55], v[140:143]\n"
    "v_exp_f32 v189, v189\n"
    "v_mfma_f32_16x16x32_bf16 v[132:135], v[220:223], v[20:23], v[132:135]\n"
    "ds_read_b64_tr_b16 v[212:213], v243 offset:49152\n"
    "v_add_f32 v249, v188, v249\n"
    "v_mfma_f32_16x16x32_bf16 v[128:131], v[216:219], v[20:23], v[128:131]\n"
    "ds_read_b64_tr_b16 v[214:215], v243 offset:57344\n"
    "s_waitcnt lgkmcnt(6)\n"
    "v_mfma_f32_16x16x32_bf16 v[128:131], v[224:227], v[24:27], v[128:131]\n"
    "v_add_f32 v249, v189, v249\n"
    "v_cvt_pk_bf16_f32 v186, v188, v189\n"
    "v_mfma_f32_16x16x32_bf16 v[132:135], v[228:231], v[24:27], v[132:135]\n"
    "v_exp_f32 v190, v190\n"
    "v_mfma_f32_16x16x32_bf16 v[140:143], v[228:231], v[56:59], v[140:143]\n"
    "ds_read_b64_tr_b16 v[216:217], v242 offset:49664\n"
    "s_add_u32 s56, s56, 0x4000\n"
    "v_mfma_f32_16x16x32_bf16 v[136:139], v[224:227], v[56:59], v[136:139]\n"
    "ds_read_b64_tr_b16 v[218:219], v242 offset:57856\n"
    "v_add_f32 v249, v190, v249\n"
    "v_mfma_f32_16x16x32_bf16 v[144:147], v[224:227], v[88:91], v[144:147]\n"
    "v_mfma_f32_16x16x32_bf16 v[148:151], v[228:231], v[88:91], v[148:151]\n"
    "v_exp_f32 v191, v191\n"
    "v_mfma_f32_16x16x32_bf16 v[156:159], v[228:231], v[120:123], v[156:159]\n"
    "ds_read_b64_tr_b16 v[220:221], v243 offset:49664\n"
    "v_add_f32 v249, v191, v249\n"
    "v_mfma_f32_16x16x32_bf16 v[152:155], v[224:227], v[120:123], v[152:155]\n"
    "ds_read_b64_tr_b16 v[222:223], v243 offset:57856\n"
    "s_waitcnt lgkmcnt(8)\n"
    "v_mfma_f32_16x16x32_bf16 v[152:155], v[232:235], v[124:127], v[152:155]\n"
    "v_cvt_pk_bf16_f32 v187, v190, v191\n"
    "v_mfma_f32_16x16x32_bf16 v[156:159], v[236:239], v[124:127], v[156:159]\n"
    "v_mfma_f32_16x16x32_bf16 v[148:151], v[236:239], v[92:95], v[148:151]\n"
    "ds_read_b64_tr_b16 v[224:225], v242 offset:50176\n"
    "v_mfma_f32_16x16x32_bf16 v[144:147], v[232:235], v[92:95], v[144:147]\n"
    "ds_read_b64_tr_b16 v[226:227], v242 offset:58368\n"
    "v_mfma_f32_16x16x32_bf16 v[136:139], v[232:235], v[60:63], v[136:139]\n"
    "v_mfma_f32_16x16x32_bf16 v[140:143], v[236:239], v[60:63], v[140:143]\n"
    "v_mfma_f32_16x16x32_bf16 v[132:135], v[236:239], v[28:31], v[132:135]\n"
    "ds_read_b64_tr_b16 v[228:229], v243 offset:50176\n"
    "v_mfma_f32_16x16x32_bf16 v[128:131], v[232:235], v[28:31], v[128:131]\n"
    "ds_read_b64_tr_b16 v[230:231], v243 offset:58368\n"
    "s_waitcnt lgkmcnt(8)\n"
    "v_mfma_f32_16x16x32_bf16 a[0:3], v[208:211], v[160:163], a[0:3]\n"
    "v_mfma_f32_16x16x32_bf16 a[16:19], v[212:215], v[160:163], a[16:19]\n"
    "v_mfma_f32_16x16x32_bf16 a[20:23], v[212:215], v[168:171], a[20:23]\n"
    "ds_read_b64_tr_b16 v[232:233], v242 offset:50688\n"
    "v_mfma_f32_16x16x32_bf16 a[4:7], v[208:211], v[168:171], a[4:7]\n"
    "ds_read_b64_tr_b16 v[234:235], v242 offset:58880\n"
    "v_mfma_f32_16x16x32_bf16 a[8:11], v[208:211], v[176:179], a[8:11]\n"
    "v_mfma_f32_16x16x32_bf16 a[24:27], v[212:215], v[176:179], a[24:27]\n"
    "v_max3_f32 v250, v128, v129, v130\n"
    "v_max3_f32 v250, v250, v131, v136\n"
    "v_mfma_f32_16x16x32_bf16 a[28:31], v[212:215], v[184:187], a[28:31]\n"
    "ds_read_b64_tr_b16 v[236:237], v243 offset:50688\n"
    "v_max3_f32 v250, v250, v137, v138\n"
    "v_mfma_f32_16x16x32_bf16 a[12:15], v[208:211], v[184:187], a[12:15]\n"
    "ds_read_b64_tr_b16 v[238:239], v243 offset:58880\n"
    "s_waitcnt lgkmcnt(8)\n"
    "v_mfma_f32_16x16x32_bf16 a[44:47], v[216:219], v[184:187], a[44:47]\n"
    "v_max3_f32 v250, v250, v139, v144\n"
    "v_max3_f32 v250, v250, v145, v146\n"
    "v_mfma_f32_16x16x32_bf16 a[60:63], v[220:223], v[184:187], a[60:63]\n"
    "v_max3_f32 v250, v250, v147, v152\n"
    "v_max3_f32 v250, v250, v153, v154\n"
    "v_mfma_f32_16x16x32_bf16 a[56:59], v[220:223], v[176:179], a[56:59]\n"
    "ds_read_b64_tr_b16 v[208:209], v242 offset:51200\n"
    "v_max3_f32 v250, v250, v155, v132\n"
    "v_mfma_f32_16x16x32_bf16 a[40:43], v[216:219], v[176:179], a[40:43]\n"
    "ds_read_b64_tr_b16 v[210:211], v242 offset:59392\n"
    "v_max3_f32 v250, v250, v133, v134\n"
    "v_mfma_f32_16x16x32_bf16 a[36:39], v[216:219], v[168:171], a[36:39]\n"
    "v_max3_f32 v250, v250, v135, v140\n"
    "v_max3_f32 v250, v250, v141, v142\n"
    "v_mfma_f32_16x16x32_bf16 a[52:55], v[220:223], v[168:171], a[52:55]\n"
    "v_max3_f32 v250, v250, v143, v148\n"
    "v_max3_f32 v250, v250, v149, v150\n"
    "v_mfma_f32_16x16x32_bf16 a[48:51], v[220:223], v[160:163], a[48:51]\n"
    "ds_read_b64_tr_b16 v[212:213], v243 offset:51200\n"
    "v_max3_f32 v250, v250, v151, v156\n"
    "v_mfma_f32_16x16x32_bf16 a[32:35], v[216:219], v[160:163], a[32:35]\n"
    "ds_read_b64_tr_b16 v[214:215], v243 offset:59392\n"
    "s_waitcnt lgkmcnt(8)\n"
    "v_mfma_f32_16x16x32_bf16 a[64:67], v[224:227], v[160:163], a[64:67]\n"
    "v_max3_f32 v250, v250, v157, v158\n"
    "v_max_f32 v250, v250, v159\n"
    "v_mfma_f32_16x16x32_bf16 a[80:83], v[228:231], v[160:163], a[80:83]\n"
    "v_cmp_lt_f32 vcc, 0x41000000, v250\n"
    "v_mfma_f32_16x16x32_bf16 a[84:87], v[228:231], v[168:171], a[84:87]\n"
    "ds_read_b64_tr_b16 v[216:217], v242 offset:51712\n"
    "v_mfma_f32_16x16x32_bf16 a[68:71], v[224:227], v[168:171], a[68:71]\n"
    "ds_read_b64_tr_b16 v[218:219], v242 offset:59904\n"
    "v_mfma_f32_16x16x32_bf16 a[72:75], v[224:227], v[176:179], a[72:75]\n"
    "s_cbranch_vccnz LS_st1_6_%=\n"
    "LS_st1_6_back_%=:\n"
    "v_mfma_f32_16x16x32_bf16 a[88:91], v[228:231], v[176:179], a[88:91]\n"
    "v_exp_f32 v128, v128\n"
    "v_mfma_f32_16x16x32_bf16 a[92:95], v[228:231], v[184:187], a[92:95]\n"
    "ds_read_b64_tr_b16 v[220:221], v243 offset:51712\n"
    "v_add_f32 v246, v128, v246\n"
    "v_mfma_f32_16x16x32_bf16 a[76:79], v[224:227], v[184:187], a[76:79]\n"
    "ds_read_b64_tr_b16 v[222:223], v243 offset:59904\n"
    "s_waitcnt lgkmcnt(8)\n"
    "v_mfma_f32_16x16x32_bf16 a[108:111], v[232:235], v[184:187], a[108:111]\n"
    "v_exp_f32 v129, v129\n"
    "v_mfma_f32_16x16x32_bf16 a[124:127], v[236:239], v[184:187], a[124:127]\n"
    "v_add_f32 v246, v129, v246\n"
    "v_cvt_pk_bf16_f32 v128, v128, v129\n"
    "v_mfma_f32_16x16x32_bf16 a[120:123], v[236:239], v[176:179], a[120:123]\n"
    "ds_read_b64_tr_b16 v[224:225], v242 offset:52224\n"
    "v_mfma_f32_16x16x32_bf16 a[104:107], v[232:235], v[176:179], a[104:107]\n"
    "ds_read_b64_tr_b16 v[226:227], v242 offset:60416\n"
    "v_mfma_f32_16x16x32_bf16 a[100:103], v[232:235], v[168:171], a[100:103]\n"
    "v_exp_f32 v130, v130\n"
    "v_mfma_f32_16x16x32_bf16 a[116:119], v[236:239], v[168:171], a[116:119]\n"
    "v_add_f32 v246, v130, v246\n"
    "v_mfma_f32_16x16x32_bf16 a[112:115], v[236:239], v[160:163], a[112:115]\n"
    "ds_read_b64_tr_b16 v[228:229], v243 offset:52224\n"
    "v_mfma_f32_16x16x32_bf16 a[96:99], v[232:235], v[160:163], a[96:99]\n"
    "ds_read_b64_tr_b16 v[230:231], v243 offset:60416\n"
    "s_waitcnt lgkmcnt(8)\n"
    "v_mfma_f32_16x16x32_bf16 a[128:131], v[208:211], v[160:163], a[128:131]\n"
    "v_exp_f32 v131, v131\n"
    "v_mfma_f32_16x16x32_bf16 a[144:147], v[212:215], v[160:163], a[144:147]\n"
    "v_add_f32 v246, v131, v246\n"
    "v_cvt_pk_bf16_f32 v129, v130, v131\n"
    "v_mfma_f32_16x16x32_bf16 a[148:151], v[212:215], v[168:171], a[148:151]\n"
    "ds_read_b64_tr_b16 v[232:233], v242 offset:52736\n"
    "v_mfma_f32_16x16x32_bf16 a[132:135], v[208:211], v[168:171], a[132:135]\n"
    "ds_read_b64_tr_b16 v[234:235], v242 offset:60928\n"
    "v_mfma_f32_16x16x32_bf16 a[136:139], v[208:211], v[176:179], a[136:139]\n"
    "v_exp_f32 v132, v132\n"
    "v_mfma_f32_16x16x32_bf16 a[152:155], v[212:215], v[176:179], a[152:155]\n"
    "v_exp_f32 v133, v133\n"
    "v_mfma_f32_16x16x32_bf16 a[156:159], v[212:215], v[184:187], a[156:159]\n"
    "ds_read_b64_tr_b16 v[236:237], v243 offset:52736\n"
    "v_add_f32 v246, v132, v246\n"
    "v_mfma_f32_16x16x32_bf16 a[140:143], v[208:211], v[184:187], a[140:143]\n"
    "ds_read_b64_tr_b16 v[238:239], v243 offset:60928\n"
    "s_waitcnt lgkmcnt(8)\n"
    "v_mfma_f32_16x16x32_bf16 a[172:175], v[216:219], v[184:187], a[172:175]\n"
    "v_add_f32 v246, v133, v246\n"
    "v_cvt_pk_bf16_f32 v130, v132, v133\n"
    "v_mfma_f32_16x16x32_bf16 a[188:191], v[220:223], v[184:187], a[188:191]\n"
    "v_exp_f32 v134, v134\n"
    "v_mfma_f32_16x16x32_bf16 a[184:187], v[220:223], v[176:179], a[184:187]\n"
    "s_waitcnt vmcnt(12)\n"
    "s_barrier\n"
    "v_mfma_f32_16x16x32_bf16 a[168:171], v[216:219], v[176:179], a[168:171]\n"
    "ds_read_b128 v[208:211], v240 offset:16384\n"
    "v_add_f32 v246, v134, v246\n"
    "v_mfma_f32_16x16x32_bf16 a[164:167], v[216:219], v[168:171], a[164:167]\n"
    "v_mfma_f32_16x16x32_bf16 a[180:183], v[220:223], v[168:171], a[180:183]\n"
    "v_exp_f32 v135, v135\n"
    "v_mfma_f32_16x16x32_bf16 a[176:179], v[220:223], v[160:163], a[176:179]\n"
    "v_add_f32 v246, v135, v246\n"
    "v_cvt_pk_bf16_f32 v131, v134, v135\n"
    "v_mfma_f32_16x16x32_bf16 a[160:163], v[216:219], v[160:163], a[160:163]\n"
    "ds_read_b128 v[212:215], v240 offset:18432\n"
    "s_waitcnt lgkmcnt(6)\n"
    "v_mfma_f32_16x16x32_bf16 a[192:195], v[224:227], v[160:163], a[192:195]\n"
    "s_add_u32 m0, s59, 0x8000\n"
    "v_mfma_f32_16x16x32_bf16 a[208:211], v[228:231], v[160:163], a[208:211]\n"
    "buffer_load_dwordx4 v245, s[44:47], s56 offen offset:0 nt lds\n"
    "v_mfma_f32_16x16x32_bf16 a[212:215], v[228:231], v[168:171], a[212:215]\n"
    "v_exp_f32 v136, v136\n"
    "v_mfma_f32_16x16x32_bf16 a[196:199], v[224:227], v[168:171], a[196:199]\n"
    "ds_read_b128 v[216:219], v241 offset:16384\n"
    "v_add_f32 v247, v136, v247\n"
    "v_mfma_f32_16x16x32_bf16 a[200:203], v[224:227], v[176:179], a[200:203]\n"
    "v_mfma_f32_16x16x32_bf16 a[216:219], v[228:231], v[176:179], a[216:219]\n"
    "v_exp_f32 v137, v137\n"
    "v_mfma_f32_16x16x32_bf16 a[220:223], v[228:231], v[184:187], a[220:223]\n"
    "v_exp_f32 v138, v138\n"
    "v_mfma_f32_16x16x32_bf16 a[204:207], v[224:227], v[184:187], a[204:207]\n"
    "ds_read_b128 v[220:223], v241 offset:18432\n"
    "s_waitcnt lgkmcnt(4)\n"
    "v_mfma_f32_16x16x32_bf16 a[236:239], v[232:235], v[184:187], a[236:239]\n"
    "v_add_f32 v247, v137, v247\n"
    "v_cvt_pk_bf16_f32 v136, v136, v137\n"
    "v_mfma_f32_16x16x32_bf16 a[252:255], v[236:239], v[184:187], a[252:255]\n"
    "v_exp_f32 v139, v139\n"
    "v_mfma_f32_16x16x32_bf16 a[248:251], v[236:239], v[176:179], a[248:251]\n"
    "s_add_u32 m0, s59, 0x8380\n"
    "v_add_f32 v247, v138, v247\n"
    "v_mfma_f32_16x16x32_bf16 a[232:235], v[232:235], v[176:179], a[232:235]\n"
    "ds_read_b128 v[224:227], v240 offset:20480\n"
    "buffer_load_dwordx4 v245, s[44:47], s56 offen offset:128 nt lds\n"
    "v_mfma_f32_16x16x32_bf16 a[228:231], v[232:235], v[168:171], a[228:231]\n"
    "v_add_f32 v247, v139, v247\n"
    "v_cvt_pk_bf16_f32 v137, v138, v139\n"
    "v_mfma_f32_16x16x32_bf16 a[244:247], v[236:239], v[168:171], a[244:247]\n"
    "v_exp_f32 v140, v140\n"
    "v_mfma_f32_16x16x32_bf16 a[240:243], v[236:239], v[160:163], a[240:243]\n"
    "v_add_f32 v247, v140, v247\n"
    "v_mfma_f32_16x16x32_bf16 a[224:227], v[232:235], v[160:163], a[224:227]\n"
    "s_cbranch_vccnz LS_st2_7_%=\n"
    "LS_st2_7_back_%=:\n"
    "ds_read_b128 v[228:231], v240 offset:22528\n"
    "s_waitcnt lgkmcnt(4)\n"
    "s_branch LS_loop_%=\n"
    "LS_tail_%=:\n"
    "v_exp_f32 v173, v173\n"
    "v_exp_f32 v174, v174\n"
    "v_add_f32 v247, v173, v247\n"
    "v_cvt_pk_bf16_f32 v170, v172, v173\n"
    "v_add_f32 v247, v174, v247\n"
    "v_exp_f32 v175, v175\n"
    "s_nop 0\n"
    "v_add_f32 v247, v175, v247\n"
    "v_cvt_pk_bf16_f32 v171, v174, v175\n"
    "v_exp_f32 v176, v176\n"
    "s_nop 0\n"
    "v_add_f32 v248, v176, v248\n"
    "v_exp_f32 v177, v177\n"
    "s_nop 0\n"
    "v_add_f32 v248, v177, v248\n"
    "v_cvt_pk_bf16_f32 v176, v176, v177\n"
    "v_exp_f32 v178, v178\n"
    "v_exp_f32 v179, v179\n"
    "v_add_f32 v248, v178, v248\n"
    "v_add_f32 v248, v179, v248\n"
    "v_cvt_pk_bf16_f32 v177, v178, v179\n"
    "v_exp_f32 v180, v180\n"
    "s_nop 0\n"
    "v_add_f32 v248, v180, v248\n"
    "v_exp_f32 v181, v181\n"
    "s_nop 0\n"
    "v_add_f32 v248, v181, v248\n"
    "v_cvt_pk_bf16_f32 v178, v180, v181\n"
    "v_exp_f32 v182, v182\n"
    "s_nop 0\n"
    "v_add_f32 v248, v182, v248\n"
    "v_exp_f32 v183, v183\n"
    "s_nop 0\n"
    "v_add_f32 v248, v183, v248\n"
    "v_cvt_pk_bf16_f32 v179, v182, v183\n"
    "v_exp_f32 v184, v184\n"
    "s_nop 0\n"
    "v_add_f32 v249, v184, v249\n"
    "v_exp_f32 v185, v185\n"
    "s_nop 0\n"
    "v_add_f32 v249, v185, v249\n"
    "v_cvt_pk_bf16_f32 v184, v184, v185\n"
    "v_exp_f32 v186, v186\n"
    "s_nop 0\n"
    "v_add_f32 v249, v186, v249\n"
    "v_exp_f32 v187, v187\n"
    "v_exp_f32 v188, v188\n"
    "ds_read_b64_tr_b16 v[208:209], v242 offset:49152\n"
    "ds_read_b64_tr_b16 v[210:211], v242 offset:57344\n"
    "v_add_f32 v249, v187, v249\n"
    "v_cvt_pk_bf16_f32 v185, v186, v187\n"
    "v_exp_f32 v189, v189\n"
    "ds_read_b64_tr_b16 v[212:213], v243 offset:49152\n"
    "v_add_f32 v249, v188, v249\n"
    "ds_read_b64_tr_b16 v[214:215], v243 offset:57344\n"
    "v_add_f32 v249, v189, v249\n"
    "v_cvt_pk_bf16_f32 v186, v188, v189\n"
    "v_exp_f32 v190, v190\n"
    "ds_read_b64_tr_b16 v[216:217], v242 offset:49664\n"
    "ds_read_b64_tr_b16 v[218:219], v242 offset:57856\n"
    "v_add_f32 v249, v190, v249\n"
    "v_exp_f32 v191, v191\n"
    "ds_read_b64_tr_b16 v[220:221], v243 offset:49664\n"
    "v_add_f32 v249, v191, v249\n"
    "ds_read_b64_tr_b16 v[222:223], v243 offset:57856\n"
    "v_cvt_pk_bf16_f32 v187, v190, v191\n"
    "ds_read_b64_tr_b16 v[224:225], v242 offset:50176\n"
    "ds_read_b64_tr_b16 v[226:227], v242 offset:58368\n"
    "ds_read_b64_tr_b16 v[228:229], v243 offset:50176\n"
    "ds_read_b64_tr_b16 v[230:231], v243 offset:58368\n"
    "s_waitcnt lgkmcnt(8)\n"
    "v_mfma_f32_16x16x32_bf16 a[0:3], v[208:211], v[160:163], a[0:3]\n"
    "v_mov_b32 v192, v246\n"
    "v_mov_b32 v196, v247\n"
    "v_mov_b32 v200, v248\n"
    "v_mfma_f32_16x16x32_bf16 a[16:19], v[212:215], v[160:163], a[16:19]\n"
    "v_mov_b32 v204, v249\n"
    "v_permlane32_swap_b32 v246, v192\n"
    "v_permlane32_swap_b32 v247, v196\n"
    "v_mfma_f32_16x16x32_bf16 a[20:23], v[212:215], v[168:171], a[20:23]\n"
    "v_permlane32_swap_b32 v248, v200\n"
    "v_permlane32_swap_b32 v249, v204\n"
    "ds_read_b64_tr_b16 v[232:233], v242 offset:50688\n"
    "v_mfma_f32_16x16x32_bf16 a[4:7], v[208:211], v[168:171], a[4:7]\n"
    "v_add_f32 v246, v246, v192\n"
    "v_add_f32 v247, v247, v196\n"
    "ds_read_b64_tr_b16 v[234:235], v242 offset:58880\n"
    "v_mfma_f32_16x16x32_bf16 a[8:11], v[208:211], v[176:179], a[8:11]\n"
    "v_add_f32 v248, v248, v200\n"
    "v_add_f32 v249, v249, v204\n"
    "v_mov_b32 v192, v246\n"
    "v_mfma_f32_16x16x32_bf16 a[24:27], v[212:215], v[176:179], a[24:27]\n"
    "v_mov_b32 v196, v247\n"
    "v_mov_b32 v200, v248\n"
    "v_mov_b32 v204, v249\n"
    "v_mfma_f32_16x16x32_bf16 a[28:31], v[212:215], v[184:187], a[28:31]\n"
    "v_permlane16_swap_b32 v246, v192\n"
    "v_permlane16_swap_b32 v247, v196\n"
    "ds_read_b64_tr_b16 v[236:237], v243 offset:50688\n"
    "v_mfma_f32_16x16x32_bf16 a[12:15], v[208:211], v[184:187], a[12:15]\n"
    "v_permlane16_swap_b32 v248, v200\n"
    "ds_read_b64_tr_b16 v[238:239], v243 offset:58880\n"
    "s_waitcnt lgkmcnt(8)\n"
    "v_mfma_f32_16x16x32_bf16 a[44:47], v[216:219], v[184:187], a[44:47]\n"
    "v_permlane16_swap_b32 v249, v204\n"
    "v_add_f32 v246, v246, v192\n"
    "v_add_f32 v247, v247, v196\n"
    "v_mfma_f32_16x16x32_bf16 a[60:63], v[220:223], v[184:187], a[60:63]\n"
    "v_add_f32 v248, v248, v200\n"
    "v_add_f32 v249, v249, v204\n"
    "v_rcp_f32 v246, v246\n"
    "v_mfma_f32_16x16x32_bf16 a[56:59], v[220:223], v[176:179], a[56:59]\n"
    "v_rcp_f32 v247, v247\n"
    "v_rcp_f32 v248, v248\n"
    "ds_read_b64_tr_b16 v[208:209], v242 offset:51200\n"
    "v_mfma_f32_16x16x32_bf16 a[40:43], v[216:219], v[176:179], a[40:43]\n"
    "v_rcp_f32 v249, v249\n"
    "v_xor_b32 v240, 32, v251\n"
    "ds_read_b64_tr_b16 v[210:211], v242 offset:59392\n"
    "v_mfma_f32_16x16x32_bf16 a[36:39], v[216:219], v[168:171], a[36:39]\n"
    "v_xor_b32 v241, 64, v251\n"
    "v_xor_b32 v244, 96, v251\n"
    "v_accvgpr_read_b32 v64, a0\n"
    "v_mfma_f32_16x16x32_bf16 a[52:55], v[220:223], v[168:171], a[52:55]\n"
    "v_accvgpr_read_b32 v65, a1\n"
    "v_accvgpr_read_b32 v66, a2\n"
    "v_accvgpr_read_b32 v67, a3\n"
    "v_mfma_f32_16x16x32_bf16 a[48:51], v[220:223], v[160:163], a[48:51]\n"
    "v_accvgpr_read_b32 v72, a16\n"
    "v_accvgpr_read_b32 v73, a17\n"
    "ds_read_b64_tr_b16 v[212:213], v243 offset:51200\n"
    "v_mfma_f32_16x16x32_bf16 a[32:35], v[216:219], v[160:163], a[32:35]\n"
    "v_accvgpr_read_b32 v74, a18\n"
    "ds_read_b64_tr_b16 v[214:215], v243 offset:59392\n"
    "s_waitcnt lgkmcnt(8)\n"
    "v_mfma_f32_16x16x32_bf16 a[64:67], v[224:227], v[160:163], a[64:67]\n"
    "v_accvgpr_read_b32 v75, a19\n"
    "v_mul_f32 v64, v246, v64\n"
    "v_mul_f32 v65, v246, v65\n"
    "v_mul_f32 v66, v246, v66\n"
    "v_mul_f32 v67, v246, v67\n"
    "v_mfma_f32_16x16x32_bf16 a[80:83], v[228:231], v[160:163], a[80:83]\n"
    "v_accvgpr_read_b32 v80, a20\n"
    "v_accvgpr_read_b32 v81, a21\n"
    "v_accvgpr_read_b32 v82, a22\n"
    "v_mfma_f32_16x16x32_bf16 a[84:87], v[228:231], v[168:171], a[84:87]\n"
    "v_accvgpr_read_b32 v83, a23\n"
    "v_mul_f32 v72, v246, v72\n"
    "v_mul_f32 v73, v246, v73\n"
    "v_mul_f32 v74, v246, v74\n"
    "v_mul_f32 v75, v246, v75\n"
    "ds_read_b64_tr_b16 v[216:217], v242 offset:51712\n"
    "v_mfma_f32_16x16x32_bf16 a[68:71], v[224:227], v[168:171], a[68:71]\n"
    "v_cvt_pk_bf16_f32 v68, v64, v65\n"
    "v_cvt_pk_bf16_f32 v69, v66, v67\n"
    "ds_read_b64_tr_b16 v[218:219], v242 offset:59904\n"
    "v_mfma_f32_16x16x32_bf16 a[72:75], v[224:227], v[176:179], a[72:75]\n"
    "v_accvgpr_read_b32 v88, a4\n"
    "v_accvgpr_read_b32 v89, a5\n"
    "v_accvgpr_read_b32 v90, a6\n"
    "v_mfma_f32_16x16x32_bf16 a[88:91], v[228:231], v[176:179], a[88:91]\n"
    "v_accvgpr_read_b32 v91, a7\n"
    "v_mul_f32 v80, v247, v80\n"
    "v_mul_f32 v81, v247, v81\n"
    "v_mul_f32 v82, v247, v82\n"
    "v_mul_f32 v83, v247, v83\n"
    "v_mfma_f32_16x16x32_bf16 a[92:95], v[228:231], v[184:187], a[92:95]\n"
    "v_cvt_pk_bf16_f32 v76, v72, v73\n"
    "v_cvt_pk_bf16_f32 v77, v74, v75\n"
    "ds_read_b64_tr_b16 v[220:221], v243 offset:51712\n"
    "v_mfma_f32_16x16x32_bf16 a[76:79], v[224:227], v[184:187], a[76:79]\n"
    "ds_write_b64 v251, v[68:69] offset:0\n"
    "ds_read_b64_tr_b16 v[222:223], v243 offset:59904\n"
    "s_waitcnt lgkmcnt(9)\n"
    "v_mfma_f32_16x16x32_bf16 a[108:111], v[232:235], v[184:187], a[108:111]\n"
    "v_accvgpr_read_b32 v96, a8\n"
    "v_accvgpr_read_b32 v97, a9\n"
    "v_accvgpr_read_b32 v98, a10\n"
    "v_mfma_f32_16x16x32_bf16 a[124:127], v[236:239], v[184:187], a[124:127]\n"
    "v_accvgpr_read_b32 v99, a11\n"
    "v_mul_f32 v88, v247, v88\n"
    "v_mul_f32 v89, v247, v89\n"
    "v_mul_f32 v90, v247, v90\n"
    "v_mul_f32 v91, v247, v91\n"
    "v_mfma_f32_16x16x32_bf16 a[120:123], v[236:239], v[176:179], a[120:123]\n"
    "v_cvt_pk_bf16_f32 v84, v80, v81\n"
    "v_cvt_pk_bf16_f32 v85, v82, v83\n"
    "ds_read_b64_tr_b16 v[224:225], v242 offset:52224\n"
    "v_mfma_f32_16x16x32_bf16 a[104:107], v[232:235], v[176:179], a[104:107]\n"
    "ds_write_b64 v240, v[76:77] offset:0\n"
    "v_accvgpr_read_b32 v104, a24\n"
    "ds_read_b64_tr_b16 v[226:227], v242 offset:60416\n"
    "v_mfma_f32_16x16x32_bf16 a[100:103], v[232:235], v[168:171], a[100:103]\n"
    "v_accvgpr_read_b32 v105, a25\n"
    "v_accvgpr_read_b32 v106, a26\n"
    "v_accvgpr_read_b32 v107, a27\n"
    "v_mfma_f32_16x16x32_bf16 a[116:119], v[236:239], v[168:171], a[116:119]\n"
    "v_mul_f32 v96, v248, v96\n"
    "v_mul_f32 v97, v248, v97\n"
    "v_mul_f32 v98, v248, v98\n"
    "v_mul_f32 v99, v248, v99\n"
    "v_mfma_f32_16x16x32_bf16 a[112:115], v[236:239], v[160:163], a[112:115]\n"
    "v_cvt_pk_bf16_f32 v92, v88, v89\n"
    "v_cvt_pk_bf16_f32 v93, v90, v91\n"
    "ds_read_b64_tr_b16 v[228:229], v243 offset:52224\n"
    "v_mfma_f32_16x16x32_bf16 a[96:99], v[232:235], v[160:163], a[96:99]\n"
    "ds_write_b64 v240, v[84:85] offset:2048\n"
    "ds_read_b64_tr_b16 v[230:231], v243 offset:60416\n"
    "s_waitcnt lgkmcnt(11)\n"
    "v_mfma_f32_16x16x32_bf16 a[128:131], v[208:211], v[160:163], a[128:131]\n"
    "v_accvgpr_read_b32 v112, a28\n"
    "v_accvgpr_read_b32 v113, a29\n"
    "v_accvgpr_read_b32 v114, a30\n"
    "v_mfma_f32_16x16x32_bf16 a[144:147], v[212:215], v[160:163], a[144:147]\n"
    "v_accvgpr_read_b32 v115, a31\n"
    "v_mul_f32 v104, v248, v104\n"
    "v_mul_f32 v105, v248, v105\n"
    "v_mul_f32 v106, v248, v106\n"
    "v_mul_f32 v107, v248, v107\n"
    "v_mfma_f32_16x16x32_bf16 a[148:151], v[212:215], v[168:171], a[148:151]\n"
    "v_cvt_pk_bf16_f32 v100, v96, v97\n"
    "v_cvt_pk_bf16_f32 v101, v98, v99\n"
    "ds_read_b64_tr_b16 v[232:233], v242 offset:52736\n"
    "v_mfma_f32_16x16x32_bf16 a[132:135], v[208:211], v[168:171], a[132:135]\n"
    "ds_write_b64 v251, v[92:93] offset:2048\n"
    "v_accvgpr_read_b32 v120, a12\n"
    "ds_read_b64_tr_b16 v[234:235], v242 offset:60928\n"
    "v_mfma_f32_16x16x32_bf16 a[136:139], v[208:211], v[176:179], a[136:139]\n"
    "v_accvgpr_read_b32 v121, a13\n"
    "v_accvgpr_read_b32 v122, a14\n"
    "v_accvgpr_read_b32 v123, a15\n"
    "v_mfma_f32_16x16x32_bf16 a[152:155], v[212:215], v[176:179], a[152:155]\n"
    "v_mul_f32 v112, v249, v112\n"
    "v_mul_f32 v113, v249, v113\n"
    "v_mul_f32 v114, v249, v114\n"
    "v_mul_f32 v115, v249, v115\n"
    "v_mfma_f32_16x16x32_bf16 a[156:159], v[212:215], v[184:187], a[156:159]\n"
    "v_cvt_pk_bf16_f32 v108, v104, v105\n"
    "v_cvt_pk_bf16_f32 v109, v106, v107\n"
    "ds_read_b64_tr_b16 v[236:237], v243 offset:52736\n"
    "v_mfma_f32_16x16x32_bf16 a[140:143], v[208:211], v[184:187], a[140:143]\n"
    "ds_write_b64 v251, v[100:101] offset:4096\n"
    "ds_read_b64_tr_b16 v[238:239], v243 offset:60928\n"
    "s_waitcnt lgkmcnt(12)\n"
    "v_mfma_f32_16x16x32_bf16 a[172:175], v[216:219], v[184:187], a[172:175]\n"
    "v_accvgpr_read_b32 v64, a44\n"
    "v_accvgpr_read_b32 v65, a45\n"
    "v_accvgpr_read_b32 v66, a46\n"
    "v_mfma_f32_16x16x32_bf16 a[188:191], v[220:223], v[184:187], a[188:191]\n"
    "v_accvgpr_read_b32 v67, a47\n"
    "v_mul_f32 v120, v249, v120\n"
    "v_mul_f32 v121, v249, v121\n"
    "v_mul_f32 v122, v249, v122\n"
    "v_mul_f32 v123, v249, v123\n"
    "v_mfma_f32_16x16x32_bf16 a[184:187], v[220:223], v[176:179], a[184:187]\n"
    "v_cvt_pk_bf16_f32 v116, v112, v113\n"
    "v_cvt_pk_bf16_f32 v117, v114, v115\n"
    "ds_write_b64 v240, v[108:109] offset:4096\n"
    "v_mfma_f32_16x16x32_bf16 a[168:171], v[216:219], v[176:179], a[168:171]\n"
    "v_accvgpr_read_b32 v72, a60\n"
    "v_accvgpr_read_b32 v73, a61\n"
    "v_accvgpr_read_b32 v74, a62\n"
    "v_mfma_f32_16x16x32_bf16 a[164:167], v[216:219], v[168:171], a[164:167]\n"
    "v_accvgpr_read_b32 v75, a63\n"
    "v_mul_f32 v64, v249, v64\n"
    "v_mul_f32 v65, v249, v65\n"
    "v_mul_f32 v66, v249, v66\n"
    "v_mul_f32 v67, v249, v67\n"
    "v_mfma_f32_16x16x32_bf16 a[180:183], v[220:223], v[168:171], a[180:183]\n"
    "v_cvt_pk_bf16_f32 v124, v120, v121\n"
    "v_cvt_pk_bf16_f32 v125, v122, v123\n"
    "ds_write_b64 v240, v[116:117] offset:6144\n"
    "v_mfma_f32_16x16x32_bf16 a[176:179], v[220:223], v[160:163], a[176:179]\n"
    "v_accvgpr_read_b32 v80, a56\n"
    "v_accvgpr_read_b32 v81, a57\n"
    "v_accvgpr_read_b32 v82, a58\n"
    "v_mfma_f32_16x16x32_bf16 a[160:163], v[216:219], v[160:163], a[160:163]\n"
    "v_accvgpr_read_b32 v83, a59\n"
    "v_mul_f32 v72, v249, v72\n"
    "v_mul_f32 v73, v249, v73\n"
    "v_mul_f32 v74, v249, v74\n"
    "v_mul_f32 v75, v249, v75\n"
    "s_waitcnt lgkmcnt(8)\n"
    "v_mfma_f32_16x16x32_bf16 a[192:195], v[224:227], v[160:163], a[192:195]\n"
    "v_cvt_pk_bf16_f32 v68, v64, v65\n"
    "v_cvt_pk_bf16_f32 v69, v66, v67\n"
    "ds_write_b64 v251, v[124:125] offset:6144\n"
    "v_mfma_f32_16x16x32_bf16 a[208:211], v[228:231], v[160:163], a[208:211]\n"
    "v_accvgpr_read_b32 v88, a40\n"
    "v_accvgpr_read_b32 v89, a41\n"
    "v_accvgpr_read_b32 v90, a42\n"
    "v_mfma_f32_16x16x32_bf16 a[212:215], v[228:231], v[168:171], a[212:215]\n"
    "v_accvgpr_read_b32 v91, a43\n"
    "v_mul_f32 v80, v248, v80\n"
    "v_mul_f32 v81, v248, v81\n"
    "v_mul_f32 v82, v248, v82\n"
    "v_mul_f32 v83, v248, v83\n"
    "v_mfma_f32_16x16x32_bf16 a[196:199], v[224:227], v[168:171], a[196:199]\n"
    "v_cvt_pk_bf16_f32 v76, v72, v73\n"
    "v_cvt_pk_bf16_f32 v77, v74, v75\n"
    "ds_write_b64 v241, v[68:69] offset:6144\n"
    "v_mfma_f32_16x16x32_bf16 a[200:203], v[224:227], v[176:179], a[200:203]\n"
    "v_accvgpr_read_b32 v96, a36\n"
    "v_accvgpr_read_b32 v97, a37\n"
    "v_accvgpr_read_b32 v98, a38\n"
    "v_mfma_f32_16x16x32_bf16 a[216:219], v[228:231], v[176:179], a[216:219]\n"
    "v_accvgpr_read_b32 v99, a39\n"
    "v_mul_f32 v88, v248, v88\n"
    "v_mul_f32 v89, v248, v89\n"
    "v_mul_f32 v90, v248, v90\n"
    "v_mul_f32 v91, v248, v91\n"
    "v_mfma_f32_16x16x32_bf16 a[220:223], v[228:231], v[184:187], a[220:223]\n"
    "v_cvt_pk_bf16_f32 v84, v80, v81\n"
    "v_cvt_pk_bf16_f32 v85, v82, v83\n"
    "ds_write_b64 v244, v[76:77] offset:6144\n"
    "v_mfma_f32_16x16x32_bf16 a[204:207], v[224:227], v[184:187], a[204:207]\n"
    "v_accvgpr_read_b32 v104, a52\n"
    "v_accvgpr_read_b32 v105, a53\n"
    "s_waitcnt lgkmcnt(5)\n"
    "v_mfma_f32_16x16x32_bf16 a[236:239], v[232:235], v[184:187], a[236:239]\n"
    "v_accvgpr_read_b32 v106, a54\n"
    "v_accvgpr_read_b32 v107, a55\n"
    "v_mul_f32 v96, v247, v96\n"
    "v_mul_f32 v97, v247, v97\n"
    "v_mul_f32 v98, v247, v98\n"
    "v_mul_f32 v99, v247, v99\n"
    "v_mfma_f32_16x16x32_bf16 a[252:255], v[236:239], v[184:187], a[252:255]\n"
    "v_cvt_pk_bf16_f32 v92, v88, v89\n"
    "v_cvt_pk_bf16_f32 v93, v90, v91\n"
    "ds_write_b64 v244, v[84:85] offset:4096\n"
    "v_mfma_f32_16x16x32_bf16 a[248:251], v[236:239], v[176:179], a[248:251]\n"
    "v_accvgpr_read_b32 v112, a48\n"
    "v_accvgpr_read_b32 v113, a49\n"
    "v_accvgpr_read_b32 v114, a50\n"
    "v_mfma_f32_16x16x32_bf16 a[232:235], v[232:235], v[176:179], a[232:235]\n"
    "v_accvgpr_read_b32 v115, a51\n"
    "v_mul_f32 v104, v247, v104\n"
    "v_mul_f32 v105, v247, v105\n"
    "v_mul_f32 v106, v247, v106\n"
    "v_mul_f32 v107, v247, v107\n"
    "v_mfma_f32_16x16x32_bf16 a[228:231], v[232:235], v[168:171], a[228:231]\n"
    "v_cvt_pk_bf16_f32 v100, v96, v97\n"
    "v_cvt_pk_bf16_f32 v101, v98, v99\n"
    "ds_write_b64 v241, v[92:93] offset:4096\n"
    "v_mfma_f32_16x16x32_bf16 a[244:247], v[236:239], v[168:171], a[244:247]\n"
    "v_accvgpr_read_b32 v120, a32\n"
    "v_accvgpr_read_b32 v121, a33\n"
    "v_accvgpr_read_b32 v122, a34\n"
    "v_mfma_f32_16x16x32_bf16 a[240:243], v[236:239], v[160:163], a[240:243]\n"
    "v_accvgpr_read_b32 v123, a35\n"
    "v_mul_f32 v112, v246, v112\n"
    "v_mul_f32 v113, v246, v113\n"
    "v_mul_f32 v114, v246, v114\n"
    "v_mul_f32 v115, v246, v115\n"
    "v_mfma_f32_16x16x32_bf16 a[224:227], v[232:235], v[160:163], a[224:227]\n"
    "v_cvt_pk_bf16_f32 v108, v104, v105\n"
    "v_cvt_pk_bf16_f32 v109, v106, v107\n"
    "ds_write_b64 v241, v[100:101] offset:2048\n"
    "v_accvgpr_read_b32 v64, a64\n"
    "v_accvgpr_read_b32 v65, a65\n"
    "v_accvgpr_read_b32 v66, a66\n"
    "v_accvgpr_read_b32 v67, a67\n"
    "v_pk_mul_f32 v[120:121], v[120:121], v[246:247] op_sel_hi:[1,0]\n"
    "v_pk_mul_f32 v[122:123], v[122:123], v[246:247] op_sel_hi:[1,0]\n"
    "v_cvt_pk_bf16_f32 v116, v112, v113\n"
    "v_cvt_pk_bf16_f32 v117, v114, v115\n"
    "ds_write_b64 v244, v[108:109] offset:2048\n"
    "v_accvgpr_read_b32 v72, a80\n"
    "v_accvgpr_read_b32 v73, a81\n"
    "v_accvgpr_read_b32 v74, a82\n"
    "v_accvgpr_read_b32 v75, a83\n"
    "v_pk_mul_f32 v[64:65], v[64:65], v[246:247] op_sel_hi:[1,0]\n"
    "v_pk_mul_f32 v[66:67], v[66:67], v[246:247] op_sel_hi:[1,0]\n"
    "v_cvt_pk_bf16_f32 v124, v120, v121\n"
    "v_cvt_pk_bf16_f32 v125, v122, v123\n"
    "ds_write_b64 v244, v[116:117] offset:0\n"
    "v_accvgpr_read_b32 v80, a84\n"
    "v_accvgpr_read_b32 v81, a85\n"
    "v_accvgpr_read_b32 v82, a86\n"
    "v_accvgpr_read_b32 v83, a87\n"
    "v_pk_mul_f32 v[72:73], v[72:73], v[246:247] op_sel_hi:[1,0]\n"
    "v_pk_mul_f32 v[74:75], v[74:75], v[246:247] op_sel_hi:[1,0]\n"
    "v_cvt_pk_bf16_f32 v68, v64, v65\n"
    "v_cvt_pk_bf16_f32 v69, v66, v67\n"
    "ds_write_b64 v241, v[124:125] offset:0\n"
    "ds_read_b128 v[0:3], v252 offset:0\n"
    "ds_read_b128 v[4:7], v252 offset:1024\n"
    "ds_read_b128 v[8:11], v252 offset:2048\n"
    "ds_read_b128 v[12:15], v252 offset:3072\n"
    "ds_read_b128 v[16:19], v252 offset:4096\n"
    "ds_read_b128 v[20:23], v252 offset:5120\n"
    "ds_read_b128 v[24:27], v252 offset:6144\n"
    "ds_read_b128 v[28:31], v252 offset:7168\n"
    "v_accvgpr_read_b32 v88, a68\n"
    "v_accvgpr_read_b32 v89, a69\n"
    "v_accvgpr_read_b32 v90, a70\n"
    "v_accvgpr_read_b32 v91, a71\n"
    "v_pk_mul_f32 v[80:81], v[80:81], v[246:247] op_sel:[0,1]\n"
    "v_pk_mul_f32 v[82:83], v[82:83], v[246:247] op_sel:[0,1]\n"
    "v_cvt_pk_bf16_f32 v76, v72, v73\n"
    "v_cvt_pk_bf16_f32 v77, v74, v75\n"
    "ds_write_b64 v251, v[68:69] offset:0\n"
    "v_accvgpr_read_b32 v96, a72\n"
    "v_accvgpr_read_b32 v97, a73\n"
    "v_accvgpr_read_b32 v98, a74\n"
    "v_accvgpr_read_b32 v99, a75\n"
    "v_pk_mul_f32 v[88:89], v[88:89], v[246:247] op_sel:[0,1]\n"
    "v_pk_mul_f32 v[90:91], v[90:91], v[246:247] op_sel:[0,1]\n"
    "v_cvt_pk_bf16_f32 v84, v80, v81\n"
    "v_cvt_pk_bf16_f32 v85, v82, v83\n"
    "ds_write_b64 v240, v[76:77] offset:0\n"
    "s_waitcnt lgkmcnt(9)\n"
    "buffer_store_dwordx4 v[0:3], v253, s[52:55], s67 offen offset:0 nt\n"
    "s_waitcnt lgkmcnt(8)\n"
    "buffer_store_dwordx4 v[4:7], v253, s[52:55], s48 offen offset:0 nt\n"
    "s_waitcnt lgkmcnt(7)\n"
    "buffer_store_dwordx4 v[8:11], v253, s[52:55], s49 offen offset:0 nt\n"
    "s_waitcnt lgkmcnt(6)\n"
    "buffer_store_dwordx4 v[12:15], v253, s[52:55], s50 offen offset:0 nt\n"
    "s_waitcnt lgkmcnt(5)\n"
    "buffer_store_dwordx4 v[16:19], v253, s[52:55], s51 offen offset:0 nt\n"
    "s_waitcnt lgkmcnt(4)\n"
    "buffer_store_dwordx4 v[20:23], v253, s[52:55], s36 offen offset:0 nt\n"
    "s_waitcnt lgkmcnt(3)\n"
    "buffer_store_dwordx4 v[24:27], v253, s[52:55], s37 offen offset:0 nt\n"
    "s_waitcnt lgkmcnt(2)\n"
    "buffer_store_dwordx4 v[28:31], v253, s[52:55], s38 offen offset:0 nt\n"
    "v_accvgpr_read_b32 v104, a88\n"
    "v_accvgpr_read_b32 v105, a89\n"
    "v_accvgpr_read_b32 v106, a90\n"
    "v_accvgpr_read_b32 v107, a91\n"
    "v_pk_mul_f32 v[96:97], v[96:97], v[248:249] op_sel_hi:[1,0]\n"
    "v_pk_mul_f32 v[98:99], v[98:99], v[248:249] op_sel_hi:[1,0]\n"
    "v_cvt_pk_bf16_f32 v92, v88, v89\n"
    "v_cvt_pk_bf16_f32 v93, v90, v91\n"
    "ds_write_b64 v240, v[84:85] offset:2048\n"
    "v_accvgpr_read_b32 v112, a92\n"
    "v_accvgpr_read_b32 v113, a93\n"
    "v_accvgpr_read_b32 v114, a94\n"
    "v_accvgpr_read_b32 v115, a95\n"
    "v_pk_mul_f32 v[104:105], v[104:105], v[248:249] op_sel_hi:[1,0]\n"
    "v_pk_mul_f32 v[106:107], v[106:107], v[248:249] op_sel_hi:[1,0]\n"
    "v_cvt_pk_bf16_f32 v100, v96, v97\n"
    "v_cvt_pk_bf16_f32 v101, v98, v99\n"
    "ds_write_b64 v251, v[92:93] offset:2048\n"
    "v_accvgpr_read_b32 v120, a76\n"
    "v_accvgpr_read_b32 v121, a77\n"
    "v_accvgpr_read_b32 v122, a78\n"
    "v_accvgpr_read_b32 v123, a79\n"
    "v_pk_mul_f32 v[112:113], v[112:113], v[248:249] op_sel:[0,1]\n"
    "v_pk_mul_f32 v[114:115], v[114:115], v[248:249] op_sel:[0,1]\n"
    "v_cvt_pk_bf16_f32 v108, v104, v105\n"
    "v_cvt_pk_bf16_f32 v109, v106, v107\n"
    "ds_write_b64 v251, v[100:101] offset:4096\n"
    "v_accvgpr_read_b32 v64, a108\n"
    "v_accvgpr_read_b32 v65, a109\n"
    "v_accvgpr_read_b32 v66, a110\n"
    "v_accvgpr_read_b32 v67, a111\n"
    "v_pk_mul_f32 v[120:121], v[120:121], v[248:249] op_sel:[0,1]\n"
    "v_pk_mul_f32 v[122:123], v[122:123], v[248:249] op_sel:[0,1]\n"
    "v_cvt_pk_bf16_f32 v116, v112, v113\n"
    "v_cvt_pk_bf16_f32 v117, v114, v115\n"
    "ds_write_b64 v240, v[108:109] offset:4096\n"
    "v_accvgpr_read_b32 v72, a124\n"
    "v_accvgpr_read_b32 v73, a125\n"
    "v_accvgpr_read_b32 v74, a126\n"
    "v_accvgpr_read_b32 v75, a127\n"
    "v_pk_mul_f32 v[64:65], v[64:65], v[248:249] op_sel:[0,1]\n"
    "v_pk_mul_f32 v[66:67], v[66:67], v[248:249] op_sel:[0,1]\n"
    "v_cvt_pk_bf16_f32 v124, v120, v121\n"
    "v_cvt_pk_bf16_f32 v125, v122, v123\n"
    "ds_write_b64 v240, v[116:117] offset:6144\n"
    "v_accvgpr_read_b32 v80, a120\n"
    "v_accvgpr_read_b32 v81, a121\n"
    "v_accvgpr_read_b32 v82, a122\n"
    "v_accvgpr_read_b32 v83, a123\n"
    "v_pk_mul_f32 v[72:73], v[72:73], v[248:249] op_sel:[0,1]\n"
    "v_pk_mul_f32 v[74:75], v[74:75], v[248:249] op_sel:[0,1]\n"
    "v_cvt_pk_bf16_f32 v68, v64, v65\n"
    "v_cvt_pk_bf16_f32 v69, v66, v67\n"
    "ds_write_b64 v251, v[124:125] offset:6144\n"
    "v_accvgpr_read_b32 v88, a104\n"
    "v_accvgpr_read_b32 v89, a105\n"
    "v_accvgpr_read_b32 v90, a106\n"
    "v_accvgpr_read_b32 v91, a107\n"
    "v_pk_mul_f32 v[80:81], v[80:81], v[248:249] op_sel_hi:[1,0]\n"
    "v_pk_mul_f32 v[82:83], v[82:83], v[248:249] op_sel_hi:[1,0]\n"
    "v_cvt_pk_bf16_f32 v76, v72, v73\n"
    "v_cvt_pk_bf16_f32 v77, v74, v75\n"
    "ds_write_b64 v241, v[68:69] offset:6144\n"
    "v_accvgpr_read_b32 v96, a100\n"
    "v_accvgpr_read_b32 v97, a101\n"
    "v_accvgpr_read_b32 v98, a102\n"
    "v_accvgpr_read_b32 v99, a103\n"
    "v_pk_mul_f32 v[88:89], v[88:89], v[248:249] op_sel_hi:[1,0]\n"
    "v_pk_mul_f32 v[90:91], v[90:91], v[248:249] op_sel_hi:[1,0]\n"
    "v_cvt_pk_bf16_f32 v84, v80, v81\n"
    "v_cvt_pk_bf16_f32 v85, v82, v83\n"
    "ds_write_b64 v244, v[76:77] offset:6144\n"
    "v_accvgpr_read_b32 v104, a116\n"
    "v_accvgpr_read_b32 v105, a117\n"
    "v_accvgpr_read_b32 v106, a118\n"
    "v_accvgpr_read_b32 v107, a119\n"
    "v_pk_mul_f32 v[96:97], v[96:97], v[246:247] op_sel:[0,1]\n"
    "v_pk_mul_f32 v[98:99], v[98:99], v[246:247] op_sel:[0,1]\n"
    "v_cvt_pk_bf16_f32 v92, v88, v89\n"
    "v_cvt_pk_bf16_f32 v93, v90, v91\n"
    "ds_write_b64 v244, v[84:85] offset:4096\n"
    "v_accvgpr_read_b32 v112, a112\n"
    "v_accvgpr_read_b32 v113, a113\n"
    "v_accvgpr_read_b32 v114, a114\n"
    "v_accvgpr_read_b32 v115, a115\n"
    "v_pk_mul_f32 v[104:105], v[104:105], v[246:247] op_sel:[0,1]\n"
    "v_pk_mul_f32 v[106:107], v[106:107], v[246:247] op_sel:[0,1]\n"
    "v_cvt_pk_bf16_f32 v100, v96, v97\n"
    "v_cvt_pk_bf16_f32 v101, v98, v99\n"
    "ds_write_b64 v241, v[92:93] offset:4096\n"
    "v_accvgpr_read_b32 v120, a96\n"
    "v_accvgpr_read_b32 v121, a97\n"
    "v_accvgpr_read_b32 v122, a98\n"
    "v_accvgpr_read_b32 v123, a99\n"
    "v_pk_mul_f32 v[112:113], v[112:113], v[246:247] op_sel_hi:[1,0]\n"
    "v_pk_mul_f32 v[114:115], v[114:115], v[246:247] op_sel_hi:[1,0]\n"
    "v_cvt_pk_bf16_f32 v108, v104, v105\n"
    "v_cvt_pk_bf16_f32 v109, v106, v107\n"
    "ds_write_b64 v241, v[100:101] offset:2048\n"
    "v_accvgpr_read_b32 v64, a128\n"
    "v_accvgpr_read_b32 v65, a129\n"
    "v_accvgpr_read_b32 v66, a130\n"
    "v_accvgpr_read_b32 v67, a131\n"
    "v_pk_mul_f32 v[120:121], v[120:121], v[246:247] op_sel_hi:[1,0]\n"
    "v_pk_mul_f32 v[122:123], v[122:123], v[246:247] op_sel_hi:[1,0]\n"
    "v_cvt_pk_bf16_f32 v116, v112, v113\n"
    "v_cvt_pk_bf16_f32 v117, v114, v115\n"
    "ds_write_b64 v244, v[108:109] offset:2048\n"
    "v_accvgpr_read_b32 v72, a144\n"
    "v_accvgpr_read_b32 v73, a145\n"
    "v_accvgpr_read_b32 v74, a146\n"
    "v_accvgpr_read_b32 v75, a147\n"
    "v_pk_mul_f32 v[64:65], v[64:65], v[246:247] op_sel_hi:[1,0]\n"
    "v_pk_mul_f32 v[66:67], v[66:67], v[246:247] op_sel_hi:[1,0]\n"
    "v_cvt_pk_bf16_f32 v124, v120, v121\n"
    "v_cvt_pk_bf16_f32 v125, v122, v123\n"
    "ds_write_b64 v244, v[116:117] offset:0\n"
    "v_accvgpr_read_b32 v80, a148\n"
    "v_accvgpr_read_b32 v81, a149\n"
    "v_accvgpr_read_b32 v82, a150\n"
    "v_accvgpr_read_b32 v83, a151\n"
    "v_pk_mul_f32 v[72:73], v[72:73], v[246:247] op_sel_hi:[1,0]\n"
    "v_pk_mul_f32 v[74:75], v[74:75], v[246:247] op_sel_hi:[1,0]\n"
    "v_cvt_pk_bf16_f32 v68, v64, v65\n"
    "v_cvt_pk_bf16_f32 v69, v66, v67\n"
    "ds_write_b64 v241, v[124:125] offset:0\n"
    "ds_read_b128 v[32:35], v252 offset:0\n"
    "ds_read_b128 v[36:39], v252 offset:1024\n"
    "ds_read_b128 v[40:43], v252 offset:2048\n"
    "ds_read_b128 v[44:47], v252 offset:3072\n"
    "ds_read_b128 v[48:51], v252 offset:4096\n"
    "ds_read_b128 v[52:55], v252 offset:5120\n"
    "ds_read_b128 v[56:59], v252 offset:6144\n"
    "ds_read_b128 v[60:63], v252 offset:7168\n"
    "v_accvgpr_read_b32 v88, a132\n"
    "v_accvgpr_read_b32 v89, a133\n"
    "v_accvgpr_read_b32 v90, a134\n"
    "v_accvgpr_read_b32 v91, a135\n"
    "v_pk_mul_f32 v[80:81], v[80:81], v[246:247] op_sel:[0,1]\n"
    "v_pk_mul_f32 v[82:83], v[82:83], v[246:247] op_sel:[0,1]\n"
    "v_cvt_pk_bf16_f32 v76, v72, v73\n"
    "v_cvt_pk_bf16_f32 v77, v74, v75\n"
    "ds_write_b64 v251, v[68:69] offset:0\n"
    "v_accvgpr_read_b32 v96, a136\n"
    "v_accvgpr_read_b32 v97, a137\n"
    "v_accvgpr_read_b32 v98, a138\n"
    "v_accvgpr_read_b32 v99, a139\n"
    "v_pk_mul_f32 v[88:89], v[88:89], v[246:247] op_sel:[0,1]\n"
    "v_pk_mul_f32 v[90:91], v[90:91], v[246:247] op_sel:[0,1]\n"
    "v_cvt_pk_bf16_f32 v84, v80, v81\n"
    "v_cvt_pk_bf16_f32 v85, v82, v83\n"
    "ds_write_b64 v240, v[76:77] offset:0\n"
    "s_waitcnt lgkmcnt(9)\n"
    "buffer_store_dwordx4 v[32:35], v253, s[52:55], s67 offen offset:128 nt\n"
    "s_waitcnt lgkmcnt(8)\n"
    "buffer_store_dwordx4 v[36:39], v253, s[52:55], s48 offen offset:128 nt\n"
    "s_waitcnt lgkmcnt(7)\n"
    "buffer_store_dwordx4 v[40:43], v253, s[52:55], s49 offen offset:128 nt\n"
    "s_waitcnt lgkmcnt(6)\n"
    "buffer_store_dwordx4 v[44:47], v253, s[52:55], s50 offen offset:128 nt\n"
    "s_waitcnt lgkmcnt(5)\n"
    "buffer_store_dwordx4 v[48:51], v253, s[52:55], s51 offen offset:128 nt\n"
    "s_waitcnt lgkmcnt(4)\n"
    "buffer_store_dwordx4 v[52:55], v253, s[52:55], s36 offen offset:128 nt\n"
    "s_waitcnt lgkmcnt(3)\n"
    "buffer_store_dwordx4 v[56:59], v253, s[52:55], s37 offen offset:128 nt\n"
    "s_waitcnt lgkmcnt(2)\n"
    "buffer_store_dwordx4 v[60:63], v253, s[52:55], s38 offen offset:128 nt\n"
    "v_accvgpr_read_b32 v104, a152\n"
    "v_accvgpr_read_b32 v105, a153\n"
    "v_accvgpr_read_b32 v106, a154\n"
    "v_accvgpr_read_b32 v107, a155\n"
    "v_pk_mul_f32 v[96:97], v[96:97], v[248:249] op_sel_hi:[1,0]\n"
    "v_pk_mul_f32 v[98:99], v[98:99], v[248:249] op_sel_hi:[1,0]\n"
    "v_cvt_pk_bf16_f32 v92, v88, v89\n"
    "v_cvt_pk_bf16_f32 v93, v90, v91\n"
    "ds_write_b64 v240, v[84:85] offset:2048\n"
    "v_accvgpr_read_b32 v112, a156\n"
    "v_accvgpr_read_b32 v113, a157\n"
    "v_accvgpr_read_b32 v114, a158\n"
    "v_accvgpr_read_b32 v115, a159\n"
    "v_pk_mul_f32 v[104:105], v[104:105], v[248:249] op_sel_hi:[1,0]\n"
    "v_pk_mul_f32 v[106:107], v[106:107], v[248:249] op_sel_hi:[1,0]\n"
    "v_cvt_pk_bf16_f32 v100, v96, v97\n"
    "v_cvt_pk_bf16_f32 v101, v98, v99\n"
    "ds_write_b64 v251, v[92:93] offset:2048\n"
    "v_accvgpr_read_b32 v120, a140\n"
    "v_accvgpr_read_b32 v121, a141\n"
    "v_accvgpr_read_b32 v122, a142\n"
    "v_accvgpr_read_b32 v123, a143\n"
    "v_pk_mul_f32 v[112:113], v[112:113], v[248:249] op_sel:[0,1]\n"
    "v_pk_mul_f32 v[114:115], v[114:115], v[248:249] op_sel:[0,1]\n"
    "v_cvt_pk_bf16_f32 v108, v104, v105\n"
    "v_cvt_pk_bf16_f32 v109, v106, v107\n"
    "ds_write_b64 v251, v[100:101] offset:4096\n"
    "v_accvgpr_read_b32 v64, a172\n"
    "v_accvgpr_read_b32 v65, a173\n"
    "v_accvgpr_read_b32 v66, a174\n"
    "v_accvgpr_read_b32 v67, a175\n"
    "v_pk_mul_f32 v[120:121], v[120:121], v[248:249] op_sel:[0,1]\n"
    "v_pk_mul_f32 v[122:123], v[122:123], v[248:249] op_sel:[0,1]\n"
    "v_cvt_pk_bf16_f32 v116, v112, v113\n"
    "v_cvt_pk_bf16_f32 v117, v114, v115\n"
    "ds_write_b64 v240, v[108:109] offset:4096\n"
    "v_accvgpr_read_b32 v72, a188\n"
    "v_accvgpr_read_b32 v73, a189\n"
    "v_accvgpr_read_b32 v74, a190\n"
    "v_accvgpr_read_b32 v75, a191\n"
    "v_pk_mul_f32 v[64:65], v[64:65], v[248:249] op_sel:[0,1]\n"
    "v_pk_mul_f32 v[66:67], v[66:67], v[248:249] op_sel:[0,1]\n"
    "v_cvt_pk_bf16_f32 v124, v120, v121\n"
    "v_cvt_pk_bf16_f32 v125, v122, v123\n"
    "ds_write_b64 v240, v[116:117] offset:6144\n"
    "v_accvgpr_read_b32 v80, a184\n"
    "v_accvgpr_read_b32 v81, a185\n"
    "v_accvgpr_read_b32 v82, a186\n"
    "v_accvgpr_read_b32 v83, a187\n"
    "v_pk_mul_f32 v[72:73], v[72:73], v[248:249] op_sel:[0,1]\n"
    "v_pk_mul_f32 v[74:75], v[74:75], v[248:249] op_sel:[0,1]\n"
    "v_cvt_pk_bf16_f32 v68, v64, v65\n"
    "v_cvt_pk_bf16_f32 v69, v66, v67\n"
    "ds_write_b64 v251, v[124:125] offset:6144\n"
    "v_accvgpr_read_b32 v88, a168\n"
    "v_accvgpr_read_b32 v89, a169\n"
    "v_accvgpr_read_b32 v90, a170\n"
    "v_accvgpr_read_b32 v91, a171\n"
    "v_pk_mul_f32 v[80:81], v[80:81], v[248:249] op_sel_hi:[1,0]\n"
    "v_pk_mul_f32 v[82:83], v[82:83], v[248:249] op_sel_hi:[1,0]\n"
    "v_cvt_pk_bf16_f32 v76, v72, v73\n"
    "v_cvt_pk_bf16_f32 v77, v74, v75\n"
    "ds_write_b64 v241, v[68:69] offset:6144\n"
    "v_accvgpr_read_b32 v96, a164\n"
    "v_accvgpr_read_b32 v97, a165\n"
    "v_accvgpr_read_b32 v98, a166\n"
    "v_accvgpr_read_b32 v99, a167\n"
    "v_pk_mul_f32 v[88:89], v[88:89], v[248:249] op_sel_hi:[1,0]\n"
    "v_pk_mul_f32 v[90:91], v[90:91], v[248:249] op_sel_hi:[1,0]\n"
    "v_cvt_pk_bf16_f32 v84, v80, v81\n"
    "v_cvt_pk_bf16_f32 v85, v82, v83\n"
    "ds_write_b64 v244, v[76:77] offset:6144\n"
    "v_accvgpr_read_b32 v104, a180\n"
    "v_accvgpr_read_b32 v105, a181\n"
    "v_accvgpr_read_b32 v106, a182\n"
    "v_accvgpr_read_b32 v107, a183\n"
    "v_pk_mul_f32 v[96:97], v[96:97], v[246:247] op_sel:[0,1]\n"
    "v_pk_mul_f32 v[98:99], v[98:99], v[246:247] op_sel:[0,1]\n"
    "v_cvt_pk_bf16_f32 v92, v88, v89\n"
    "v_cvt_pk_bf16_f32 v93, v90, v91\n"
    "ds_write_b64 v244, v[84:85] offset:4096\n"
    "v_accvgpr_read_b32 v112, a176\n"
    "v_accvgpr_read_b32 v113, a177\n"
    "v_accvgpr_read_b32 v114, a178\n"
    "v_accvgpr_read_b32 v115, a179\n"
    "v_pk_mul_f32 v[104:105], v[104:105], v[246:247] op_sel:[0,1]\n"
    "v_pk_mul_f32 v[106:107], v[106:107], v[246:247] op_sel:[0,1]\n"
    "v_cvt_pk_bf16_f32 v100, v96, v97\n"
    "v_cvt_pk_bf16_f32 v101, v98, v99\n"
    "ds_write_b64 v241, v[92:93] offset:4096\n"
    "v_accvgpr_read_b32 v120, a160\n"
    "v_accvgpr_read_b32 v121, a161\n"
    "v_accvgpr_read_b32 v122, a162\n"
    "v_accvgpr_read_b32 v123, a163\n"
    "v_pk_mul_f32 v[112:113], v[112:113], v[246:247] op_sel_hi:[1,0]\n"
    "v_pk_mul_f32 v[114:115], v[114:115], v[246:247] op_sel_hi:[1,0]\n"
    "v_cvt_pk_bf16_f32 v108, v104, v105\n"
    "v_cvt_pk_bf16_f32 v109, v106, v107\n"
    "ds_write_b64 v241, v[100:101] offset:2048\n"
    "v_accvgpr_read_b32 v64, a192\n"
    "v_accvgpr_read_b32 v65, a193\n"
    "v_accvgpr_read_b32 v66, a194\n"
    "v_accvgpr_read_b32 v67, a195\n"
    "v_pk_mul_f32 v[120:121], v[120:121], v[246:247] op_sel_hi:[1,0]\n"
    "v_pk_mul_f32 v[122:123], v[122:123], v[246:247] op_sel_hi:[1,0]\n"
    "v_cvt_pk_bf16_f32 v116, v112, v113\n"
    "v_cvt_pk_bf16_f32 v117, v114, v115\n"
    "ds_write_b64 v244, v[108:109] offset:2048\n"
    "v_accvgpr_read_b32 v72, a208\n"
    "v_accvgpr_read_b32 v73, a209\n"
    "v_accvgpr_read_b32 v74, a210\n"
    "v_accvgpr_read_b32 v75, a211\n"
    "v_pk_mul_f32 v[64:65], v[64:65], v[246:247] op_sel_hi:[1,0]\n"
    "v_pk_mul_f32 v[66:67], v[66:67], v[246:247] op_sel_hi:[1,0]\n"
    "v_cvt_pk_bf16_f32 v124, v120, v121\n"
    "v_cvt_pk_bf16_f32 v125, v122, v123\n"
    "ds_write_b64 v244, v[116:117] offset:0\n"
    "v_accvgpr_read_b32 v80, a212\n"
    "v_accvgpr_read_b32 v81, a213\n"
    "v_accvgpr_read_b32 v82, a214\n"
    "v_accvgpr_read_b32 v83, a215\n"
    "v_pk_mul_f32 v[72:73], v[72:73], v[246:247] op_sel_hi:[1,0]\n"
    "v_pk_mul_f32 v[74:75], v[74:75], v[246:247] op_sel_hi:[1,0]\n"
    "v_cvt_pk_bf16_f32 v68, v64, v65\n"
    "v_cvt_pk_bf16_f32 v69, v66, v67\n"
    "ds_write_b64 v241, v[124:125] offset:0\n"
    "ds_read_b128 v[0:3], v252 offset:0\n"
    "ds_read_b128 v[4:7], v252 offset:1024\n"
    "ds_read_b128 v[8:11], v252 offset:2048\n"
    "ds_read_b128 v[12:15], v252 offset:3072\n"
    "ds_read_b128 v[16:19], v252 offset:4096\n"
    "ds_read_b128 v[20:23], v252 offset:5120\n"
    "ds_read_b128 v[24:27], v252 offset:6144\n"
    "ds_read_b128 v[28:31], v252 offset:7168\n"
    "v_accvgpr_read_b32 v88, a196\n"
    "v_accvgpr_read_b32 v89, a197\n"
    "v_accvgpr_read_b32 v90, a198\n"
    "v_accvgpr_read_b32 v91, a199\n"
    "v_pk_mul_f32 v[80:81], v[80:81], v[246:247] op_sel:[0,1]\n"
    "v_pk_mul_f32 v[82:83], v[82:83], v[246:247] op_sel:[0,1]\n"
    "v_cvt_pk_bf16_f32 v76, v72, v73\n"
    "v_cvt_pk_bf16_f32 v77, v74, v75\n"
    "ds_write_b64 v251, v[68:69] offset:0\n"
    "v_accvgpr_read_b32 v96, a200\n"
    "v_accvgpr_read_b32 v97, a201\n"
    "v_accvgpr_read_b32 v98, a202\n"
    "v_accvgpr_read_b32 v99, a203\n"
    "v_pk_mul_f32 v[88:89], v[88:89], v[246:247] op_sel:[0,1]\n"
    "v_pk_mul_f32 v[90:91], v[90:91], v[246:247] op_sel:[0,1]\n"
    "v_cvt_pk_bf16_f32 v84, v80, v81\n"
    "v_cvt_pk_bf16_f32 v85, v82, v83\n"
    "ds_write_b64 v240, v[76:77] offset:0\n"
    "s_waitcnt lgkmcnt(9)\n"
    "buffer_store_dwordx4 v[0:3], v253, s[52:55], s67 offen offset:256 nt\n"
    "s_waitcnt lgkmcnt(8)\n"
    "buffer_store_dwordx4 v[4:7], v253, s[52:55], s48 offen offset:256 nt\n"
    "s_waitcnt lgkmcnt(7)\n"
    "buffer_store_dwordx4 v[8:11], v253, s[52:55], s49 offen offset:256 nt\n"
    "s_waitcnt lgkmcnt(6)\n"
    "buffer_store_dwordx4 v[12:15], v253, s[52:55], s50 offen offset:256 nt\n"
    "s_waitcnt lgkmcnt(5)\n"
    "buffer_store_dwordx4 v[16:19], v253, s[52:55], s51 offen offset:256 nt\n"
    "s_waitcnt lgkmcnt(4)\n"
    "buffer_store_dwordx4 v[20:23], v253, s[52:55], s36 offen offset:256 nt\n"
    "s_waitcnt lgkmcnt(3)\n"
    "buffer_store_dwordx4 v[24:27], v253, s[52:55], s37 offen offset:256 nt\n"
    "s_waitcnt lgkmcnt(2)\n"
    "buffer_store_dwordx4 v[28:31], v253, s[52:55], s38 offen offset:256 nt\n"
    "v_accvgpr_read_b32 v104, a216\n"
    "v_accvgpr_read_b32 v105, a217\n"
    "v_accvgpr_read_b32 v106, a218\n"
    "v_accvgpr_read_b32 v107, a219\n"
    "v_pk_mul_f32 v[96:97], v[96:97], v[248:249] op_sel_hi:[1,0]\n"
    "v_pk_mul_f32 v[98:99], v[98:99], v[248:249] op_sel_hi:[1,0]\n"
    "v_cvt_pk_bf16_f32 v92, v88, v89\n"
    "v_cvt_pk_bf16_f32 v93, v90, v91\n"
    "ds_write_b64 v240, v[84:85] offset:2048\n"
    "v_accvgpr_read_b32 v112, a220\n"
    "v_accvgpr_read_b32 v113, a221\n"
    "v_accvgpr_read_b32 v114, a222\n"
    "v_accvgpr_read_b32 v115, a223\n"
    "v_pk_mul_f32 v[104:105], v[104:105], v[248:249] op_sel_hi:[1,0]\n"
    "v_pk_mul_f32 v[106:107], v[106:107], v[248:249] op_sel_hi:[1,0]\n"
    "v_cvt_pk_bf16_f32 v100, v96, v97\n"
    "v_cvt_pk_bf16_f32 v101, v98, v99\n"
    "ds_write_b64 v251, v[92:93] offset:2048\n"
    "v_accvgpr_read_b32 v120, a204\n"
    "v_accvgpr_read_b32 v121, a205\n"
    "v_accvgpr_read_b32 v122, a206\n"
    "v_accvgpr_read_b32 v123, a207\n"
    "v_pk_mul_f32 v[112:113], v[112:113], v[248:249] op_sel:[0,1]\n"
    "v_pk_mul_f32 v[114:115], v[114:115], v[248:249] op_sel:[0,1]\n"
    "v_cvt_pk_bf16_f32 v108, v104, v105\n"
    "v_cvt_pk_bf16_f32 v109, v106, v107\n"
    "ds_write_b64 v251, v[100:101] offset:4096\n"
    "v_accvgpr_read_b32 v64, a236\n"
    "v_accvgpr_read_b32 v65, a237\n"
    "v_accvgpr_read_b32 v66, a238\n"
    "v_accvgpr_read_b32 v67, a239\n"
    "v_pk_mul_f32 v[120:121], v[120:121], v[248:249] op_sel:[0,1]\n"
    "v_pk_mul_f32 v[122:123], v[122:123], v[248:249] op_sel:[0,1]\n"
    "v_cvt_pk_bf16_f32 v116, v112, v113\n"
    "v_cvt_pk_bf16_f32 v117, v114, v115\n"
    "ds_write_b64 v240, v[108:109] offset:4096\n"
    "v_accvgpr_read_b32 v72, a252\n"
    "v_accvgpr_read_b32 v73, a253\n"
    "v_accvgpr_read_b32 v74, a254\n"
    "v_accvgpr_read_b32 v75, a255\n"
    "v_pk_mul_f32 v[64:65], v[64:65], v[248:249] op_sel:[0,1]\n"
    "v_pk_mul_f32 v[66:67], v[66:67], v[248:249] op_sel:[0,1]\n"
    "v_cvt_pk_bf16_f32 v124, v120, v121\n"
    "v_cvt_pk_bf16_f32 v125, v122, v123\n"
    "ds_write_b64 v240, v[116:117] offset:6144\n"
    "v_accvgpr_read_b32 v80, a248\n"
    "v_accvgpr_read_b32 v81, a249\n"
    "v_accvgpr_read_b32 v82, a250\n"
    "v_accvgpr_read_b32 v83, a251\n"
    "v_pk_mul_f32 v[72:73], v[72:73], v[248:249] op_sel:[0,1]\n"
    "v_pk_mul_f32 v[74:75], v[74:75], v[248:249] op_sel:[0,1]\n"
    "v_cvt_pk_bf16_f32 v68, v64, v65\n"
    "v_cvt_pk_bf16_f32 v69, v66, v67\n"
    "ds_write_b64 v251, v[124:125] offset:6144\n"
    "v_accvgpr_read_b32 v88, a232\n"
    "v_accvgpr_read_b32 v89, a233\n"
    "v_accvgpr_read_b32 v90, a234\n"
    "v_accvgpr_read_b32 v91, a235\n"
    "v_pk_mul_f32 v[80:81], v[80:81], v[248:249] op_sel_hi:[1,0]\n"
    "v_pk_mul_f32 v[82:83], v[82:83], v[248:249] op_sel_hi:[1,0]\n"
    "v_cvt_pk_bf16_f32 v76, v72, v73\n"
    "v_cvt_pk_bf16_f32 v77, v74, v75\n"
    "ds_write_b64 v241, v[68:69] offset:6144\n"
    "v_accvgpr_read_b32 v96, a228\n"
    "v_accvgpr_read_b32 v97, a229\n"
    "v_accvgpr_read_b32 v98, a230\n"
    "v_accvgpr_read_b32 v99, a231\n"
    "v_pk_mul_f32 v[88:89], v[88:89], v[248:249] op_sel_hi:[1,0]\n"
    "v_pk_mul_f32 v[90:91], v[90:91], v[248:249] op_sel_hi:[1,0]\n"
    "v_cvt_pk_bf16_f32 v84, v80, v81\n"
    "v_cvt_pk_bf16_f32 v85, v82, v83\n"
    "ds_write_b64 v244, v[76:77] offset:6144\n"
    "v_accvgpr_read_b32 v104, a244\n"
    "v_accvgpr_read_b32 v105, a245\n"
    "v_accvgpr_read_b32 v106, a246\n"
    "v_accvgpr_read_b32 v107, a247\n"
    "v_pk_mul_f32 v[96:97], v[96:97], v[246:247] op_sel:[0,1]\n"
    "v_pk_mul_f32 v[98:99], v[98:99], v[246:247] op_sel:[0,1]\n"
    "v_cvt_pk_bf16_f32 v92, v88, v89\n"
    "v_cvt_pk_bf16_f32 v93, v90, v91\n"
    "ds_write_b64 v244, v[84:85] offset:4096\n"
    "v_accvgpr_read_b32 v112, a240\n"
    "v_accvgpr_read_b32 v113, a241\n"
    "v_accvgpr_read_b32 v114, a242\n"
    "v_accvgpr_read_b32 v115, a243\n"
    "v_pk_mul_f32 v[104:105], v[104:105], v[246:247] op_sel:[0,1]\n"
    "v_pk_mul_f32 v[106:107], v[106:107], v[246:247] op_sel:[0,1]\n"
    "v_cvt_pk_bf16_f32 v100, v96, v97\n"
    "v_cvt_pk_bf16_f32 v101, v98, v99\n"
    "ds_write_b64 v241, v[92:93] offset:4096\n"
    "v_accvgpr_read_b32 v120, a224\n"
    "v_accvgpr_read_b32 v121, a225\n"
    "v_accvgpr_read_b32 v122, a226\n"
    "v_accvgpr_read_b32 v123, a227\n"
    "v_pk_mul_f32 v[112:113], v[112:113], v[246:247] op_sel_hi:[1,0]\n"
    "v_pk_mul_f32 v[114:115], v[114:115], v[246:247] op_sel_hi:[1,0]\n"
    "v_cvt_pk_bf16_f32 v108, v104, v105\n"
    "v_cvt_pk_bf16_f32 v109, v106, v107\n"
    "ds_write_b64 v241, v[100:101] offset:2048\n"
    "v_pk_mul_f32 v[120:121], v[120:121], v[246:247] op_sel_hi:[1,0]\n"
    "v_pk_mul_f32 v[122:123], v[122:123], v[246:247] op_sel_hi:[1,0]\n"
    "v_cvt_pk_bf16_f32 v116, v112, v113\n"
    "v_cvt_pk_bf16_f32 v117, v114, v115\n"
    "ds_write_b64 v244, v[108:109] offset:2048\n"
    "v_cvt_pk_bf16_f32 v124, v120, v121\n"
    "v_cvt_pk_bf16_f32 v125, v122, v123\n"
    "ds_write_b64 v244, v[116:117] offset:0\n"
    "ds_write_b64 v241, v[124:125] offset:0\n"
    "ds_read_b128 v[32:35], v252 offset:0\n"
    "ds_read_b128 v[36:39], v252 offset:1024\n"
    "ds_read_b128 v[40:43], v252 offset:2048\n"
    "ds_read_b128 v[44:47], v252 offset:3072\n"
    "ds_read_b128 v[48:51], v252 offset:4096\n"
    "ds_read_b128 v[52:55], v252 offset:5120\n"
    "ds_read_b128 v[56:59], v252 offset:6144\n"
    "ds_read_b128 v[60:63], v252 offset:7168\n"
    "s_waitcnt lgkmcnt(7)\n"
    "buffer_store_dwordx4 v[32:35], v253, s[52:55], s67 offen offset:384 nt\n"
    "s_waitcnt lgkmcnt(6)\n"
    "buffer_store_dwordx4 v[36:39], v253, s[52:55], s48 offen offset:384 nt\n"
    "s_waitcnt lgkmcnt(5)\n"
    "buffer_store_dwordx4 v[40:43], v253, s[52:55], s49 offen offset:384 nt\n"
    "s_waitcnt lgkmcnt(4)\n"
    "buffer_store_dwordx4 v[44:47], v253, s[52:55], s50 offen offset:384 nt\n"
    "s_waitcnt lgkmcnt(3)\n"
    "buffer_store_dwordx4 v[48:51], v253, s[52:55], s51 offen offset:384 nt\n"
    "s_waitcnt lgkmcnt(2)\n"
    "buffer_store_dwordx4 v[52:55], v253, s[52:55], s36 offen offset:384 nt\n"
    "s_waitcnt lgkmcnt(1)\n"
    "buffer_store_dwordx4 v[56:59], v253, s[52:55], s37 offen offset:384 nt\n"
    "s_waitcnt lgkmcnt(0)\n"
    "buffer_store_dwordx4 v[60:63], v253, s[52:55], s38 offen offset:384 nt\n"
    "s_endpgm\n"
    "LS_hotend_%=:\n"
    "LS_st1_0_%=:\n"
    "s_call_b64 s[62:63], LS_rare1_1_%=\n"
    "s_branch LS_st1_0_back_%=\n"
    "LS_st2_1_%=:\n"
    "s_call_b64 s[62:63], LS_rare2_0_%=\n"
    "s_branch LS_st2_1_back_%=\n"
    "LS_st1_2_%=:\n"
    "s_call_b64 s[62:63], LS_rare1_0_%=\n"
    "s_branch LS_st1_2_back_%=\n"
    "LS_st2_3_%=:\n"
    "s_call_b64 s[62:63], LS_rare2_1_%=\n"
    "s_branch LS_st2_3_back_%=\n"
    "LS_st1_4_%=:\n"
    "s_call_b64 s[62:63], LS_rare1_1_%=\n"
    "s_branch LS_st1_4_back_%=\n"
    "LS_st2_5_%=:\n"
    "s_call_b64 s[62:63], LS_rare2_0_%=\n"
    "s_branch LS_st2_5_back_%=\n"
    "LS_st1_6_%=:\n"
    "s_call_b64 s[62:63], LS_rare1_0_%=\n"
    "s_branch LS_st1_6_back_%=\n"
    "LS_st2_7_%=:\n"
    "s_call_b64 s[62:63], LS_rare2_1_%=\n"
    "s_branch LS_st2_7_back_%=\n"
    "LS_rare1_0_%=:\n"
    "s_nop 7\n"
    "v_max3_f32 v164, v128, v129, v130\n"
    "v_max3_f32 v164, v164, v131, v132\n"
    "v_max3_f32 v164, v164, v133, v134\n"
    "v_max_f32 v164, v164, v135\n"
    "v_mov_b32 v165, v164\n"
    "s_nop 1\n"
    "v_permlane32_swap_b32 v164, v165\n"
    "s_nop 1\n"
    "v_max_f32 v164, v164, v165\n"
    "v_mov_b32 v165, v164\n"
    "s_nop 1\n"
    "v_permlane16_swap_b32 v164, v165\n"
    "s_nop 1\n"
    "v_max_f32 v164, v164, v165\n"
    "v_max_f32 v164, 0, v164\n"
    "v_sub_f32 v128, v128, v164\n"
    "v_sub_f32 v129, v129, v164\n"
    "v_sub_f32 v130, v130, v164\n"
    "v_sub_f32 v131, v131, v164\n"
    "v_sub_f32 v132, v132, v164\n"
    "v_sub_f32 v133, v133, v164\n"
    "v_sub_f32 v134, v134, v164\n"
    "v_sub_f32 v135, v135, v164\n"
    "v_sub_f32 v192, v192, v164\n"
    "v_sub_f32 v193, v193, v164\n"
    "v_sub_f32 v194, v194, v164\n"
    "v_sub_f32 v195, v195, v164\n"
    "v_exp_f32 v166, -v164\n"
    "s_nop 0\n"
    "v_mul_f32 v246, v166, v246\n"
    "v_max3_f32 v172, v136, v137, v138\n"
    "v_max3_f32 v172, v172, v139, v140\n"
    "v_max3_f32 v172, v172, v141, v142\n"
    "v_max_f32 v172, v172, v143\n"
    "v_mov_b32 v173, v172\n"
    "s_nop 1\n"
    "v_permlane32_swap_b32 v172, v173\n"
    "s_nop 1\n"
    "v_max_f32 v172, v172, v173\n"
    "v_mov_b32 v173, v172\n"
    "s_nop 1\n"
    "v_permlane16_swap_b32 v172, v173\n"
    "s_nop 1\n"
    "v_max_f32 v172, v172, v173\n"
    "v_max_f32 v172, 0, v172\n"
    "v_sub_f32 v136, v136, v172\n"
    "v_sub_f32 v137, v137, v172\n"
    "v_sub_f32 v138, v138, v172\n"
    "v_sub_f32 v139, v139, v172\n"
    "v_sub_f32 v140, v140, v172\n"
    "v_sub_f32 v141, v141, v172\n"
    "v_sub_f32 v142, v142, v172\n"
    "v_sub_f32 v143, v143, v172\n"
    "v_sub_f32 v196, v196, v172\n"
    "v_sub_f32 v197, v197, v172\n"
    "v_sub_f32 v198, v198, v172\n"
    "v_sub_f32 v199, v199, v172\n"
    "v_exp_f32 v174, -v172\n"
    "s_nop 0\n"
    "v_mul_f32 v247, v174, v247\n"
    "v_max3_f32 v180, v144, v145, v146\n"
    "v_max3_f32 v180, v180, v147, v148\n"
    "v_max3_f32 v180, v180, v149, v150\n"
    "v_max_f32 v180, v180, v151\n"
    "v_mov_b32 v181, v180\n"
    "s_nop 1\n"
    "v_permlane32_swap_b32 v180, v181\n"
    "s_nop 1\n"
    "v_max_f32 v180, v180, v181\n"
    "v_mov_b32 v181, v180\n"
    "s_nop 1\n"
    "v_permlane16_swap_b32 v180, v181\n"
    "s_nop 1\n"
    "v_max_f32 v180, v180, v181\n"
    "v_max_f32 v180, 0, v180\n"
    "v_sub_f32 v144, v144, v180\n"
    "v_sub_f32 v145, v145, v180\n"
    "v_sub_f32 v146, v146, v180\n"
    "v_sub_f32 v147, v147, v180\n"
    "v_sub_f32 v148, v148, v180\n"
    "v_sub_f32 v149, v149, v180\n"
    "v_sub_f32 v150, v150, v180\n"
    "v_sub_f32 v151, v151, v180\n"
    "v_sub_f32 v200, v200, v180\n"
    "v_sub_f32 v201, v201, v180\n"
    "v_sub_f32 v202, v202, v180\n"
    "v_sub_f32 v203, v203, v180\n"
    "v_exp_f32 v182, -v180\n"
    "s_nop 0\n"
    "v_mul_f32 v248, v182, v248\n"
    "v_max3_f32 v188, v152, v153, v154\n"
    "v_max3_f32 v188, v188, v155, v156\n"
    "v_max3_f32 v188, v188, v157, v158\n"
    "v_max_f32 v188, v188, v159\n"
    "v_mov_b32 v189, v188\n"
    "s_nop 1\n"
    "v_permlane32_swap_b32 v188, v189\n"
    "s_nop 1\n"
    "v_max_f32 v188, v188, v189\n"
    "v_mov_b32 v189, v188\n"
    "s_nop 1\n"
    "v_permlane16_swap_b32 v188, v189\n"
    "s_nop 1\n"
    "v_max_f32 v188, v188, v189\n"
    "v_max_f32 v188, 0, v188\n"
    "v_sub_f32 v152, v152, v188\n"
    "v_sub_f32 v153, v153, v188\n"
    "v_sub_f32 v154, v154, v188\n"
    "v_sub_f32 v155, v155, v188\n"
    "v_sub_f32 v156, v156, v188\n"
    "v_sub_f32 v157, v157, v188\n"
    "v_sub_f32 v158, v158, v188\n"
    "v_sub_f32 v159, v159, v188\n"
    "v_sub_f32 v204, v204, v188\n"
    "v_sub_f32 v205, v205, v188\n"
    "v_sub_f32 v206, v206, v188\n"
    "v_sub_f32 v207, v207, v188\n"
    "v_exp_f32 v190, -v188\n"
    "s_nop 0\n"
    "v_mul_f32 v249, v190, v249\n"
    "s_nop 3\n"
    "s_setpc_b64 s[62:63]\n"
    "LS_rare1_1_%=:\n"
    "s_nop 7\n"
    "v_max3_f32 v132, v160, v161, v162\n"
    "v_max3_f32 v132, v132, v163, v164\n"
    "v_max3_f32 v132, v132, v165, v166\n"
    "v_max_f32 v132, v132, v167\n"
    "v_mov_b32 v133, v132\n"
    "s_nop 1\n"
    "v_permlane32_swap_b32 v132, v133\n"
    "s_nop 1\n"
    "v_max_f32 v132, v132, v133\n"
    "v_mov_b32 v133, v132\n"
    "s_nop 1\n"
    "v_permlane16_swap_b32 v132, v133\n"
    "s_nop 1\n"
    "v_max_f32 v132, v132, v133\n"
    "v_max_f32 v132, 0, v132\n"
    "v_sub_f32 v160, v160, v132\n"
    "v_sub_f32 v161, v161, v132\n"
    "v_sub_f32 v162, v162, v132\n"
    "v_sub_f32 v163, v163, v132\n"
    "v_sub_f32 v164, v164, v132\n"
    "v_sub_f32 v165, v165, v132\n"
    "v_sub_f32 v166, v166, v132\n"
    "v_sub_f32 v167, v167, v132\n"
    "v_sub_f32 v192, v192, v132\n"
    "v_sub_f32 v193, v193, v132\n"
    "v_sub_f32 v194, v194, v132\n"
    "v_sub_f32 v195, v195, v132\n"
    "v_exp_f32 v134, -v132\n"
    "s_nop 0\n"
    "v_mul_f32 v246, v134, v246\n"
    "v_max3_f32 v140, v168, v169, v170\n"
    "v_max3_f32 v140, v140, v171, v172\n"
    "v_max3_f32 v140, v140, v173, v174\n"
    "v_max_f32 v140, v140, v175\n"
    "v_mov_b32 v141, v140\n"
    "s_nop 1\n"
    "v_permlane32_swap_b32 v140, v141\n"
    "s_nop 1\n"
    "v_max_f32 v140, v140, v141\n"
    "v_mov_b32 v141, v140\n"
    "s_nop 1\n"
    "v_permlane16_swap_b32 v140, v141\n"
    "s_nop 1\n"
    "v_max_f32 v140, v140, v141\n"
    "v_max_f32 v140, 0, v140\n"
    "v_sub_f32 v168, v168, v140\n"
    "v_sub_f32 v169, v169, v140\n"
    "v_sub_f32 v170, v170, v140\n"
    "v_sub_f32 v171, v171, v140\n"
    "v_sub_f32 v172, v172, v140\n"
    "v_sub_f32 v173, v173, v140\n"
    "v_sub_f32 v174, v174, v140\n"
    "v_sub_f32 v175, v175, v140\n"
    "v_sub_f32 v196, v196, v140\n"
    "v_sub_f32 v197, v197, v140\n"
    "v_sub_f32 v198, v198, v140\n"
    "v_sub_f32 v199, v199, v140\n"
    "v_exp_f32 v142, -v140\n"
    "s_nop 0\n"
    "v_mul_f32 v247, v142, v247\n"
    "v_max3_f32 v148, v176, v177, v178\n"
    "v_max3_f32 v148, v148, v179, v180\n"
    "v_max3_f32 v148, v148, v181, v182\n"
    "v_max_f32 v148, v148, v183\n"
    "v_mov_b32 v149, v148\n"
    "s_nop 1\n"
    "v_permlane32_swap_b32 v148, v149\n"
    "s_nop 1\n"
    "v_max_f32 v148, v148, v149\n"
    "v_mov_b32 v149, v148\n"
    "s_nop 1\n"
    "v_permlane16_swap_b32 v148, v149\n"
    "s_nop 1\n"
    "v_max_f32 v148, v148, v149\n"
    "v_max_f32 v148, 0, v148\n"
    "v_sub_f32 v176, v176, v148\n"
    "v_sub_f32 v177, v177, v148\n"
    "v_sub_f32 v178, v178, v148\n"
    "v_sub_f32 v179, v179, v148\n"
    "v_sub_f32 v180, v180, v148\n"
    "v_sub_f32 v181, v181, v148\n"
    "v_sub_f32 v182, v182, v148\n"
    "v_sub_f32 v183, v183, v148\n"
    "v_sub_f32 v200, v200, v148\n"
    "v_sub_f32 v201, v201, v148\n"
    "v_sub_f32 v202, v202, v148\n"
    "v_sub_f32 v203, v203, v148\n"
    "v_exp_f32 v150, -v148\n"
    "s_nop 0\n"
    "v_mul_f32 v248, v150, v248\n"
    "v_max3_f32 v156, v184, v185, v186\n"
    "v_max3_f32 v156, v156, v187, v188\n"
    "v_max3_f32 v156, v156, v189, v190\n"
    "v_max_f32 v156, v156, v191\n"
    "v_mov_b32 v157, v156\n"
    "s_nop 1\n"
    "v_permlane32_swap_b32 v156, v157\n"
    "s_nop 1\n"
    "v_max_f32 v156, v156, v157\n"
    "v_mov_b32 v157, v156\n"
    "s_nop 1\n"
    "v_permlane16_swap_b32 v156, v157\n"
    "s_nop 1\n"
    "v_max_f32 v156, v156, v157\n"
    "v_max_f32 v156, 0, v156\n"
    "v_sub_f32 v184, v184, v156\n"
    "v_sub_f32 v185, v185, v156\n"
    "v_sub_f32 v186, v186, v156\n"
    "v_sub_f32 v187, v187, v156\n"
    "v_sub_f32 v188, v188, v156\n"
    "v_sub_f32 v189, v189, v156\n"
    "v_sub_f32 v190, v190, v156\n"
    "v_sub_f32 v191, v191, v156\n"
    "v_sub_f32 v204, v204, v156\n"
    "v_sub_f32 v205, v205, v156\n"
    "v_sub_f32 v206, v206, v156\n"
    "v_sub_f32 v207, v207, v156\n"
    "v_exp_f32 v158, -v156\n"
    "s_nop 0\n"
    "v_mul_f32 v249, v158, v249\n"
    "s_nop 3\n"
    "s_setpc_b64 s[62:63]\n"
    "LS_rare2_0_%=:\n"
    "s_nop 15\n"
    "v_accvgpr_read_b32 v132, a0\n"
    "v_mul_f32 v132, v134, v132\n"
    "v_accvgpr_write_b32 a0, v132\n"
    "v_accvgpr_read_b32 v140, a1\n"
    "v_mul_f32 v140, v134, v140\n"
    "v_accvgpr_write_b32 a1, v140\n"
    "v_accvgpr_read_b32 v148, a2\n"
    "v_mul_f32 v148, v134, v148\n"
    "v_accvgpr_write_b32 a2, v148\n"
    "v_accvgpr_read_b32 v156, a3\n"
    "v_mul_f32 v156, v134, v156\n"
    "v_accvgpr_write_b32 a3, v156\n"
    "v_accvgpr_read_b32 v133, a4\n"
    "v_mul_f32 v133, v142, v133\n"
    "v_accvgpr_write_b32 a4, v133\n"
    "v_accvgpr_read_b32 v141, a5\n"
    "v_mul_f32 v141, v142, v141\n"
    "v_accvgpr_write_b32 a5, v141\n"
    "v_accvgpr_read_b32 v149, a6\n"
    "v_mul_f32 v149, v142, v149\n"
    "v_accvgpr_write_b32 a6, v149\n"
    "v_accvgpr_read_b32 v157, a7\n"
    "v_mul_f32 v157, v142, v157\n"
    "v_accvgpr_write_b32 a7, v157\n"
    "v_accvgpr_read_b32 v132, a8\n"
    "v_mul_f32 v132, v150, v132\n"
    "v_accvgpr_write_b32 a8, v132\n"
    "v_accvgpr_read_b32 v140, a9\n"
    "v_mul_f32 v140, v150, v140\n"
    "v_accvgpr_write_b32 a9, v140\n"
    "v_accvgpr_read_b32 v148, a10\n"
    "v_mul_f32 v148, v150, v148\n"
    "v_accvgpr_write_b32 a10, v148\n"
    "v_accvgpr_read_b32 v156, a11\n"
    "v_mul_f32 v156, v150, v156\n"
    "v_accvgpr_write_b32 a11, v156\n"
    "v_accvgpr_read_b32 v133, a12\n"
    "v_mul_f32 v133, v158, v133\n"
    "v_accvgpr_write_b32 a12, v133\n"
    "v_accvgpr_read_b32 v141, a13\n"
    "v_mul_f32 v141, v158, v141\n"
    "v_accvgpr_write_b32 a13, v141\n"
    "v_accvgpr_read_b32 v149, a14\n"
    "v_mul_f32 v149, v158, v149\n"
    "v_accvgpr_write_b32 a14, v149\n"
    "v_accvgpr_read_b32 v157, a15\n"
    "v_mul_f32 v157, v158, v157\n"
    "v_accvgpr_write_b32 a15, v157\n"
    "v_accvgpr_read_b32 v132, a16\n"
    "v_mul_f32 v132, v134, v132\n"
    "v_accvgpr_write_b32 a16, v132\n"
    "v_accvgpr_read_b32 v140, a17\n"
    "v_mul_f32 v140, v134, v140\n"
    "v_accvgpr_write_b32 a17, v140\n"
    "v_accvgpr_read_b32 v148, a18\n"
    "v_mul_f32 v148, v134, v148\n"
    "v_accvgpr_write_b32 a18, v148\n"
    "v_accvgpr_read_b32 v156, a19\n"
    "v_mul_f32 v156, v134, v156\n"
    "v_accvgpr_write_b32 a19, v156\n"
    "v_accvgpr_read_b32 v133, a20\n"
    "v_mul_f32 v133, v142, v133\n"
    "v_accvgpr_write_b32 a20, v133\n"
    "v_accvgpr_read_b32 v141, a21\n"
    "v_mul_f32 v141, v142, v141\n"
    "v_accvgpr_write_b32 a21, v141\n"
    "v_accvgpr_read_b32 v149, a22\n"
    "v_mul_f32 v149, v142, v149\n"
    "v_accvgpr_write_b32 a22, v149\n"
    "v_accvgpr_read_b32 v157, a23\n"
    "v_mul_f32 v157, v142, v157\n"
    "v_accvgpr_write_b32 a23, v157\n"
    "v_accvgpr_read_b32 v132, a24\n"
    "v_mul_f32 v132, v150, v132\n"
    "v_accvgpr_write_b32 a24, v132\n"
    "v_accvgpr_read_b32 v140, a25\n"
    "v_mul_f32 v140, v150, v140\n"
    "v_accvgpr_write_b32 a25, v140\n"
    "v_accvgpr_read_b32 v148, a26\n"
    "v_mul_f32 v148, v150, v148\n"
    "v_accvgpr_write_b32 a26, v148\n"
    "v_accvgpr_read_b32 v156, a27\n"
    "v_mul_f32 v156, v150, v156\n"
    "v_accvgpr_write_b32 a27, v156\n"
    "v_accvgpr_read_b32 v133, a28\n"
    "v_mul_f32 v133, v158, v133\n"
    "v_accvgpr_write_b32 a28, v133\n"
    "v_accvgpr_read_b32 v141, a29\n"
    "v_mul_f32 v141, v158, v141\n"
    "v_accvgpr_write_b32 a29, v141\n"
    "v_accvgpr_read_b32 v149, a30\n"
    "v_mul_f32 v149, v158, v149\n"
    "v_accvgpr_write_b32 a30, v149\n"
    "v_accvgpr_read_b32 v157, a31\n"
    "v_mul_f32 v157, v158, v157\n"
    "v_accvgpr_write_b32 a31, v157\n"
    "v_accvgpr_read_b32 v132, a32\n"
    "v_mul_f32 v132, v134, v132\n"
    "v_accvgpr_write_b32 a32, v132\n"
    "v_accvgpr_read_b32 v140, a33\n"
    "v_mul_f32 v140, v134, v140\n"
    "v_accvgpr_write_b32 a33, v140\n"
    "v_accvgpr_read_b32 v148, a34\n"
    "v_mul_f32 v148, v134, v148\n"
    "v_accvgpr_write_b32 a34, v148\n"
    "v_accvgpr_read_b32 v156, a35\n"
    "v_mul_f32 v156, v134, v156\n"
    "v_accvgpr_write_b32 a35, v156\n"
    "v_accvgpr_read_b32 v133, a36\n"
    "v_mul_f32 v133, v142, v133\n"
    "v_accvgpr_write_b32 a36, v133\n"
    "v_accvgpr_read_b32 v141, a37\n"
    "v_mul_f32 v141, v142, v141\n"
    "v_accvgpr_write_b32 a37, v141\n"
    "v_accvgpr_read_b32 v149, a38\n"
    "v_mul_f32 v149, v142, v149\n"
    "v_accvgpr_write_b32 a38, v149\n"
    "v_accvgpr_read_b32 v157, a39\n"
    "v_mul_f32 v157, v142, v157\n"
    "v_accvgpr_write_b32 a39, v157\n"
    "v_accvgpr_read_b32 v132, a40\n"
    "v_mul_f32 v132, v150, v132\n"
    "v_accvgpr_write_b32 a40, v132\n"
    "v_accvgpr_read_b32 v140, a41\n"
    "v_mul_f32 v140, v150, v140\n"
    "v_accvgpr_write_b32 a41, v140\n"
    "v_accvgpr_read_b32 v148, a42\n"
    "v_mul_f32 v148, v150, v148\n"
    "v_accvgpr_write_b32 a42, v148\n"
    "v_accvgpr_read_b32 v156, a43\n"
    "v_mul_f32 v156, v150, v156\n"
    "v_accvgpr_write_b32 a43, v156\n"
    "v_accvgpr_read_b32 v133, a44\n"
    "v_mul_f32 v133, v158, v133\n"
    "v_accvgpr_write_b32 a44, v133\n"
    "v_accvgpr_read_b32 v141, a45\n"
    "v_mul_f32 v141, v158, v141\n"
    "v_accvgpr_write_b32 a45, v141\n"
    "v_accvgpr_read_b32 v149, a46\n"
    "v_mul_f32 v149, v158, v149\n"
    "v_accvgpr_write_b32 a46, v149\n"
    "v_accvgpr_read_b32 v157, a47\n"
    "v_mul_f32 v157, v158, v157\n"
    "v_accvgpr_write_b32 a47, v157\n"
    "v_accvgpr_read_b32 v132, a48\n"
    "v_mul_f32 v132, v134, v132\n"
    "v_accvgpr_write_b32 a48, v132\n"
    "v_accvgpr_read_b32 v140, a49\n"
    "v_mul_f32 v140, v134, v140\n"
    "v_accvgpr_write_b32 a49, v140\n"
    "v_accvgpr_read_b32 v148, a50\n"
    "v_mul_f32 v148, v134, v148\n"
    "v_accvgpr_write_b32 a50, v148\n"
    "v_accvgpr_read_b32 v156, a51\n"
    "v_mul_f32 v156, v134, v156\n"
    "v_accvgpr_write_b32 a51, v156\n"
    "v_accvgpr_read_b32 v133, a52\n"
    "v_mul_f32 v133, v142, v133\n"
    "v_accvgpr_write_b32 a52, v133\n"
    "v_accvgpr_read_b32 v141, a53\n"
    "v_mul_f32 v141, v142, v141\n"
    "v_accvgpr_write_b32 a53, v141\n"
    "v_accvgpr_read_b32 v149, a54\n"
    "v_mul_f32 v149, v142, v149\n"
    "v_accvgpr_write_b32 a54, v149\n"
    "v_accvgpr_read_b32 v157, a55\n"
    "v_mul_f32 v157, v142, v157\n"
    "v_accvgpr_write_b32 a55, v157\n"
    "v_accvgpr_read_b32 v132, a56\n"
    "v_mul_f32 v132, v150, v132\n"
    "v_accvgpr_write_b32 a56, v132\n"
    "v_accvgpr_read_b32 v140, a57\n"
    "v_mul_f32 v140, v150, v140\n"
    "v_accvgpr_write_b32 a57, v140\n"
    "v_accvgpr_read_b32 v148, a58\n"
    "v_mul_f32 v148, v150, v148\n"
    "v_accvgpr_write_b32 a58, v148\n"
    "v_accvgpr_read_b32 v156, a59\n"
    "v_mul_f32 v156, v150, v156\n"
    "v_accvgpr_write_b32 a59, v156\n"
    "v_accvgpr_read_b32 v133, a60\n"
    "v_mul_f32 v133, v158, v133\n"
    "v_accvgpr_write_b32 a60, v133\n"
    "v_accvgpr_read_b32 v141, a61\n"
    "v_mul_f32 v141, v158, v141\n"
    "v_accvgpr_write_b32 a61, v141\n"
    "v_accvgpr_read_b32 v149, a62\n"
    "v_mul_f32 v149, v158, v149\n"
    "v_accvgpr_write_b32 a62, v149\n"
    "v_accvgpr_read_b32 v157, a63\n"
    "v_mul_f32 v157, v158, v157\n"
    "v_accvgpr_write_b32 a63, v157\n"
    "v_accvgpr_read_b32 v132, a64\n"
    "v_mul_f32 v132, v134, v132\n"
    "v_accvgpr_write_b32 a64, v132\n"
    "v_accvgpr_read_b32 v140, a65\n"
    "v_mul_f32 v140, v134, v140\n"
    "v_accvgpr_write_b32 a65, v140\n"
    "v_accvgpr_read_b32 v148, a66\n"
    "v_mul_f32 v148, v134, v148\n"
    "v_accvgpr_write_b32 a66, v148\n"
    "v_accvgpr_read_b32 v156, a67\n"
    "v_mul_f32 v156, v134, v156\n"
    "v_accvgpr_write_b32 a67, v156\n"
    "v_accvgpr_read_b32 v133, a68\n"
    "v_mul_f32 v133, v142, v133\n"
    "v_accvgpr_write_b32 a68, v133\n"
    "v_accvgpr_read_b32 v141, a69\n"
    "v_mul_f32 v141, v142, v141\n"
    "v_accvgpr_write_b32 a69, v141\n"
    "v_accvgpr_read_b32 v149, a70\n"
    "v_mul_f32 v149, v142, v149\n"
    "v_accvgpr_write_b32 a70, v149\n"
    "v_accvgpr_read_b32 v157, a71\n"
    "v_mul_f32 v157, v142, v157\n"
    "v_accvgpr_write_b32 a71, v157\n"
    "v_accvgpr_read_b32 v132, a72\n"
    "v_mul_f32 v132, v150, v132\n"
    "v_accvgpr_write_b32 a72, v132\n"
    "v_accvgpr_read_b32 v140, a73\n"
    "v_mul_f32 v140, v150, v140\n"
    "v_accvgpr_write_b32 a73, v140\n"
    "v_accvgpr_read_b32 v148, a74\n"
    "v_mul_f32 v148, v150, v148\n"
    "v_accvgpr_write_b32 a74, v148\n"
    "v_accvgpr_read_b32 v156, a75\n"
    "v_mul_f32 v156, v150, v156\n"
    "v_accvgpr_write_b32 a75, v156\n"
    "v_accvgpr_read_b32 v133, a76\n"
    "v_mul_f32 v133, v158, v133\n"
    "v_accvgpr_write_b32 a76, v133\n"
    "v_accvgpr_read_b32 v141, a77\n"
    "v_mul_f32 v141, v158, v141\n"
    "v_accvgpr_write_b32 a77, v141\n"
    "v_accvgpr_read_b32 v149, a78\n"
    "v_mul_f32 v149, v158, v149\n"
    "v_accvgpr_write_b32 a78, v149\n"
    "v_accvgpr_read_b32 v157, a79\n"
    "v_mul_f32 v157, v158, v157\n"
    "v_accvgpr_write_b32 a79, v157\n"
    "v_accvgpr_read_b32 v132, a80\n"
    "v_mul_f32 v132, v134, v132\n"
    "v_accvgpr_write_b32 a80, v132\n"
    "v_accvgpr_read_b32 v140, a81\n"
    "v_mul_f32 v140, v134, v140\n"
    "v_accvgpr_write_b32 a81, v140\n"
    "v_accvgpr_read_b32 v148, a82\n"
    "v_mul_f32 v148, v134, v148\n"
    "v_accvgpr_write_b32 a82, v148\n"
    "v_accvgpr_read_b32 v156, a83\n"
    "v_mul_f32 v156, v134, v156\n"
    "v_accvgpr_write_b32 a83, v156\n"
    "v_accvgpr_read_b32 v133, a84\n"
    "v_mul_f32 v133, v142, v133\n"
    "v_accvgpr_write_b32 a84, v133\n"
    "v_accvgpr_read_b32 v141, a85\n"
    "v_mul_f32 v141, v142, v141\n"
    "v_accvgpr_write_b32 a85, v141\n"
    "v_accvgpr_read_b32 v149, a86\n"
    "v_mul_f32 v149, v142, v149\n"
    "v_accvgpr_write_b32 a86, v149\n"
    "v_accvgpr_read_b32 v157, a87\n"
    "v_mul_f32 v157, v142, v157\n"
    "v_accvgpr_write_b32 a87, v157\n"
    "v_accvgpr_read_b32 v132, a88\n"
    "v_mul_f32 v132, v150, v132\n"
    "v_accvgpr_write_b32 a88, v132\n"
    "v_accvgpr_read_b32 v140, a89\n"
    "v_mul_f32 v140, v150, v140\n"
    "v_accvgpr_write_b32 a89, v140\n"
    "v_accvgpr_read_b32 v148, a90\n"
    "v_mul_f32 v148, v150, v148\n"
    "v_accvgpr_write_b32 a90, v148\n"
    "v_accvgpr_read_b32 v156, a91\n"
    "v_mul_f32 v156, v150, v156\n"
    "v_accvgpr_write_b32 a91, v156\n"
    "v_accvgpr_read_b32 v133, a92\n"
    "v_mul_f32 v133, v158, v133\n"
    "v_accvgpr_write_b32 a92, v133\n"
    "v_accvgpr_read_b32 v141, a93\n"
    "v_mul_f32 v141, v158, v141\n"
    "v_accvgpr_write_b32 a93, v141\n"
    "v_accvgpr_read_b32 v149, a94\n"
    "v_mul_f32 v149, v158, v149\n"
    "v_accvgpr_write_b32 a94, v149\n"
    "v_accvgpr_read_b32 v157, a95\n"
    "v_mul_f32 v157, v158, v157\n"
    "v_accvgpr_write_b32 a95, v157\n"
    "v_accvgpr_read_b32 v132, a96\n"
    "v_mul_f32 v132, v134, v132\n"
    "v_accvgpr_write_b32 a96, v132\n"
    "v_accvgpr_read_b32 v140, a97\n"
    "v_mul_f32 v140, v134, v140\n"
    "v_accvgpr_write_b32 a97, v140\n"
    "v_accvgpr_read_b32 v148, a98\n"
    "v_mul_f32 v148, v134, v148\n"
    "v_accvgpr_write_b32 a98, v148\n"
    "v_accvgpr_read_b32 v156, a99\n"
    "v_mul_f32 v156, v134, v156\n"
    "v_accvgpr_write_b32 a99, v156\n"
    "v_accvgpr_read_b32 v133, a100\n"
    "v_mul_f32 v133, v142, v133\n"
    "v_accvgpr_write_b32 a100, v133\n"
    "v_accvgpr_read_b32 v141, a101\n"
    "v_mul_f32 v141, v142, v141\n"
    "v_accvgpr_write_b32 a101, v141\n"
    "v_accvgpr_read_b32 v149, a102\n"
    "v_mul_f32 v149, v142, v149\n"
    "v_accvgpr_write_b32 a102, v149\n"
    "v_accvgpr_read_b32 v157, a103\n"
    "v_mul_f32 v157, v142, v157\n"
    "v_accvgpr_write_b32 a103, v157\n"
    "v_accvgpr_read_b32 v132, a104\n"
    "v_mul_f32 v132, v150, v132\n"
    "v_accvgpr_write_b32 a104, v132\n"
    "v_accvgpr_read_b32 v140, a105\n"
    "v_mul_f32 v140, v150, v140\n"
    "v_accvgpr_write_b32 a105, v140\n"
    "v_accvgpr_read_b32 v148, a106\n"
    "v_mul_f32 v148, v150, v148\n"
    "v_accvgpr_write_b32 a106, v148\n"
    "v_accvgpr_read_b32 v156, a107\n"
    "v_mul_f32 v156, v150, v156\n"
    "v_accvgpr_write_b32 a107, v156\n"
    "v_accvgpr_read_b32 v133, a108\n"
    "v_mul_f32 v133, v158, v133\n"
    "v_accvgpr_write_b32 a108, v133\n"
    "v_accvgpr_read_b32 v141, a109\n"
    "v_mul_f32 v141, v158, v141\n"
    "v_accvgpr_write_b32 a109, v141\n"
    "v_accvgpr_read_b32 v149, a110\n"
    "v_mul_f32 v149, v158, v149\n"
    "v_accvgpr_write_b32 a110, v149\n"
    "v_accvgpr_read_b32 v157, a111\n"
    "v_mul_f32 v157, v158, v157\n"
    "v_accvgpr_write_b32 a111, v157\n"
    "v_accvgpr_read_b32 v132, a112\n"
    "v_mul_f32 v132, v134, v132\n"
    "v_accvgpr_write_b32 a112, v132\n"
    "v_accvgpr_read_b32 v140, a113\n"
    "v_mul_f32 v140, v134, v140\n"
    "v_accvgpr_write_b32 a113, v140\n"
    "v_accvgpr_read_b32 v148, a114\n"
    "v_mul_f32 v148, v134, v148\n"
    "v_accvgpr_write_b32 a114, v148\n"
    "v_accvgpr_read_b32 v156, a115\n"
    "v_mul_f32 v156, v134, v156\n"
    "v_accvgpr_write_b32 a115, v156\n"
    "v_accvgpr_read_b32 v133, a116\n"
    "v_mul_f32 v133, v142, v133\n"
    "v_accvgpr_write_b32 a116, v133\n"
    "v_accvgpr_read_b32 v141, a117\n"
    "v_mul_f32 v141, v142, v141\n"
    "v_accvgpr_write_b32 a117, v141\n"
    "v_accvgpr_read_b32 v149, a118\n"
    "v_mul_f32 v149, v142, v149\n"
    "v_accvgpr_write_b32 a118, v149\n"
    "v_accvgpr_read_b32 v157, a119\n"
    "v_mul_f32 v157, v142, v157\n"
    "v_accvgpr_write_b32 a119, v157\n"
    "v_accvgpr_read_b32 v132, a120\n"
    "v_mul_f32 v132, v150, v132\n"
    "v_accvgpr_write_b32 a120, v132\n"
    "v_accvgpr_read_b32 v140, a121\n"
    "v_mul_f32 v140, v150, v140\n"
    "v_accvgpr_write_b32 a121, v140\n"
    "v_accvgpr_read_b32 v148, a122\n"
    "v_mul_f32 v148, v150, v148\n"
    "v_accvgpr_write_b32 a122, v148\n"
    "v_accvgpr_read_b32 v156, a123\n"
    "v_mul_f32 v156, v150, v156\n"
    "v_accvgpr_write_b32 a123, v156\n"
    "v_accvgpr_read_b32 v133, a124\n"
    "v_mul_f32 v133, v158, v133\n"
    "v_accvgpr_write_b32 a124, v133\n"
    "v_accvgpr_read_b32 v141, a125\n"
    "v_mul_f32 v141, v158, v141\n"
    "v_accvgpr_write_b32 a125, v141\n"
    "v_accvgpr_read_b32 v149, a126\n"
    "v_mul_f32 v149, v158, v149\n"
    "v_accvgpr_write_b32 a126, v149\n"
    "v_accvgpr_read_b32 v157, a127\n"
    "v_mul_f32 v157, v158, v157\n"
    "v_accvgpr_write_b32 a127, v157\n"
    "v_accvgpr_read_b32 v132, a128\n"
    "v_mul_f32 v132, v134, v132\n"
    "v_accvgpr_write_b32 a128, v132\n"
    "v_accvgpr_read_b32 v140, a129\n"
    "v_mul_f32 v140, v134, v140\n"
    "v_accvgpr_write_b32 a129, v140\n"
    "v_accvgpr_read_b32 v148, a130\n"
    "v_mul_f32 v148, v134, v148\n"
    "v_accvgpr_write_b32 a130, v148\n"
    "v_accvgpr_read_b32 v156, a131\n"
    "v_mul_f32 v156, v134, v156\n"
    "v_accvgpr_write_b32 a131, v156\n"
    "v_accvgpr_read_b32 v133, a132\n"
    "v_mul_f32 v133, v142, v133\n"
    "v_accvgpr_write_b32 a132, v133\n"
    "v_accvgpr_read_b32 v141, a133\n"
    "v_mul_f32 v141, v142, v141\n"
    "v_accvgpr_write_b32 a133, v141\n"
    "v_accvgpr_read_b32 v149, a134\n"
    "v_mul_f32 v149, v142, v149\n"
    "v_accvgpr_write_b32 a134, v149\n"
    "v_accvgpr_read_b32 v157, a135\n"
    "v_mul_f32 v157, v142, v157\n"
    "v_accvgpr_write_b32 a135, v157\n"
    "v_accvgpr_read_b32 v132, a136\n"
    "v_mul_f32 v132, v150, v132\n"
    "v_accvgpr_write_b32 a136, v132\n"
    "v_accvgpr_read_b32 v140, a137\n"
    "v_mul_f32 v140, v150, v140\n"
    "v_accvgpr_write_b32 a137, v140\n"
    "v_accvgpr_read_b32 v148, a138\n"
    "v_mul_f32 v148, v150, v148\n"
    "v_accvgpr_write_b32 a138, v148\n"
    "v_accvgpr_read_b32 v156, a139\n"
    "v_mul_f32 v156, v150, v156\n"
    "v_accvgpr_write_b32 a139, v156\n"
    "v_accvgpr_read_b32 v133, a140\n"
    "v_mul_f32 v133, v158, v133\n"
    "v_accvgpr_write_b32 a140, v133\n"
    "v_accvgpr_read_b32 v141, a141\n"
    "v_mul_f32 v141, v158, v141\n"
    "v_accvgpr_write_b32 a141, v141\n"
    "v_accvgpr_read_b32 v149, a142\n"
    "v_mul_f32 v149, v158, v149\n"
    "v_accvgpr_write_b32 a142, v149\n"
    "v_accvgpr_read_b32 v157, a143\n"
    "v_mul_f32 v157, v158, v157\n"
    "v_accvgpr_write_b32 a143, v157\n"
    "v_accvgpr_read_b32 v132, a144\n"
    "v_mul_f32 v132, v134, v132\n"
    "v_accvgpr_write_b32 a144, v132\n"
    "v_accvgpr_read_b32 v140, a145\n"
    "v_mul_f32 v140, v134, v140\n"
    "v_accvgpr_write_b32 a145, v140\n"
    "v_accvgpr_read_b32 v148, a146\n"
    "v_mul_f32 v148, v134, v148\n"
    "v_accvgpr_write_b32 a146, v148\n"
    "v_accvgpr_read_b32 v156, a147\n"
    "v_mul_f32 v156, v134, v156\n"
    "v_accvgpr_write_b32 a147, v156\n"
    "v_accvgpr_read_b32 v133, a148\n"
    "v_mul_f32 v133, v142, v133\n"
    "v_accvgpr_write_b32 a148, v133\n"
    "v_accvgpr_read_b32 v141, a149\n"
    "v_mul_f32 v141, v142, v141\n"
    "v_accvgpr_write_b32 a149, v141\n"
    "v_accvgpr_read_b32 v149, a150\n"
    "v_mul_f32 v149, v142, v149\n"
    "v_accvgpr_write_b32 a150, v149\n"
    "v_accvgpr_read_b32 v157, a151\n"
    "v_mul_f32 v157, v142, v157\n"
    "v_accvgpr_write_b32 a151, v157\n"
    "v_accvgpr_read_b32 v132, a152\n"
    "v_mul_f32 v132, v150, v132\n"
    "v_accvgpr_write_b32 a152, v132\n"
    "v_accvgpr_read_b32 v140, a153\n"
    "v_mul_f32 v140, v150, v140\n"
    "v_accvgpr_write_b32 a153, v140\n"
    "v_accvgpr_read_b32 v148, a154\n"
    "v_mul_f32 v148, v150, v148\n"
    "v_accvgpr_write_b32 a154, v148\n"
    "v_accvgpr_read_b32 v156, a155\n"
    "v_mul_f32 v156, v150, v156\n"
    "v_accvgpr_write_b32 a155, v156\n"
    "v_accvgpr_read_b32 v133, a156\n"
    "v_mul_f32 v133, v158, v133\n"
    "v_accvgpr_write_b32 a156, v133\n"
    "v_accvgpr_read_b32 v141, a157\n"
    "v_mul_f32 v141, v158, v141\n"
    "v_accvgpr_write_b32 a157, v141\n"
    "v_accvgpr_read_b32 v149, a158\n"
    "v_mul_f32 v149, v158, v149\n"
    "v_accvgpr_write_b32 a158, v149\n"
    "v_accvgpr_read_b32 v157, a159\n"
    "v_mul_f32 v157, v158, v157\n"
    "v_accvgpr_write_b32 a159, v157\n"
    "v_accvgpr_read_b32 v132, a160\n"
    "v_mul_f32 v132, v134, v132\n"
    "v_accvgpr_write_b32 a160, v132\n"
    "v_accvgpr_read_b32 v140, a161\n"
    "v_mul_f32 v140, v134, v140\n"
    "v_accvgpr_write_b32 a161, v140\n"
    "v_accvgpr_read_b32 v148, a162\n"
    "v_mul_f32 v148, v134, v148\n"
    "v_accvgpr_write_b32 a162, v148\n"
    "v_accvgpr_read_b32 v156, a163\n"
    "v_mul_f32 v156, v134, v156\n"
    "v_accvgpr_write_b32 a163, v156\n"
    "v_accvgpr_read_b32 v133, a164\n"
    "v_mul_f32 v133, v142, v133\n"
    "v_accvgpr_write_b32 a164, v133\n"
    "v_accvgpr_read_b32 v141, a165\n"
    "v_mul_f32 v141, v142, v141\n"
    "v_accvgpr_write_b32 a165, v141\n"
    "v_accvgpr_read_b32 v149, a166\n"
    "v_mul_f32 v149, v142, v149\n"
    "v_accvgpr_write_b32 a166, v149\n"
    "v_accvgpr_read_b32 v157, a167\n"
    "v_mul_f32 v157, v142, v157\n"
    "v_accvgpr_write_b32 a167, v157\n"
    "v_accvgpr_read_b32 v132, a168\n"
    "v_mul_f32 v132, v150, v132\n"
    "v_accvgpr_write_b32 a168, v132\n"
    "v_accvgpr_read_b32 v140, a169\n"
    "v_mul_f32 v140, v150, v140\n"
    "v_accvgpr_write_b32 a169, v140\n"
    "v_accvgpr_read_b32 v148, a170\n"
    "v_mul_f32 v148, v150, v148\n"
    "v_accvgpr_write_b32 a170, v148\n"
    "v_accvgpr_read_b32 v156, a171\n"
    "v_mul_f32 v156, v150, v156\n"
    "v_accvgpr_write_b32 a171, v156\n"
    "v_accvgpr_read_b32 v133, a172\n"
    "v_mul_f32 v133, v158, v133\n"
    "v_accvgpr_write_b32 a172, v133\n"
    "v_accvgpr_read_b32 v141, a173\n"
    "v_mul_f32 v141, v158, v141\n"
    "v_accvgpr_write_b32 a173, v141\n"
    "v_accvgpr_read_b32 v149, a174\n"
    "v_mul_f32 v149, v158, v149\n"
    "v_accvgpr_write_b32 a174, v149\n"
    "v_accvgpr_read_b32 v157, a175\n"
    "v_mul_f32 v157, v158, v157\n"
    "v_accvgpr_write_b32 a175, v157\n"
    "v_accvgpr_read_b32 v132, a176\n"
    "v_mul_f32 v132, v134, v132\n"
    "v_accvgpr_write_b32 a176, v132\n"
    "v_accvgpr_read_b32 v140, a177\n"
    "v_mul_f32 v140, v134, v140\n"
    "v_accvgpr_write_b32 a177, v140\n"
    "v_accvgpr_read_b32 v148, a178\n"
    "v_mul_f32 v148, v134, v148\n"
    "v_accvgpr_write_b32 a178, v148\n"
    "v_accvgpr_read_b32 v156, a179\n"
    "v_mul_f32 v156, v134, v156\n"
    "v_accvgpr_write_b32 a179, v156\n"
    "v_accvgpr_read_b32 v133, a180\n"
    "v_mul_f32 v133, v142, v133\n"
    "v_accvgpr_write_b32 a180, v133\n"
    "v_accvgpr_read_b32 v141, a181\n"
    "v_mul_f32 v141, v142, v141\n"
    "v_accvgpr_write_b32 a181, v141\n"
    "v_accvgpr_read_b32 v149, a182\n"
    "v_mul_f32 v149, v142, v149\n"
    "v_accvgpr_write_b32 a182, v149\n"
    "v_accvgpr_read_b32 v157, a183\n"
    "v_mul_f32 v157, v142, v157\n"
    "v_accvgpr_write_b32 a183, v157\n"
    "v_accvgpr_read_b32 v132, a184\n"
    "v_mul_f32 v132, v150, v132\n"
    "v_accvgpr_write_b32 a184, v132\n"
    "v_accvgpr_read_b32 v140, a185\n"
    "v_mul_f32 v140, v150, v140\n"
    "v_accvgpr_write_b32 a185, v140\n"
    "v_accvgpr_read_b32 v148, a186\n"
    "v_mul_f32 v148, v150, v148\n"
    "v_accvgpr_write_b32 a186, v148\n"
    "v_accvgpr_read_b32 v156, a187\n"
    "v_mul_f32 v156, v150, v156\n"
    "v_accvgpr_write_b32 a187, v156\n"
    "v_accvgpr_read_b32 v133, a188\n"
    "v_mul_f32 v133, v158, v133\n"
    "v_accvgpr_write_b32 a188, v133\n"
    "v_accvgpr_read_b32 v141, a189\n"
    "v_mul_f32 v141, v158, v141\n"
    "v_accvgpr_write_b32 a189, v141\n"
    "v_accvgpr_read_b32 v149, a190\n"
    "v_mul_f32 v149, v158, v149\n"
    "v_accvgpr_write_b32 a190, v149\n"
    "v_accvgpr_read_b32 v157, a191\n"
    "v_mul_f32 v157, v158, v157\n"
    "v_accvgpr_write_b32 a191, v157\n"
    "v_accvgpr_read_b32 v132, a192\n"
    "v_mul_f32 v132, v134, v132\n"
    "v_accvgpr_write_b32 a192, v132\n"
    "v_accvgpr_read_b32 v140, a193\n"
    "v_mul_f32 v140, v134, v140\n"
    "v_accvgpr_write_b32 a193, v140\n"
    "v_accvgpr_read_b32 v148, a194\n"
    "v_mul_f32 v148, v134, v148\n"
    "v_accvgpr_write_b32 a194, v148\n"
    "v_accvgpr_read_b32 v156, a195\n"
    "v_mul_f32 v156, v134, v156\n"
    "v_accvgpr_write_b32 a195, v156\n"
    "v_accvgpr_read_b32 v133, a196\n"
    "v_mul_f32 v133, v142, v133\n"
    "v_accvgpr_write_b32 a196, v133\n"
    "v_accvgpr_read_b32 v141, a197\n"
    "v_mul_f32 v141, v142, v141\n"
    "v_accvgpr_write_b32 a197, v141\n"
    "v_accvgpr_read_b32 v149, a198\n"
    "v_mul_f32 v149, v142, v149\n"
    "v_accvgpr_write_b32 a198, v149\n"
    "v_accvgpr_read_b32 v157, a199\n"
    "v_mul_f32 v157, v142, v157\n"
    "v_accvgpr_write_b32 a199, v157\n"
    "v_accvgpr_read_b32 v132, a200\n"
    "v_mul_f32 v132, v150, v132\n"
    "v_accvgpr_write_b32 a200, v132\n"
    "v_accvgpr_read_b32 v140, a201\n"
    "v_mul_f32 v140, v150, v140\n"
    "v_accvgpr_write_b32 a201, v140\n"
    "v_accvgpr_read_b32 v148, a202\n"
    "v_mul_f32 v148, v150, v148\n"
    "v_accvgpr_write_b32 a202, v148\n"
    "v_accvgpr_read_b32 v156, a203\n"
    "v_mul_f32 v156, v150, v156\n"
    "v_accvgpr_write_b32 a203, v156\n"
    "v_accvgpr_read_b32 v133, a204\n"
    "v_mul_f32 v133, v158, v133\n"
    "v_accvgpr_write_b32 a204, v133\n"
    "v_accvgpr_read_b32 v141, a205\n"
    "v_mul_f32 v141, v158, v141\n"
    "v_accvgpr_write_b32 a205, v141\n"
    "v_accvgpr_read_b32 v149, a206\n"
    "v_mul_f32 v149, v158, v149\n"
    "v_accvgpr_write_b32 a206, v149\n"
    "v_accvgpr_read_b32 v157, a207\n"
    "v_mul_f32 v157, v158, v157\n"
    "v_accvgpr_write_b32 a207, v157\n"
    "v_accvgpr_read_b32 v132, a208\n"
    "v_mul_f32 v132, v134, v132\n"
    "v_accvgpr_write_b32 a208, v132\n"
    "v_accvgpr_read_b32 v140, a209\n"
    "v_mul_f32 v140, v134, v140\n"
    "v_accvgpr_write_b32 a209, v140\n"
    "v_accvgpr_read_b32 v148, a210\n"
    "v_mul_f32 v148, v134, v148\n"
    "v_accvgpr_write_b32 a210, v148\n"
    "v_accvgpr_read_b32 v156, a211\n"
    "v_mul_f32 v156, v134, v156\n"
    "v_accvgpr_write_b32 a211, v156\n"
    "v_accvgpr_read_b32 v133, a212\n"
    "v_mul_f32 v133, v142, v133\n"
    "v_accvgpr_write_b32 a212, v133\n"
    "v_accvgpr_read_b32 v141, a213\n"
    "v_mul_f32 v141, v142, v141\n"
    "v_accvgpr_write_b32 a213, v141\n"
    "v_accvgpr_read_b32 v149, a214\n"
    "v_mul_f32 v149, v142, v149\n"
    "v_accvgpr_write_b32 a214, v149\n"
    "v_accvgpr_read_b32 v157, a215\n"
    "v_mul_f32 v157, v142, v157\n"
    "v_accvgpr_write_b32 a215, v157\n"
    "v_accvgpr_read_b32 v132, a216\n"
    "v_mul_f32 v132, v150, v132\n"
    "v_accvgpr_write_b32 a216, v132\n"
    "v_accvgpr_read_b32 v140, a217\n"
    "v_mul_f32 v140, v150, v140\n"
    "v_accvgpr_write_b32 a217, v140\n"
    "v_accvgpr_read_b32 v148, a218\n"
    "v_mul_f32 v148, v150, v148\n"
    "v_accvgpr_write_b32 a218, v148\n"
    "v_accvgpr_read_b32 v156, a219\n"
    "v_mul_f32 v156, v150, v156\n"
    "v_accvgpr_write_b32 a219, v156\n"
    "v_accvgpr_read_b32 v133, a220\n"
    "v_mul_f32 v133, v158, v133\n"
    "v_accvgpr_write_b32 a220, v133\n"
    "v_accvgpr_read_b32 v141, a221\n"
    "v_mul_f32 v141, v158, v141\n"
    "v_accvgpr_write_b32 a221, v141\n"
    "v_accvgpr_read_b32 v149, a222\n"
    "v_mul_f32 v149, v158, v149\n"
    "v_accvgpr_write_b32 a222, v149\n"
    "v_accvgpr_read_b32 v157, a223\n"
    "v_mul_f32 v157, v158, v157\n"
    "v_accvgpr_write_b32 a223, v157\n"
    "v_accvgpr_read_b32 v132, a224\n"
    "v_mul_f32 v132, v134, v132\n"
    "v_accvgpr_write_b32 a224, v132\n"
    "v_accvgpr_read_b32 v140, a225\n"
    "v_mul_f32 v140, v134, v140\n"
    "v_accvgpr_write_b32 a225, v140\n"
    "v_accvgpr_read_b32 v148, a226\n"
    "v_mul_f32 v148, v134, v148\n"
    "v_accvgpr_write_b32 a226, v148\n"
    "v_accvgpr_read_b32 v156, a227\n"
    "v_mul_f32 v156, v134, v156\n"
    "v_accvgpr_write_b32 a227, v156\n"
    "v_accvgpr_read_b32 v133, a228\n"
    "v_mul_f32 v133, v142, v133\n"
    "v_accvgpr_write_b32 a228, v133\n"
    "v_accvgpr_read_b32 v141, a229\n"
    "v_mul_f32 v141, v142, v141\n"
    "v_accvgpr_write_b32 a229, v141\n"
    "v_accvgpr_read_b32 v149, a230\n"
    "v_mul_f32 v149, v142, v149\n"
    "v_accvgpr_write_b32 a230, v149\n"
    "v_accvgpr_read_b32 v157, a231\n"
    "v_mul_f32 v157, v142, v157\n"
    "v_accvgpr_write_b32 a231, v157\n"
    "v_accvgpr_read_b32 v132, a232\n"
    "v_mul_f32 v132, v150, v132\n"
    "v_accvgpr_write_b32 a232, v132\n"
    "v_accvgpr_read_b32 v140, a233\n"
    "v_mul_f32 v140, v150, v140\n"
    "v_accvgpr_write_b32 a233, v140\n"
    "v_accvgpr_read_b32 v148, a234\n"
    "v_mul_f32 v148, v150, v148\n"
    "v_accvgpr_write_b32 a234, v148\n"
    "v_accvgpr_read_b32 v156, a235\n"
    "v_mul_f32 v156, v150, v156\n"
    "v_accvgpr_write_b32 a235, v156\n"
    "v_accvgpr_read_b32 v133, a236\n"
    "v_mul_f32 v133, v158, v133\n"
    "v_accvgpr_write_b32 a236, v133\n"
    "v_accvgpr_read_b32 v141, a237\n"
    "v_mul_f32 v141, v158, v141\n"
    "v_accvgpr_write_b32 a237, v141\n"
    "v_accvgpr_read_b32 v149, a238\n"
    "v_mul_f32 v149, v158, v149\n"
    "v_accvgpr_write_b32 a238, v149\n"
    "v_accvgpr_read_b32 v157, a239\n"
    "v_mul_f32 v157, v158, v157\n"
    "v_accvgpr_write_b32 a239, v157\n"
    "v_accvgpr_read_b32 v132, a240\n"
    "v_mul_f32 v132, v134, v132\n"
    "v_accvgpr_write_b32 a240, v132\n"
    "v_accvgpr_read_b32 v140, a241\n"
    "v_mul_f32 v140, v134, v140\n"
    "v_accvgpr_write_b32 a241, v140\n"
    "v_accvgpr_read_b32 v148, a242\n"
    "v_mul_f32 v148, v134, v148\n"
    "v_accvgpr_write_b32 a242, v148\n"
    "v_accvgpr_read_b32 v156, a243\n"
    "v_mul_f32 v156, v134, v156\n"
    "v_accvgpr_write_b32 a243, v156\n"
    "v_accvgpr_read_b32 v133, a244\n"
    "v_mul_f32 v133, v142, v133\n"
    "v_accvgpr_write_b32 a244, v133\n"
    "v_accvgpr_read_b32 v141, a245\n"
    "v_mul_f32 v141, v142, v141\n"
    "v_accvgpr_write_b32 a245, v141\n"
    "v_accvgpr_read_b32 v149, a246\n"
    "v_mul_f32 v149, v142, v149\n"
    "v_accvgpr_write_b32 a246, v149\n"
    "v_accvgpr_read_b32 v157, a247\n"
    "v_mul_f32 v157, v142, v157\n"
    "v_accvgpr_write_b32 a247, v157\n"
    "v_accvgpr_read_b32 v132, a248\n"
    "v_mul_f32 v132, v150, v132\n"
    "v_accvgpr_write_b32 a248, v132\n"
    "v_accvgpr_read_b32 v140, a249\n"
    "v_mul_f32 v140, v150, v140\n"
    "v_accvgpr_write_b32 a249, v140\n"
    "v_accvgpr_read_b32 v148, a250\n"
    "v_mul_f32 v148, v150, v148\n"
    "v_accvgpr_write_b32 a250, v148\n"
    "v_accvgpr_read_b32 v156, a251\n"
    "v_mul_f32 v156, v150, v156\n"
    "v_accvgpr_write_b32 a251, v156\n"
    "v_accvgpr_read_b32 v133, a252\n"
    "v_mul_f32 v133, v158, v133\n"
    "v_accvgpr_write_b32 a252, v133\n"
    "v_accvgpr_read_b32 v141, a253\n"
    "v_mul_f32 v141, v158, v141\n"
    "v_accvgpr_write_b32 a253, v141\n"
    "v_accvgpr_read_b32 v149, a254\n"
    "v_mul_f32 v149, v158, v149\n"
    "v_accvgpr_write_b32 a254, v149\n"
    "v_accvgpr_read_b32 v157, a255\n"
    "v_mul_f32 v157, v158, v157\n"
    "v_accvgpr_write_b32 a255, v157\n"
    "s_nop 3\n"
    "s_setpc_b64 s[62:63]\n"
    "LS_rare2_1_%=:\n"
    "s_nop 15\n"
    "v_accvgpr_read_b32 v164, a0\n"
    "v_mul_f32 v164, v166, v164\n"
    "v_accvgpr_write_b32 a0, v164\n"
    "v_accvgpr_read_b32 v172, a1\n"
    "v_mul_f32 v172, v166, v172\n"
    "v_accvgpr_write_b32 a1, v172\n"
    "v_accvgpr_read_b32 v180, a2\n"
    "v_mul_f32 v180, v166, v180\n"
    "v_accvgpr_write_b32 a2, v180\n"
    "v_accvgpr_read_b32 v188, a3\n"
    "v_mul_f32 v188, v166, v188\n"
    "v_accvgpr_write_b32 a3, v188\n"
    "v_accvgpr_read_b32 v165, a4\n"
    "v_mul_f32 v165, v174, v165\n"
    "v_accvgpr_write_b32 a4, v165\n"
    "v_accvgpr_read_b32 v173, a5\n"
    "v_mul_f32 v173, v174, v173\n"
    "v_accvgpr_write_b32 a5, v173\n"
    "v_accvgpr_read_b32 v181, a6\n"
    "v_mul_f32 v181, v174, v181\n"
    "v_accvgpr_write_b32 a6, v181\n"
    "v_accvgpr_read_b32 v189, a7\n"
    "v_mul_f32 v189, v174, v189\n"
    "v_accvgpr_write_b32 a7, v189\n"
    "v_accvgpr_read_b32 v164, a8\n"
    "v_mul_f32 v164, v182, v164\n"
    "v_accvgpr_write_b32 a8, v164\n"
    "v_accvgpr_read_b32 v172, a9\n"
    "v_mul_f32 v172, v182, v172\n"
    "v_accvgpr_write_b32 a9, v172\n"
    "v_accvgpr_read_b32 v180, a10\n"
    "v_mul_f32 v180, v182, v180\n"
    "v_accvgpr_write_b32 a10, v180\n"
    "v_accvgpr_read_b32 v188, a11\n"
    "v_mul_f32 v188, v182, v188\n"
    "v_accvgpr_write_b32 a11, v188\n"
    "v_accvgpr_read_b32 v165, a12\n"
    "v_mul_f32 v165, v190, v165\n"
    "v_accvgpr_write_b32 a12, v165\n"
    "v_accvgpr_read_b32 v173, a13\n"
    "v_mul_f32 v173, v190, v173\n"
    "v_accvgpr_write_b32 a13, v173\n"
    "v_accvgpr_read_b32 v181, a14\n"
    "v_mul_f32 v181, v190, v181\n"
    "v_accvgpr_write_b32 a14, v181\n"
    "v_accvgpr_read_b32 v189, a15\n"
    "v_mul_f32 v189, v190, v189\n"
    "v_accvgpr_write_b32 a15, v189\n"
    "v_accvgpr_read_b32 v164, a16\n"
    "v_mul_f32 v164, v166, v164\n"
    "v_accvgpr_write_b32 a16, v164\n"
    "v_accvgpr_read_b32 v172, a17\n"
    "v_mul_f32 v172, v166, v172\n"
    "v_accvgpr_write_b32 a17, v172\n"
    "v_accvgpr_read_b32 v180, a18\n"
    "v_mul_f32 v180, v166, v180\n"
    "v_accvgpr_write_b32 a18, v180\n"
    "v_accvgpr_read_b32 v188, a19\n"
    "v_mul_f32 v188, v166, v188\n"
    "v_accvgpr_write_b32 a19, v188\n"
    "v_accvgpr_read_b32 v165, a20\n"
    "v_mul_f32 v165, v174, v165\n"
    "v_accvgpr_write_b32 a20, v165\n"
    "v_accvgpr_read_b32 v173, a21\n"
    "v_mul_f32 v173, v174, v173\n"
    "v_accvgpr_write_b32 a21, v173\n"
    "v_accvgpr_read_b32 v181, a22\n"
    "v_mul_f32 v181, v174, v181\n"
    "v_accvgpr_write_b32 a22, v181\n"
    "v_accvgpr_read_b32 v189, a23\n"
    "v_mul_f32 v189, v174, v189\n"
    "v_accvgpr_write_b32 a23, v189\n"
    "v_accvgpr_read_b32 v164, a24\n"
    "v_mul_f32 v164, v182, v164\n"
    "v_accvgpr_write_b32 a24, v164\n"
    "v_accvgpr_read_b32 v172, a25\n"
    "v_mul_f32 v172, v182, v172\n"
    "v_accvgpr_write_b32 a25, v172\n"
    "v_accvgpr_read_b32 v180, a26\n"
    "v_mul_f32 v180, v182, v180\n"
    "v_accvgpr_write_b32 a26, v180\n"
    "v_accvgpr_read_b32 v188, a27\n"
    "v_mul_f32 v188, v182, v188\n"
    "v_accvgpr_write_b32 a27, v188\n"
    "v_accvgpr_read_b32 v165, a28\n"
    "v_mul_f32 v165, v190, v165\n"
    "v_accvgpr_write_b32 a28, v165\n"
    "v_accvgpr_read_b32 v173, a29\n"
    "v_mul_f32 v173, v190, v173\n"
    "v_accvgpr_write_b32 a29, v173\n"
    "v_accvgpr_read_b32 v181, a30\n"
    "v_mul_f32 v181, v190, v181\n"
    "v_accvgpr_write_b32 a30, v181\n"
    "v_accvgpr_read_b32 v189, a31\n"
    "v_mul_f32 v189, v190, v189\n"
    "v_accvgpr_write_b32 a31, v189\n"
    "v_accvgpr_read_b32 v164, a32\n"
    "v_mul_f32 v164, v166, v164\n"
    "v_accvgpr_write_b32 a32, v164\n"
    "v_accvgpr_read_b32 v172, a33\n"
    "v_mul_f32 v172, v166, v172\n"
    "v_accvgpr_write_b32 a33, v172\n"
    "v_accvgpr_read_b32 v180, a34\n"
    "v_mul_f32 v180, v166, v180\n"
    "v_accvgpr_write_b32 a34, v180\n"
    "v_accvgpr_read_b32 v188, a35\n"
    "v_mul_f32 v188, v166, v188\n"
    "v_accvgpr_write_b32 a35, v188\n"
    "v_accvgpr_read_b32 v165, a36\n"
    "v_mul_f32 v165, v174, v165\n"
    "v_accvgpr_write_b32 a36, v165\n"
    "v_accvgpr_read_b32 v173, a37\n"
    "v_mul_f32 v173, v174, v173\n"
    "v_accvgpr_write_b32 a37, v173\n"
    "v_accvgpr_read_b32 v181, a38\n"
    "v_mul_f32 v181, v174, v181\n"
    "v_accvgpr_write_b32 a38, v181\n"
    "v_accvgpr_read_b32 v189, a39\n"
    "v_mul_f32 v189, v174, v189\n"
    "v_accvgpr_write_b32 a39, v189\n"
    "v_accvgpr_read_b32 v164, a40\n"
    "v_mul_f32 v164, v182, v164\n"
    "v_accvgpr_write_b32 a40, v164\n"
    "v_accvgpr_read_b32 v172, a41\n"
    "v_mul_f32 v172, v182, v172\n"
    "v_accvgpr_write_b32 a41, v172\n"
    "v_accvgpr_read_b32 v180, a42\n"
    "v_mul_f32 v180, v182, v180\n"
    "v_accvgpr_write_b32 a42, v180\n"
    "v_accvgpr_read_b32 v188, a43\n"
    "v_mul_f32 v188, v182, v188\n"
    "v_accvgpr_write_b32 a43, v188\n"
    "v_accvgpr_read_b32 v165, a44\n"
    "v_mul_f32 v165, v190, v165\n"
    "v_accvgpr_write_b32 a44, v165\n"
    "v_accvgpr_read_b32 v173, a45\n"
    "v_mul_f32 v173, v190, v173\n"
    "v_accvgpr_write_b32 a45, v173\n"
    "v_accvgpr_read_b32 v181, a46\n"
    "v_mul_f32 v181, v190, v181\n"
    "v_accvgpr_write_b32 a46, v181\n"
    "v_accvgpr_read_b32 v189, a47\n"
    "v_mul_f32 v189, v190, v189\n"
    "v_accvgpr_write_b32 a47, v189\n"
    "v_accvgpr_read_b32 v164, a48\n"
    "v_mul_f32 v164, v166, v164\n"
    "v_accvgpr_write_b32 a48, v164\n"
    "v_accvgpr_read_b32 v172, a49\n"
    "v_mul_f32 v172, v166, v172\n"
    "v_accvgpr_write_b32 a49, v172\n"
    "v_accvgpr_read_b32 v180, a50\n"
    "v_mul_f32 v180, v166, v180\n"
    "v_accvgpr_write_b32 a50, v180\n"
    "v_accvgpr_read_b32 v188, a51\n"
    "v_mul_f32 v188, v166, v188\n"
    "v_accvgpr_write_b32 a51, v188\n"
    "v_accvgpr_read_b32 v165, a52\n"
    "v_mul_f32 v165, v174, v165\n"
    "v_accvgpr_write_b32 a52, v165\n"
    "v_accvgpr_read_b32 v173, a53\n"
    "v_mul_f32 v173, v174, v173\n"
    "v_accvgpr_write_b32 a53, v173\n"
    "v_accvgpr_read_b32 v181, a54\n"
    "v_mul_f32 v181, v174, v181\n"
    "v_accvgpr_write_b32 a54, v181\n"
    "v_accvgpr_read_b32 v189, a55\n"
    "v_mul_f32 v189, v174, v189\n"
    "v_accvgpr_write_b32 a55, v189\n"
    "v_accvgpr_read_b32 v164, a56\n"
    "v_mul_f32 v164, v182, v164\n"
    "v_accvgpr_write_b32 a56, v164\n"
    "v_accvgpr_read_b32 v172, a57\n"
    "v_mul_f32 v172, v182, v172\n"
    "v_accvgpr_write_b32 a57, v172\n"
    "v_accvgpr_read_b32 v180, a58\n"
    "v_mul_f32 v180, v182, v180\n"
    "v_accvgpr_write_b32 a58, v180\n"
    "v_accvgpr_read_b32 v188, a59\n"
    "v_mul_f32 v188, v182, v188\n"
    "v_accvgpr_write_b32 a59, v188\n"
    "v_accvgpr_read_b32 v165, a60\n"
    "v_mul_f32 v165, v190, v165\n"
    "v_accvgpr_write_b32 a60, v165\n"
    "v_accvgpr_read_b32 v173, a61\n"
    "v_mul_f32 v173, v190, v173\n"
    "v_accvgpr_write_b32 a61, v173\n"
    "v_accvgpr_read_b32 v181, a62\n"
    "v_mul_f32 v181, v190, v181\n"
    "v_accvgpr_write_b32 a62, v181\n"
    "v_accvgpr_read_b32 v189, a63\n"
    "v_mul_f32 v189, v190, v189\n"
    "v_accvgpr_write_b32 a63, v189\n"
    "v_accvgpr_read_b32 v164, a64\n"
    "v_mul_f32 v164, v166, v164\n"
    "v_accvgpr_write_b32 a64, v164\n"
    "v_accvgpr_read_b32 v172, a65\n"
    "v_mul_f32 v172, v166, v172\n"
    "v_accvgpr_write_b32 a65, v172\n"
    "v_accvgpr_read_b32 v180, a66\n"
    "v_mul_f32 v180, v166, v180\n"
    "v_accvgpr_write_b32 a66, v180\n"
    "v_accvgpr_read_b32 v188, a67\n"
    "v_mul_f32 v188, v166, v188\n"
    "v_accvgpr_write_b32 a67, v188\n"
    "v_accvgpr_read_b32 v165, a68\n"
    "v_mul_f32 v165, v174, v165\n"
    "v_accvgpr_write_b32 a68, v165\n"
    "v_accvgpr_read_b32 v173, a69\n"
    "v_mul_f32 v173, v174, v173\n"
    "v_accvgpr_write_b32 a69, v173\n"
    "v_accvgpr_read_b32 v181, a70\n"
    "v_mul_f32 v181, v174, v181\n"
    "v_accvgpr_write_b32 a70, v181\n"
    "v_accvgpr_read_b32 v189, a71\n"
    "v_mul_f32 v189, v174, v189\n"
    "v_accvgpr_write_b32 a71, v189\n"
    "v_accvgpr_read_b32 v164, a72\n"
    "v_mul_f32 v164, v182, v164\n"
    "v_accvgpr_write_b32 a72, v164\n"
    "v_accvgpr_read_b32 v172, a73\n"
    "v_mul_f32 v172, v182, v172\n"
    "v_accvgpr_write_b32 a73, v172\n"
    "v_accvgpr_read_b32 v180, a74\n"
    "v_mul_f32 v180, v182, v180\n"
    "v_accvgpr_write_b32 a74, v180\n"
    "v_accvgpr_read_b32 v188, a75\n"
    "v_mul_f32 v188, v182, v188\n"
    "v_accvgpr_write_b32 a75, v188\n"
    "v_accvgpr_read_b32 v165, a76\n"
    "v_mul_f32 v165, v190, v165\n"
    "v_accvgpr_write_b32 a76, v165\n"
    "v_accvgpr_read_b32 v173, a77\n"
    "v_mul_f32 v173, v190, v173\n"
    "v_accvgpr_write_b32 a77, v173\n"
    "v_accvgpr_read_b32 v181, a78\n"
    "v_mul_f32 v181, v190, v181\n"
    "v_accvgpr_write_b32 a78, v181\n"
    "v_accvgpr_read_b32 v189, a79\n"
    "v_mul_f32 v189, v190, v189\n"
    "v_accvgpr_write_b32 a79, v189\n"
    "v_accvgpr_read_b32 v164, a80\n"
    "v_mul_f32 v164, v166, v164\n"
    "v_accvgpr_write_b32 a80, v164\n"
    "v_accvgpr_read_b32 v172, a81\n"
    "v_mul_f32 v172, v166, v172\n"
    "v_accvgpr_write_b32 a81, v172\n"
    "v_accvgpr_read_b32 v180, a82\n"
    "v_mul_f32 v180, v166, v180\n"
    "v_accvgpr_write_b32 a82, v180\n"
    "v_accvgpr_read_b32 v188, a83\n"
    "v_mul_f32 v188, v166, v188\n"
    "v_accvgpr_write_b32 a83, v188\n"
    "v_accvgpr_read_b32 v165, a84\n"
    "v_mul_f32 v165, v174, v165\n"
    "v_accvgpr_write_b32 a84, v165\n"
    "v_accvgpr_read_b32 v173, a85\n"
    "v_mul_f32 v173, v174, v173\n"
    "v_accvgpr_write_b32 a85, v173\n"
    "v_accvgpr_read_b32 v181, a86\n"
    "v_mul_f32 v181, v174, v181\n"
    "v_accvgpr_write_b32 a86, v181\n"
    "v_accvgpr_read_b32 v189, a87\n"
    "v_mul_f32 v189, v174, v189\n"
    "v_accvgpr_write_b32 a87, v189\n"
    "v_accvgpr_read_b32 v164, a88\n"
    "v_mul_f32 v164, v182, v164\n"
    "v_accvgpr_write_b32 a88, v164\n"
    "v_accvgpr_read_b32 v172, a89\n"
    "v_mul_f32 v172, v182, v172\n"
    "v_accvgpr_write_b32 a89, v172\n"
    "v_accvgpr_read_b32 v180, a90\n"
    "v_mul_f32 v180, v182, v180\n"
    "v_accvgpr_write_b32 a90, v180\n"
    "v_accvgpr_read_b32 v188, a91\n"
    "v_mul_f32 v188, v182, v188\n"
    "v_accvgpr_write_b32 a91, v188\n"
    "v_accvgpr_read_b32 v165, a92\n"
    "v_mul_f32 v165, v190, v165\n"
    "v_accvgpr_write_b32 a92, v165\n"
    "v_accvgpr_read_b32 v173, a93\n"
    "v_mul_f32 v173, v190, v173\n"
    "v_accvgpr_write_b32 a93, v173\n"
    "v_accvgpr_read_b32 v181, a94\n"
    "v_mul_f32 v181, v190, v181\n"
    "v_accvgpr_write_b32 a94, v181\n"
    "v_accvgpr_read_b32 v189, a95\n"
    "v_mul_f32 v189, v190, v189\n"
    "v_accvgpr_write_b32 a95, v189\n"
    "v_accvgpr_read_b32 v164, a96\n"
    "v_mul_f32 v164, v166, v164\n"
    "v_accvgpr_write_b32 a96, v164\n"
    "v_accvgpr_read_b32 v172, a97\n"
    "v_mul_f32 v172, v166, v172\n"
    "v_accvgpr_write_b32 a97, v172\n"
    "v_accvgpr_read_b32 v180, a98\n"
    "v_mul_f32 v180, v166, v180\n"
    "v_accvgpr_write_b32 a98, v180\n"
    "v_accvgpr_read_b32 v188, a99\n"
    "v_mul_f32 v188, v166, v188\n"
    "v_accvgpr_write_b32 a99, v188\n"
    "v_accvgpr_read_b32 v165, a100\n"
    "v_mul_f32 v165, v174, v165\n"
    "v_accvgpr_write_b32 a100, v165\n"
    "v_accvgpr_read_b32 v173, a101\n"
    "v_mul_f32 v173, v174, v173\n"
    "v_accvgpr_write_b32 a101, v173\n"
    "v_accvgpr_read_b32 v181, a102\n"
    "v_mul_f32 v181, v174, v181\n"
    "v_accvgpr_write_b32 a102, v181\n"
    "v_accvgpr_read_b32 v189, a103\n"
    "v_mul_f32 v189, v174, v189\n"
    "v_accvgpr_write_b32 a103, v189\n"
    "v_accvgpr_read_b32 v164, a104\n"
    "v_mul_f32 v164, v182, v164\n"
    "v_accvgpr_write_b32 a104, v164\n"
    "v_accvgpr_read_b32 v172, a105\n"
    "v_mul_f32 v172, v182, v172\n"
    "v_accvgpr_write_b32 a105, v172\n"
    "v_accvgpr_read_b32 v180, a106\n"
    "v_mul_f32 v180, v182, v180\n"
    "v_accvgpr_write_b32 a106, v180\n"
    "v_accvgpr_read_b32 v188, a107\n"
    "v_mul_f32 v188, v182, v188\n"
    "v_accvgpr_write_b32 a107, v188\n"
    "v_accvgpr_read_b32 v165, a108\n"
    "v_mul_f32 v165, v190, v165\n"
    "v_accvgpr_write_b32 a108, v165\n"
    "v_accvgpr_read_b32 v173, a109\n"
    "v_mul_f32 v173, v190, v173\n"
    "v_accvgpr_write_b32 a109, v173\n"
    "v_accvgpr_read_b32 v181, a110\n"
    "v_mul_f32 v181, v190, v181\n"
    "v_accvgpr_write_b32 a110, v181\n"
    "v_accvgpr_read_b32 v189, a111\n"
    "v_mul_f32 v189, v190, v189\n"
    "v_accvgpr_write_b32 a111, v189\n"
    "v_accvgpr_read_b32 v164, a112\n"
    "v_mul_f32 v164, v166, v164\n"
    "v_accvgpr_write_b32 a112, v164\n"
    "v_accvgpr_read_b32 v172, a113\n"
    "v_mul_f32 v172, v166, v172\n"
    "v_accvgpr_write_b32 a113, v172\n"
    "v_accvgpr_read_b32 v180, a114\n"
    "v_mul_f32 v180, v166, v180\n"
    "v_accvgpr_write_b32 a114, v180\n"
    "v_accvgpr_read_b32 v188, a115\n"
    "v_mul_f32 v188, v166, v188\n"
    "v_accvgpr_write_b32 a115, v188\n"
    "v_accvgpr_read_b32 v165, a116\n"
    "v_mul_f32 v165, v174, v165\n"
    "v_accvgpr_write_b32 a116, v165\n"
    "v_accvgpr_read_b32 v173, a117\n"
    "v_mul_f32 v173, v174, v173\n"
    "v_accvgpr_write_b32 a117, v173\n"
    "v_accvgpr_read_b32 v181, a118\n"
    "v_mul_f32 v181, v174, v181\n"
    "v_accvgpr_write_b32 a118, v181\n"
    "v_accvgpr_read_b32 v189, a119\n"
    "v_mul_f32 v189, v174, v189\n"
    "v_accvgpr_write_b32 a119, v189\n"
    "v_accvgpr_read_b32 v164, a120\n"
    "v_mul_f32 v164, v182, v164\n"
    "v_accvgpr_write_b32 a120, v164\n"
    "v_accvgpr_read_b32 v172, a121\n"
    "v_mul_f32 v172, v182, v172\n"
    "v_accvgpr_write_b32 a121, v172\n"
    "v_accvgpr_read_b32 v180, a122\n"
    "v_mul_f32 v180, v182, v180\n"
    "v_accvgpr_write_b32 a122, v180\n"
    "v_accvgpr_read_b32 v188, a123\n"
    "v_mul_f32 v188, v182, v188\n"
    "v_accvgpr_write_b32 a123, v188\n"
    "v_accvgpr_read_b32 v165, a124\n"
    "v_mul_f32 v165, v190, v165\n"
    "v_accvgpr_write_b32 a124, v165\n"
    "v_accvgpr_read_b32 v173, a125\n"
    "v_mul_f32 v173, v190, v173\n"
    "v_accvgpr_write_b32 a125, v173\n"
    "v_accvgpr_read_b32 v181, a126\n"
    "v_mul_f32 v181, v190, v181\n"
    "v_accvgpr_write_b32 a126, v181\n"
    "v_accvgpr_read_b32 v189, a127\n"
    "v_mul_f32 v189, v190, v189\n"
    "v_accvgpr_write_b32 a127, v189\n"
    "v_accvgpr_read_b32 v164, a128\n"
    "v_mul_f32 v164, v166, v164\n"
    "v_accvgpr_write_b32 a128, v164\n"
    "v_accvgpr_read_b32 v172, a129\n"
    "v_mul_f32 v172, v166, v172\n"
    "v_accvgpr_write_b32 a129, v172\n"
    "v_accvgpr_read_b32 v180, a130\n"
    "v_mul_f32 v180, v166, v180\n"
    "v_accvgpr_write_b32 a130, v180\n"
    "v_accvgpr_read_b32 v188, a131\n"
    "v_mul_f32 v188, v166, v188\n"
    "v_accvgpr_write_b32 a131, v188\n"
    "v_accvgpr_read_b32 v165, a132\n"
    "v_mul_f32 v165, v174, v165\n"
    "v_accvgpr_write_b32 a132, v165\n"
    "v_accvgpr_read_b32 v173, a133\n"
    "v_mul_f32 v173, v174, v173\n"
    "v_accvgpr_write_b32 a133, v173\n"
    "v_accvgpr_read_b32 v181, a134\n"
    "v_mul_f32 v181, v174, v181\n"
    "v_accvgpr_write_b32 a134, v181\n"
    "v_accvgpr_read_b32 v189, a135\n"
    "v_mul_f32 v189, v174, v189\n"
    "v_accvgpr_write_b32 a135, v189\n"
    "v_accvgpr_read_b32 v164, a136\n"
    "v_mul_f32 v164, v182, v164\n"
    "v_accvgpr_write_b32 a136, v164\n"
    "v_accvgpr_read_b32 v172, a137\n"
    "v_mul_f32 v172, v182, v172\n"
    "v_accvgpr_write_b32 a137, v172\n"
    "v_accvgpr_read_b32 v180, a138\n"
    "v_mul_f32 v180, v182, v180\n"
    "v_accvgpr_write_b32 a138, v180\n"
    "v_accvgpr_read_b32 v188, a139\n"
    "v_mul_f32 v188, v182, v188\n"
    "v_accvgpr_write_b32 a139, v188\n"
    "v_accvgpr_read_b32 v165, a140\n"
    "v_mul_f32 v165, v190, v165\n"
    "v_accvgpr_write_b32 a140, v165\n"
    "v_accvgpr_read_b32 v173, a141\n"
    "v_mul_f32 v173, v190, v173\n"
    "v_accvgpr_write_b32 a141, v173\n"
    "v_accvgpr_read_b32 v181, a142\n"
    "v_mul_f32 v181, v190, v181\n"
    "v_accvgpr_write_b32 a142, v181\n"
    "v_accvgpr_read_b32 v189, a143\n"
    "v_mul_f32 v189, v190, v189\n"
    "v_accvgpr_write_b32 a143, v189\n"
    "v_accvgpr_read_b32 v164, a144\n"
    "v_mul_f32 v164, v166, v164\n"
    "v_accvgpr_write_b32 a144, v164\n"
    "v_accvgpr_read_b32 v172, a145\n"
    "v_mul_f32 v172, v166, v172\n"
    "v_accvgpr_write_b32 a145, v172\n"
    "v_accvgpr_read_b32 v180, a146\n"
    "v_mul_f32 v180, v166, v180\n"
    "v_accvgpr_write_b32 a146, v180\n"
    "v_accvgpr_read_b32 v188, a147\n"
    "v_mul_f32 v188, v166, v188\n"
    "v_accvgpr_write_b32 a147, v188\n"
    "v_accvgpr_read_b32 v165, a148\n"
    "v_mul_f32 v165, v174, v165\n"
    "v_accvgpr_write_b32 a148, v165\n"
    "v_accvgpr_read_b32 v173, a149\n"
    "v_mul_f32 v173, v174, v173\n"
    "v_accvgpr_write_b32 a149, v173\n"
    "v_accvgpr_read_b32 v181, a150\n"
    "v_mul_f32 v181, v174, v181\n"
    "v_accvgpr_write_b32 a150, v181\n"
    "v_accvgpr_read_b32 v189, a151\n"
    "v_mul_f32 v189, v174, v189\n"
    "v_accvgpr_write_b32 a151, v189\n"
    "v_accvgpr_read_b32 v164, a152\n"
    "v_mul_f32 v164, v182, v164\n"
    "v_accvgpr_write_b32 a152, v164\n"
    "v_accvgpr_read_b32 v172, a153\n"
    "v_mul_f32 v172, v182, v172\n"
    "v_accvgpr_write_b32 a153, v172\n"
    "v_accvgpr_read_b32 v180, a154\n"
    "v_mul_f32 v180, v182, v180\n"
    "v_accvgpr_write_b32 a154, v180\n"
    "v_accvgpr_read_b32 v188, a155\n"
    "v_mul_f32 v188, v182, v188\n"
    "v_accvgpr_write_b32 a155, v188\n"
    "v_accvgpr_read_b32 v165, a156\n"
    "v_mul_f32 v165, v190, v165\n"
    "v_accvgpr_write_b32 a156, v165\n"
    "v_accvgpr_read_b32 v173, a157\n"
    "v_mul_f32 v173, v190, v173\n"
    "v_accvgpr_write_b32 a157, v173\n"
    "v_accvgpr_read_b32 v181, a158\n"
    "v_mul_f32 v181, v190, v181\n"
    "v_accvgpr_write_b32 a158, v181\n"
    "v_accvgpr_read_b32 v189, a159\n"
    "v_mul_f32 v189, v190, v189\n"
    "v_accvgpr_write_b32 a159, v189\n"
    "v_accvgpr_read_b32 v164, a160\n"
    "v_mul_f32 v164, v166, v164\n"
    "v_accvgpr_write_b32 a160, v164\n"
    "v_accvgpr_read_b32 v172, a161\n"
    "v_mul_f32 v172, v166, v172\n"
    "v_accvgpr_write_b32 a161, v172\n"
    "v_accvgpr_read_b32 v180, a162\n"
    "v_mul_f32 v180, v166, v180\n"
    "v_accvgpr_write_b32 a162, v180\n"
    "v_accvgpr_read_b32 v188, a163\n"
    "v_mul_f32 v188, v166, v188\n"
    "v_accvgpr_write_b32 a163, v188\n"
    "v_accvgpr_read_b32 v165, a164\n"
    "v_mul_f32 v165, v174, v165\n"
    "v_accvgpr_write_b32 a164, v165\n"
    "v_accvgpr_read_b32 v173, a165\n"
    "v_mul_f32 v173, v174, v173\n"
    "v_accvgpr_write_b32 a165, v173\n"
    "v_accvgpr_read_b32 v181, a166\n"
    "v_mul_f32 v181, v174, v181\n"
    "v_accvgpr_write_b32 a166, v181\n"
    "v_accvgpr_read_b32 v189, a167\n"
    "v_mul_f32 v189, v174, v189\n"
    "v_accvgpr_write_b32 a167, v189\n"
    "v_accvgpr_read_b32 v164, a168\n"
    "v_mul_f32 v164, v182, v164\n"
    "v_accvgpr_write_b32 a168, v164\n"
    "v_accvgpr_read_b32 v172, a169\n"
    "v_mul_f32 v172, v182, v172\n"
    "v_accvgpr_write_b32 a169, v172\n"
    "v_accvgpr_read_b32 v180, a170\n"
    "v_mul_f32 v180, v182, v180\n"
    "v_accvgpr_write_b32 a170, v180\n"
    "v_accvgpr_read_b32 v188, a171\n"
    "v_mul_f32 v188, v182, v188\n"
    "v_accvgpr_write_b32 a171, v188\n"
    "v_accvgpr_read_b32 v165, a172\n"
    "v_mul_f32 v165, v190, v165\n"
    "v_accvgpr_write_b32 a172, v165\n"
    "v_accvgpr_read_b32 v173, a173\n"
    "v_mul_f32 v173, v190, v173\n"
    "v_accvgpr_write_b32 a173, v173\n"
    "v_accvgpr_read_b32 v181, a174\n"
    "v_mul_f32 v181, v190, v181\n"
    "v_accvgpr_write_b32 a174, v181\n"
    "v_accvgpr_read_b32 v189, a175\n"
    "v_mul_f32 v189, v190, v189\n"
    "v_accvgpr_write_b32 a175, v189\n"
    "v_accvgpr_read_b32 v164, a176\n"
    "v_mul_f32 v164, v166, v164\n"
    "v_accvgpr_write_b32 a176, v164\n"
    "v_accvgpr_read_b32 v172, a177\n"
    "v_mul_f32 v172, v166, v172\n"
    "v_accvgpr_write_b32 a177, v172\n"
    "v_accvgpr_read_b32 v180, a178\n"
    "v_mul_f32 v180, v166, v180\n"
    "v_accvgpr_write_b32 a178, v180\n"
    "v_accvgpr_read_b32 v188, a179\n"
    "v_mul_f32 v188, v166, v188\n"
    "v_accvgpr_write_b32 a179, v188\n"
    "v_accvgpr_read_b32 v165, a180\n"
    "v_mul_f32 v165, v174, v165\n"
    "v_accvgpr_write_b32 a180, v165\n"
    "v_accvgpr_read_b32 v173, a181\n"
    "v_mul_f32 v173, v174, v173\n"
    "v_accvgpr_write_b32 a181, v173\n"
    "v_accvgpr_read_b32 v181, a182\n"
    "v_mul_f32 v181, v174, v181\n"
    "v_accvgpr_write_b32 a182, v181\n"
    "v_accvgpr_read_b32 v189, a183\n"
    "v_mul_f32 v189, v174, v189\n"
    "v_accvgpr_write_b32 a183, v189\n"
    "v_accvgpr_read_b32 v164, a184\n"
    "v_mul_f32 v164, v182, v164\n"
    "v_accvgpr_write_b32 a184, v164\n"
    "v_accvgpr_read_b32 v172, a185\n"
    "v_mul_f32 v172, v182, v172\n"
    "v_accvgpr_write_b32 a185, v172\n"
    "v_accvgpr_read_b32 v180, a186\n"
    "v_mul_f32 v180, v182, v180\n"
    "v_accvgpr_write_b32 a186, v180\n"
    "v_accvgpr_read_b32 v188, a187\n"
    "v_mul_f32 v188, v182, v188\n"
    "v_accvgpr_write_b32 a187, v188\n"
    "v_accvgpr_read_b32 v165, a188\n"
    "v_mul_f32 v165, v190, v165\n"
    "v_accvgpr_write_b32 a188, v165\n"
    "v_accvgpr_read_b32 v173, a189\n"
    "v_mul_f32 v173, v190, v173\n"
    "v_accvgpr_write_b32 a189, v173\n"
    "v_accvgpr_read_b32 v181, a190\n"
    "v_mul_f32 v181, v190, v181\n"
    "v_accvgpr_write_b32 a190, v181\n"
    "v_accvgpr_read_b32 v189, a191\n"
    "v_mul_f32 v189, v190, v189\n"
    "v_accvgpr_write_b32 a191, v189\n"
    "v_accvgpr_read_b32 v164, a192\n"
    "v_mul_f32 v164, v166, v164\n"
    "v_accvgpr_write_b32 a192, v164\n"
    "v_accvgpr_read_b32 v172, a193\n"
    "v_mul_f32 v172, v166, v172\n"
    "v_accvgpr_write_b32 a193, v172\n"
    "v_accvgpr_read_b32 v180, a194\n"
    "v_mul_f32 v180, v166, v180\n"
    "v_accvgpr_write_b32 a194, v180\n"
    "v_accvgpr_read_b32 v188, a195\n"
    "v_mul_f32 v188, v166, v188\n"
    "v_accvgpr_write_b32 a195, v188\n"
    "v_accvgpr_read_b32 v165, a196\n"
    "v_mul_f32 v165, v174, v165\n"
    "v_accvgpr_write_b32 a196, v165\n"
    "v_accvgpr_read_b32 v173, a197\n"
    "v_mul_f32 v173, v174, v173\n"
    "v_accvgpr_write_b32 a197, v173\n"
    "v_accvgpr_read_b32 v181, a198\n"
    "v_mul_f32 v181, v174, v181\n"
    "v_accvgpr_write_b32 a198, v181\n"
    "v_accvgpr_read_b32 v189, a199\n"
    "v_mul_f32 v189, v174, v189\n"
    "v_accvgpr_write_b32 a199, v189\n"
    "v_accvgpr_read_b32 v164, a200\n"
    "v_mul_f32 v164, v182, v164\n"
    "v_accvgpr_write_b32 a200, v164\n"
    "v_accvgpr_read_b32 v172, a201\n"
    "v_mul_f32 v172, v182, v172\n"
    "v_accvgpr_write_b32 a201, v172\n"
    "v_accvgpr_read_b32 v180, a202\n"
    "v_mul_f32 v180, v182, v180\n"
    "v_accvgpr_write_b32 a202, v180\n"
    "v_accvgpr_read_b32 v188, a203\n"
    "v_mul_f32 v188, v182, v188\n"
    "v_accvgpr_write_b32 a203, v188\n"
    "v_accvgpr_read_b32 v165, a204\n"
    "v_mul_f32 v165, v190, v165\n"
    "v_accvgpr_write_b32 a204, v165\n"
    "v_accvgpr_read_b32 v173, a205\n"
    "v_mul_f32 v173, v190, v173\n"
    "v_accvgpr_write_b32 a205, v173\n"
    "v_accvgpr_read_b32 v181, a206\n"
    "v_mul_f32 v181, v190, v181\n"
    "v_accvgpr_write_b32 a206, v181\n"
    "v_accvgpr_read_b32 v189, a207\n"
    "v_mul_f32 v189, v190, v189\n"
    "v_accvgpr_write_b32 a207, v189\n"
    "v_accvgpr_read_b32 v164, a208\n"
    "v_mul_f32 v164, v166, v164\n"
    "v_accvgpr_write_b32 a208, v164\n"
    "v_accvgpr_read_b32 v172, a209\n"
    "v_mul_f32 v172, v166, v172\n"
    "v_accvgpr_write_b32 a209, v172\n"
    "v_accvgpr_read_b32 v180, a210\n"
    "v_mul_f32 v180, v166, v180\n"
    "v_accvgpr_write_b32 a210, v180\n"
    "v_accvgpr_read_b32 v188, a211\n"
    "v_mul_f32 v188, v166, v188\n"
    "v_accvgpr_write_b32 a211, v188\n"
    "v_accvgpr_read_b32 v165, a212\n"
    "v_mul_f32 v165, v174, v165\n"
    "v_accvgpr_write_b32 a212, v165\n"
    "v_accvgpr_read_b32 v173, a213\n"
    "v_mul_f32 v173, v174, v173\n"
    "v_accvgpr_write_b32 a213, v173\n"
    "v_accvgpr_read_b32 v181, a214\n"
    "v_mul_f32 v181, v174, v181\n"
    "v_accvgpr_write_b32 a214, v181\n"
    "v_accvgpr_read_b32 v189, a215\n"
    "v_mul_f32 v189, v174, v189\n"
    "v_accvgpr_write_b32 a215, v189\n"
    "v_accvgpr_read_b32 v164, a216\n"
    "v_mul_f32 v164, v182, v164\n"
    "v_accvgpr_write_b32 a216, v164\n"
    "v_accvgpr_read_b32 v172, a217\n"
    "v_mul_f32 v172, v182, v172\n"
    "v_accvgpr_write_b32 a217, v172\n"
    "v_accvgpr_read_b32 v180, a218\n"
    "v_mul_f32 v180, v182, v180\n"
    "v_accvgpr_write_b32 a218, v180\n"
    "v_accvgpr_read_b32 v188, a219\n"
    "v_mul_f32 v188, v182, v188\n"
    "v_accvgpr_write_b32 a219, v188\n"
    "v_accvgpr_read_b32 v165, a220\n"
    "v_mul_f32 v165, v190, v165\n"
    "v_accvgpr_write_b32 a220, v165\n"
    "v_accvgpr_read_b32 v173, a221\n"
    "v_mul_f32 v173, v190, v173\n"
    "v_accvgpr_write_b32 a221, v173\n"
    "v_accvgpr_read_b32 v181, a222\n"
    "v_mul_f32 v181, v190, v181\n"
    "v_accvgpr_write_b32 a222, v181\n"
    "v_accvgpr_read_b32 v189, a223\n"
    "v_mul_f32 v189, v190, v189\n"
    "v_accvgpr_write_b32 a223, v189\n"
    "v_accvgpr_read_b32 v164, a224\n"
    "v_mul_f32 v164, v166, v164\n"
    "v_accvgpr_write_b32 a224, v164\n"
    "v_accvgpr_read_b32 v172, a225\n"
    "v_mul_f32 v172, v166, v172\n"
    "v_accvgpr_write_b32 a225, v172\n"
    "v_accvgpr_read_b32 v180, a226\n"
    "v_mul_f32 v180, v166, v180\n"
    "v_accvgpr_write_b32 a226, v180\n"
    "v_accvgpr_read_b32 v188, a227\n"
    "v_mul_f32 v188, v166, v188\n"
    "v_accvgpr_write_b32 a227, v188\n"
    "v_accvgpr_read_b32 v165, a228\n"
    "v_mul_f32 v165, v174, v165\n"
    "v_accvgpr_write_b32 a228, v165\n"
    "v_accvgpr_read_b32 v173, a229\n"
    "v_mul_f32 v173, v174, v173\n"
    "v_accvgpr_write_b32 a229, v173\n"
    "v_accvgpr_read_b32 v181, a230\n"
    "v_mul_f32 v181, v174, v181\n"
    "v_accvgpr_write_b32 a230, v181\n"
    "v_accvgpr_read_b32 v189, a231\n"
    "v_mul_f32 v189, v174, v189\n"
    "v_accvgpr_write_b32 a231, v189\n"
    "v_accvgpr_read_b32 v164, a232\n"
    "v_mul_f32 v164, v182, v164\n"
    "v_accvgpr_write_b32 a232, v164\n"
    "v_accvgpr_read_b32 v172, a233\n"
    "v_mul_f32 v172, v182, v172\n"
    "v_accvgpr_write_b32 a233, v172\n"
    "v_accvgpr_read_b32 v180, a234\n"
    "v_mul_f32 v180, v182, v180\n"
    "v_accvgpr_write_b32 a234, v180\n"
    "v_accvgpr_read_b32 v188, a235\n"
    "v_mul_f32 v188, v182, v188\n"
    "v_accvgpr_write_b32 a235, v188\n"
    "v_accvgpr_read_b32 v165, a236\n"
    "v_mul_f32 v165, v190, v165\n"
    "v_accvgpr_write_b32 a236, v165\n"
    "v_accvgpr_read_b32 v173, a237\n"
    "v_mul_f32 v173, v190, v173\n"
    "v_accvgpr_write_b32 a237, v173\n"
    "v_accvgpr_read_b32 v181, a238\n"
    "v_mul_f32 v181, v190, v181\n"
    "v_accvgpr_write_b32 a238, v181\n"
    "v_accvgpr_read_b32 v189, a239\n"
    "v_mul_f32 v189, v190, v189\n"
    "v_accvgpr_write_b32 a239, v189\n"
    "v_accvgpr_read_b32 v164, a240\n"
    "v_mul_f32 v164, v166, v164\n"
    "v_accvgpr_write_b32 a240, v164\n"
    "v_accvgpr_read_b32 v172, a241\n"
    "v_mul_f32 v172, v166, v172\n"
    "v_accvgpr_write_b32 a241, v172\n"
    "v_accvgpr_read_b32 v180, a242\n"
    "v_mul_f32 v180, v166, v180\n"
    "v_accvgpr_write_b32 a242, v180\n"
    "v_accvgpr_read_b32 v188, a243\n"
    "v_mul_f32 v188, v166, v188\n"
    "v_accvgpr_write_b32 a243, v188\n"
    "v_accvgpr_read_b32 v165, a244\n"
    "v_mul_f32 v165, v174, v165\n"
    "v_accvgpr_write_b32 a244, v165\n"
    "v_accvgpr_read_b32 v173, a245\n"
    "v_mul_f32 v173, v174, v173\n"
    "v_accvgpr_write_b32 a245, v173\n"
    "v_accvgpr_read_b32 v181, a246\n"
    "v_mul_f32 v181, v174, v181\n"
    "v_accvgpr_write_b32 a246, v181\n"
    "v_accvgpr_read_b32 v189, a247\n"
    "v_mul_f32 v189, v174, v189\n"
    "v_accvgpr_write_b32 a247, v189\n"
    "v_accvgpr_read_b32 v164, a248\n"
    "v_mul_f32 v164, v182, v164\n"
    "v_accvgpr_write_b32 a248, v164\n"
    "v_accvgpr_read_b32 v172, a249\n"
    "v_mul_f32 v172, v182, v172\n"
    "v_accvgpr_write_b32 a249, v172\n"
    "v_accvgpr_read_b32 v180, a250\n"
    "v_mul_f32 v180, v182, v180\n"
    "v_accvgpr_write_b32 a250, v180\n"
    "v_accvgpr_read_b32 v188, a251\n"
    "v_mul_f32 v188, v182, v188\n"
    "v_accvgpr_write_b32 a251, v188\n"
    "v_accvgpr_read_b32 v165, a252\n"
    "v_mul_f32 v165, v190, v165\n"
    "v_accvgpr_write_b32 a252, v165\n"
    "v_accvgpr_read_b32 v173, a253\n"
    "v_mul_f32 v173, v190, v173\n"
    "v_accvgpr_write_b32 a253, v173\n"
    "v_accvgpr_read_b32 v181, a254\n"
    "v_mul_f32 v181, v190, v181\n"
    "v_accvgpr_write_b32 a254, v181\n"
    "v_accvgpr_read_b32 v189, a255\n"
    "v_mul_f32 v189, v190, v189\n"
    "v_accvgpr_write_b32 a255, v189\n"
    "s_nop 3\n"
    "s_setpc_b64 s[62:63]\n"
    "L_end_%=:\n"
    :
    : [ka] "s"(__builtin_amdgcn_kernarg_segment_ptr()), [blk] "s"(b), [w] "s"(w), [lds] "s"((unsigned)(size_t)lds)
    : "v0", "v1", "v2", "v3", "v4", "v5", "v6", "v7", "v8", "v9", "v10", "v11", "v12", "v13", "v14", "v15", "v16", "v17", "v18", "v19", "v20", "v21", "v22", "v23", "v24", "v25", "v26", "v27", "v28", "v29", "v30", "v31", "v32", "v33", "v34", "v35", "v36", "v37", "v38", "v39", "v40", "v41", "v42", "v43", "v44", "v45", "v46", "v47", "v48", "v49", "v50", "v51", "v52", "v53", "v54", "v55", "v56", "v57", "v58", "v59", "v60", "v61", "v62", "v63", "v64", "v65", "v66", "v67", "v68", "v69", "v70", "v71", "v72", "v73", "v74", "v75", "v76", "v77", "v78", "v79", "v80", "v81", "v82", "v83", "v84", "v85", "v86", "v87", "v88", "v89", "v90", "v91", "v92", "v93", "v94", "v95", "v96", "v97", "v98", "v99", "v100", "v101", "v102", "v103", "v104", "v105", "v106", "v107", "v108", "v109", "v110", "v111", "v112", "v113", "v114", "v115", "v116", "v117", "v118", "v119", "v120", "v121", "v122", "v123", "v124", "v125", "v126", "v127", "v128", "v129", "v130", "v131", "v132", "v133", "v134", "v135", "v136", "v137", "v138", "v139", "v140", "v141", "v142", "v143", "v144", "v145", "v146", "v147", "v148", "v149", "v150", "v151", "v152", "v153", "v154", "v155", "v156", "v157", "v158", "v159", "v160", "v161", "v162", "v163", "v164", "v165", "v166", "v167", "v168", "v169", "v170", "v171", "v172", "v173", "v174", "v175", "v176", "v177", "v178", "v179", "v180", "v181", "v182", "v183", "v184", "v185", "v186", "v187", "v188", "v189", "v190", "v191", "v192", "v193", "v194", "v195", "v196", "v197", "v198", "v199", "v200", "v201", "v202", "v203", "v204", "v205", "v206", "v207", "v208", "v209", "v210", "v211", "v212", "v213", "v214", "v215", "v216", "v217", "v218", "v219", "v220", "v221", "v222", "v223", "v224", "v225", "v226", "v227", "v228", "v229", "v230", "v231", "v232", "v233", "v234", "v235", "v236", "v237", "v238", "v239", "v240", "v241", "v242", "v243", "v244", "v245", "v246", "v247", "v248", "v249", "v250", "v251", "v252", "v253", "v254", "v255", "a0", "a1", "a2", "a3", "a4", "a5", "a6", "a7", "a8", "a9", "a10", "a11", "a12", "a13", "a14", "a15", "a16", "a17", "a18", "a19", "a20", "a21", "a22", "a23", "a24", "a25", "a26", "a27", "a28", "a29", "a30", "a31", "a32", "a33", "a34", "a35", "a36", "a37", "a38", "a39", "a40", "a41", "a42", "a43", "a44", "a45", "a46", "a47", "a48", "a49", "a50", "a51", "a52", "a53", "a54", "a55", "a56", "a57", "a58", "a59", "a60", "a61", "a62", "a63", "a64", "a65", "a66", "a67", "a68", "a69", "a70", "a71", "a72", "a73", "a74", "a75", "a76", "a77", "a78", "a79", "a80", "a81", "a82", "a83", "a84", "a85", "a86", "a87", "a88", "a89", "a90", "a91", "a92", "a93", "a94", "a95", "a96", "a97", "a98", "a99", "a100", "a101", "a102", "a103", "a104", "a105", "a106", "a107", "a108", "a109", "a110", "a111", "a112", "a113", "a114", "a115", "a116", "a117", "a118", "a119", "a120", "a121", "a122", "a123", "a124", "a125", "a126", "a127", "a128", "a129", "a130", "a131", "a132", "a133", "a134", "a135", "a136", "a137", "a138", "a139", "a140", "a141", "a142", "a143", "a144", "a145", "a146", "a147", "a148", "a149", "a150", "a151", "a152", "a153", "a154", "a155", "a156", "a157", "a158", "a159", "a160", "a161", "a162", "a163", "a164", "a165", "a166", "a167", "a168", "a169", "a170", "a171", "a172", "a173", "a174", "a175", "a176", "a177", "a178", "a179", "a180", "a181", "a182", "a183", "a184", "a185", "a186", "a187", "a188", "a189", "a190", "a191", "a192", "a193", "a194", "a195", "a196", "a197", "a198", "a199", "a200", "a201", "a202", "a203", "a204", "a205", "a206", "a207", "a208", "a209", "a210", "a211", "a212", "a213", "a214", "a215", "a216", "a217", "a218", "a219", "a220", "a221", "a222", "a223", "a224", "a225", "a226", "a227", "a228", "a229", "a230", "a231", "a232", "a233", "a234", "a235", "a236", "a237", "a238", "a239", "a240", "a241", "a242", "a243", "a244", "a245", "a246", "a247", "a248", "a249", "a250", "a251", "a252", "a253", "a254", "a255", "s36", "s37", "s38", "s39", "s40", "s41", "s42", "s43", "s44", "s45", "s46", "s47", "s48", "s49", "s50", "s51", "s52", "s53", "s54", "s55", "s56", "s57", "s58", "s59", "s60", "s61", "s62", "s63", "s64", "s65", "s66", "s67", "s68", "s69", "s70", "s71", "s72", "s73", "s74", "s75", "s76", "s77", "s78", "s79", "s80", "s81", "s82", "s83", "s84", "s85", "s86", "s87", "s88", "s89", "s90", "s91", "s92", "s93", "s94", "s95", "s96", "s97", "s98", "s99", "s100", "s101", "vcc", "scc", "memory");
}

extern "C" void kernel_launch(void* const* d_in, const int* in_sizes, int n_in, void* d_out, int out_size, void* d_ws, size_t ws_size,
                              hipStream_t stream) {
  mha_fwd_d256<<<256, 256, 0, stream>>>((const unsigned short*)d_in[0], (const unsigned short*)d_in[1], (const unsigned short*)d_in[2],
                                        (unsigned short*)d_out, (unsigned*)d_ws);
}
```
